```python
import torch

def _softmax(x, axis=-1):
    return torch.softmax(x, dim=axis)
def _gelu(x):
    return torch.nn.functional.gelu(x, approximate="tanh")
def _silu(x):
    return torch.nn.functional.silu(x)
def _sigmoid(x):
    return torch.sigmoid(x)
def _layernorm(x, axis=-1, eps=1e-5):
    m = x.mean(dim=axis, keepdim=True)
    v = x.var(dim=axis, keepdim=True, unbiased=False)
    return (x - m) / torch.sqrt(v + eps)
def _scatter_add(tgt, idx, src, axis=0):
    return tgt.clone().index_add_(axis, idx, src)
def _segment_sum(src, seg, n):
    out = torch.zeros((n,) + src.shape[1:], dtype=src.dtype)
    return out.index_add_(0, seg, src)
def _pad(x, pw):
    flat = tuple(v for pair in reversed(pw) for v in pair)
    return torch.nn.functional.pad(x, flat)

kernel_name = 'recurrent_scan__ssm_conv_s4__swiglu_ffn__prompt'

def setup_inputs():

    _w = (lambda a: (lambda t: t.to(torch.bfloat16) if t.is_floating_point() else t)(torch.as_tensor(a)))
    def _A_setup():
        B, L, D, N = 1, 2048, 512, 128
        A_re = -(lambda _x: torch.exp(torch.as_tensor(_x)))((lambda *s: torch.randn(*s, dtype=torch.bfloat16) * 3.2)(D, N))
        A_im = torch.pi * (lambda *s: torch.randn(*s, dtype=torch.bfloat16) * 0.05)(D, N)
        B_re = (lambda *s: torch.randn(*s, dtype=torch.bfloat16) * 0.1)(D, N)
        B_im = (lambda *s: torch.randn(*s, dtype=torch.bfloat16) * 0.1)(D, N)
        C_re = (lambda *s: torch.randn(*s, dtype=torch.bfloat16) * 0.1)(D, N)
        C_im = (lambda *s: torch.randn(*s, dtype=torch.bfloat16) * 0.1)(D, N)
        log_dt = (lambda *s: torch.randn(*s, dtype=torch.bfloat16) * 0.05)(D) * 0.5 - 4.0
        Dskip = (lambda *s: torch.randn(*s, dtype=torch.bfloat16) * 0.05)(D)
        return {
            "u": _w((lambda *s: torch.randn(*s, dtype=torch.bfloat16) * 0.05)(B, L, D)),
            "A_re": _w(A_re),
            "A_im": _w(A_im),
            "B_re": _w(B_re),
            "B_im": _w(B_im),
            "C_re": _w(C_re),
            "C_im": _w(C_im),
            "log_dt": _w(log_dt),
            "Dskip": _w(Dskip),
        }
    _A_in = _A_setup()

    def _B_setup():
        B, S, D, F = 1, 2048, 512, 1792
        return {
            "x": _w((lambda *s: torch.randn(*s, dtype=torch.bfloat16) * 0.05)(B, S, D)),
            "w_gate": _w((lambda *s: torch.randn(*s, dtype=torch.bfloat16) * 0.05)(D, F)),
            "w_up": _w((lambda *s: torch.randn(*s, dtype=torch.bfloat16) * 0.05)(D, F)),
            "w_down": _w((lambda *s: torch.randn(*s, dtype=torch.bfloat16) * 0.05)(F, D)),
        }
    _B_in = _B_setup()

    return {
        "A_u": _A_in["u"],
        "A_A_re": _A_in["A_re"],
        "A_A_im": _A_in["A_im"],
        "A_B_re": _A_in["B_re"],
        "A_B_im": _A_in["B_im"],
        "A_C_re": _A_in["C_re"],
        "A_C_im": _A_in["C_im"],
        "A_log_dt": _A_in["log_dt"],
        "A_Dskip": _A_in["Dskip"],
        "B_x": _B_in["x"],
        "B_w_gate": _B_in["w_gate"],
        "B_w_up": _B_in["w_up"],
        "B_w_down": _B_in["w_down"],
    }

def reference(A_u, A_A_re, A_A_im, A_B_re, A_B_im, A_C_re, A_C_im, A_log_dt, A_Dskip, B_x, B_w_gate, B_w_up, B_w_down):
    def _A_ref(u, A_re, A_im, B_re, B_im, C_re, C_im, log_dt, Dskip):
        Bsz, L, D = u.shape
        dt = torch.exp(log_dt)[:, None]
        A = A_re + 1j * A_im
        Bc = B_re + 1j * B_im
        Cc = C_re + 1j * C_im
        logdA = dt * A
        dB = dt * Bc
        l = (lambda x, dt: x.to(dt))(torch.arange(L), A_re.dtype)
        P = torch.exp(logdA[:, :, None] * l[None, None, :])
        K = torch.einsum("dn,dnl,dn->dl", Cc, P, dB).real
        L2 = 2 * L
        Kp = _pad(K, ((0, 0), (0, L)))
        up = (lambda x, *d: x.permute(*d))(u, 0, 2, 1)
        up = _pad(up, ((0, 0), (0, 0), (0, L)))
        Kf = (lambda x, axes=(-2,-1): torch.fft.rfft2(x.float(), dim=axes))(Kp[:, None, :], axes=(-2, -1))
        Uf = (lambda x, axes=(-2,-1): torch.fft.rfft2(x.float(), dim=axes))(up[:, :, None, :], axes=(-2, -1))
        Yf = Uf * Kf[None, :, :, :]
        y = (lambda x, s, axes=(-2,-1): torch.fft.irfft2(x, s=s, dim=axes).to(torch.bfloat16))(Yf, (1, L2), axes=(-2, -1))[:, :, 0, :L]
        y = (lambda x, *d: x.permute(*d))(y, 0, 2, 1) + u * Dskip[None, None, :]
        return y

    def _B_ref(x, w_gate, w_up, w_down):
        g = _silu(x @ w_gate)
        u = x @ w_up
        return (g * u) @ w_down

    x = A_u
    xn = _layernorm(x, axis=-1)
    s = _A_ref(xn, A_A_re, A_A_im, A_B_re, A_B_im, A_C_re, A_C_im, A_log_dt, A_Dskip)
    g = _silu(_B_ref(xn, B_w_gate, B_w_up, B_w_down))
    return x + s * g
```

```cpp
#include <hip/hip_runtime.h>
#include <stdint.h>

typedef unsigned short bf16_t;
typedef short bf16x8 __attribute__((ext_vector_type(8)));
typedef short s16x4 __attribute__((ext_vector_type(4)));
typedef float f32x2 __attribute__((ext_vector_type(2)));
typedef float f32x4 __attribute__((ext_vector_type(4)));
typedef float f32x16 __attribute__((ext_vector_type(16)));
typedef unsigned u32x4 __attribute__((ext_vector_type(4)));
typedef unsigned u32x2 __attribute__((ext_vector_type(2)));
typedef __bf16 bf16v2 __attribute__((ext_vector_type(2)));
#define LAS __attribute__((address_space(3)))
#define DI __device__ __forceinline__

constexpr int L_ = 2048, D_ = 512, N_ = 128, F_ = 1792;

constexpr size_t WS_XN = 0;
constexpr size_t WS_XT = 2u << 20;
constexpr size_t WS_ST = 4u << 20;
constexpr size_t WS_H = 6u << 20;

DI float bf2f(unsigned u16) { return __uint_as_float(u16 << 16); }
DI float bflo(unsigned w) { return __uint_as_float(w << 16); }
DI float bfhi(unsigned w) { return __uint_as_float(w & 0xffff0000u); }
DI unsigned pk2(float lo, float hi) {
    f32x2 v = {lo, hi};
    bf16v2 b = __builtin_convertvector(v, bf16v2);
    return __builtin_bit_cast(unsigned, b);
}
DI float rbf(float x) { return bflo(pk2(x, x)); }

#define WAITV(n) asm volatile("s_waitcnt vmcnt(" #n ")" ::: "memory")
#define WAITL(n) asm volatile("s_waitcnt lgkmcnt(" #n ")" ::: "memory")
#define BAR() __builtin_amdgcn_s_barrier()
#define SCHED() __builtin_amdgcn_sched_barrier(0)

DI float wave_sum(float v) {
#pragma unroll
    for (int o = 32; o >= 1; o >>= 1) v += __shfl_xor(v, o);
    return v;
}

DI s16x4 tr_read(LAS char* p) {
    return __builtin_amdgcn_ds_read_tr16_b64_v4i16((LAS s16x4*)p);
}
DI bf16x8 cat8(s16x4 a, s16x4 b) { return __builtin_shufflevector(a, b, 0, 1, 2, 3, 4, 5, 6, 7); }
DI void glds16(const void* g, LAS char* l) {
    __builtin_amdgcn_global_load_lds((const unsigned*)g, (LAS unsigned*)l, 16, 0, 0);
}

DI void ln_phase(const bf16_t* __restrict__ x, char* __restrict__ ws, int tb, LAS char* lds) {
    const int tid = threadIdx.x, w = tid >> 6, lane = tid & 63;
    constexpr int RS = 1056;
    u32x4 raw[2];
#pragma unroll
    for (int r = 0; r < 2; ++r) raw[r] = *(const u32x4*)(x + (size_t)(8 * tb + 2 * w + r) * D_ + lane * 8);
#pragma unroll
    for (int r = 0; r < 2; ++r) {
        float v[8];
#pragma unroll
        for (int j = 0; j < 4; ++j) { v[2 * j] = bflo(raw[r][j]); v[2 * j + 1] = bfhi(raw[r][j]); }
        float s = 0.f;
#pragma unroll
        for (int j = 0; j < 8; ++j) s += v[j];
        s = wave_sum(s);
        const float mean = s * (1.0f / 512.0f);
        float q = 0.f;
#pragma unroll
        for (int j = 0; j < 8; ++j) { const float d = v[j] - mean; q += d * d; }
        q = wave_sum(q);
        const float mb = rbf(mean);
        const float vb = rbf(q * (1.0f / 512.0f));
        const float sd = rbf(sqrtf(rbf(vb + 1e-5f)));
        u32x4 o;
#pragma unroll
        for (int j = 0; j < 4; ++j) {
            const float a = rbf(v[2 * j] - mb) / sd, b = rbf(v[2 * j + 1] - mb) / sd;
            o[j] = pk2(a, b);
        }
        *(LAS u32x4*)(lds + (2 * w + r) * RS + lane * 16) = o;
    }
    __syncthreads();
    const int mt = tb >> 4, tok0 = (tb & 15) * 8;
    char* xn = ws + WS_XN;
#pragma unroll
    for (int i = 0; i < 2; ++i) {
        const int e = tid + 256 * i, half = e & 1, tok = (e >> 1) & 7, U = e >> 4;
        const u32x4 v = *(LAS u32x4*)(lds + tok * RS + U * 32 + half * 16);
        *(u32x4*)(xn + ((size_t)((mt * 32 + U) * 128 + tok0 + tok)) * 32 + half * 16) = v;
    }
    char* xt = ws + WS_XT;
    const int g16 = lane >> 4, q = (lane & 15) >> 2, pp = lane & 3;
#pragma unroll
    for (int i = 0; i < 2; ++i) {
        const int c0 = 64 * (2 * w + i) + 16 * g16;
        const s16x4 a = tr_read(lds + q * RS + (c0 + 4 * pp) * 2);
        const s16x4 b = tr_read(lds + (4 + q) * RS + (c0 + 4 * pp) * 2);
        const bf16x8 v = cat8(a, b);
        *(bf16x8*)(xt + ((size_t)tb * 512 + c0 + (lane & 15)) * 16) = v;
    }
}

struct cf { float r, i; };
DI cf cmul(cf a, cf b) { return cf{a.r * b.r - a.i * b.i, a.r * b.i + a.i * b.r}; }
DI cf zdir(float a2, float th, float l) {
    const float m = __builtin_amdgcn_exp2f(a2 * l);
    const float r = __builtin_amdgcn_fractf(th * l);
    return cf{m * __builtin_amdgcn_cosf(r), m * __builtin_amdgcn_sinf(r)};
}

constexpr int KG_RS = 272;
constexpr int KG_WAVE = 53 * KG_RS;
constexpr int KG_PART = 4 * KG_WAVE + 11 * KG_RS;
constexpr int KG_KC = KG_PART + 2 * 12 * 256;
constexpr int KG_END = KG_KC + 2 * 1312;
constexpr int CV_K = 0;
constexpr int CV_KCH = 8320;
constexpr int CV_U = 2 * CV_KCH;
constexpr int CV_UCH = 4608;
constexpr int CV_RED = CV_U + 2 * CV_UCH;
constexpr int CV_END = CV_RED + 16384;

DI void kgen_phase(const bf16_t* __restrict__ A_re, const bf16_t* __restrict__ A_im, const bf16_t* __restrict__ B_re,
                   const bf16_t* __restrict__ B_im, const bf16_t* __restrict__ C_re, const bf16_t* __restrict__ C_im,
                   const bf16_t* __restrict__ log_dt, int c0, LAS char* lds) {
    const int tid = threadIdx.x, w = tid >> 6, lane = tid & 63;
    const int ch = c0 + (w >> 1), n = 64 * (w & 1) + lane;
    const int pi = ch * N_ + n;
    const float are = bf2f(A_re[pi]), aim = bf2f(A_im[pi]), br = bf2f(B_re[pi]), bi = bf2f(B_im[pi]);
    const float cr = bf2f(C_re[pi]), ci = bf2f(C_im[pi]);
    const float dt = rbf(expf(bf2f(log_dt[ch])));
    const float lr = dt * are, li = dt * aim;
    const cf dB{dt * br, dt * bi};
    const cf CB = cmul(cf{cr, ci}, dB);
    const float a2 = lr * 1.44269504088896341f, th = li * 0.15915494309189535f;
    LAS char* qs = lds + w * KG_WAVE + lane * 4;
    LAS char* ps = qs + 21 * KG_RS;
    auto putq = [&](int r, cf q) { *(LAS unsigned*)(qs + r * KG_RS) = pk2(q.r, -q.i); };
    auto putp = [&](int r, cf p) { *(LAS unsigned*)(ps + r * KG_RS) = pk2(p.r, p.i); };
    const cf z1 = zdir(a2, th, 1.f), w8 = zdir(a2, th, 8.f), Z256 = zdir(a2, th, 256.f);
    const cf z2 = cmul(z1, z1), z3 = cmul(z2, z1), z4 = cmul(z2, z2), z5 = cmul(z4, z1), z6 = cmul(z4, z2), z7 = cmul(z4, z3);
    putq(0, CB); putq(1, cmul(CB, z1)); putq(2, cmul(CB, z2)); putq(3, cmul(CB, z3));
    putq(4, cmul(CB, z4)); putq(5, cmul(CB, z5)); putq(6, cmul(CB, z6)); putq(7, cmul(CB, z7));
    const cf q256 = cmul(CB, Z256);
    putq(8, q256); putq(9, cmul(q256, z2)); putq(10, cmul(q256, z4)); putq(11, cmul(q256, z6));
    const cf q512 = cmul(q256, Z256);
    putq(12, q512); putq(13, cmul(q512, z4));
    const cf q768 = cmul(q512, Z256);
    putq(14, q768); putq(15, cmul(q768, z4));
    const cf q1024 = cmul(q768, Z256);
    putq(16, q1024);
    const cf q1280 = cmul(q1024, Z256); putq(17, q1280);
    const cf q1536 = cmul(q1280, Z256); putq(18, q1536);
    const cf q1792 = cmul(q1536, Z256); putq(19, q1792);
    putq(20, cmul(q1792, Z256));
    cf p{1.f, 0.f};
#pragma unroll
    for (int j = 0; j < 32; ++j) { putp(j, p); p = cmul(p, w8); }
    f32x16 acc = {};
    LAS char* qa = lds + w * KG_WAVE + (lane & 31) * KG_RS + (lane >> 5) * 16;
#pragma unroll
    for (int s = 0; s < 8; ++s) {
        const bf16x8 a = *(LAS bf16x8*)(qa + s * 32);
        const bf16x8 b = *(LAS bf16x8*)(qa + 21 * KG_RS + s * 32);
        acc = __builtin_amdgcn_mfma_f32_32x32x16_bf16(a, b, acc, 0, 0, 0);
    }
    LAS float* part = (LAS float*)(lds + KG_PART) + (w >> 1) * 12 * 64;
    if (w & 1) {
#pragma unroll
        for (int r = 0; r < 12; ++r) part[r * 64 + lane] = acc[r];
    }
    __syncthreads();
    if (!(w & 1)) {
        LAS bf16_t* kc = (LAS bf16_t*)(lds + KG_KC) + (w >> 1) * 656;
        const int j = lane & 31, hh = lane >> 5;
#pragma unroll
        for (int r = 0; r < 12; ++r) {
            const float val = acc[r] + part[r * 64 + lane];
            const int row = (r & 3) + 8 * (r >> 2) + 4 * hh;
            int l1;
            if (row < 8) l1 = row;
            else if (row < 12) l1 = 256 + 2 * (row - 8);
            else if (row < 16) l1 = 512 + 256 * ((row - 12) >> 1) + 4 * (row & 1);
            else l1 = 1024 + 256 * (row - 16);
            const int v = l1 + 8 * j;
            const int idx = v < 128 ? v : (int)(__float_as_uint((float)v) >> 16) - 17024;
            const bool ok = row < 20 || (row == 20 && j == 0);
            if (ok) kc[idx] = (bf16_t)(pk2(val, val) & 0xffffu);
        }
    }
    __syncthreads();
}

DI void kexpand_phase(LAS char* src_kc, LAS char* dst) {
    const int tid = threadIdx.x;
    for (int task = tid; task < 520; task += 256) {
        const int ch = task >= 260, g = task - 260 * ch;
        const LAS bf16_t* kc = (const LAS bf16_t*)src_kc + ch * 656;
        unsigned val[9];
#pragma unroll
        for (int e = 0; e < 9; ++e) {
            const int l = 2047 - 8 * g - e;
            const int lc = l < 0 ? 0 : l;
            const int idx = lc < 128 ? lc : (int)(pk2((float)lc, 0.f) & 0xffffu) - 17024;
            const unsigned t = kc[idx];
            val[e] = l < 0 ? 0u : t;
        }
        u32x4 E, O;
#pragma unroll
        for (int m = 0; m < 4; ++m) { E[m] = val[2 * m] | (val[2 * m + 1] << 16); O[m] = val[2 * m + 1] | (val[2 * m + 2] << 16); }
        *(LAS u32x4*)(dst + CV_K + ch * CV_KCH + g * 16) = E;
        *(LAS u32x4*)(dst + CV_K + ch * CV_KCH + 4160 + g * 16) = O;
    }
}

DI void uload_phase(const char* __restrict__ ws, int c0, LAS char* lds) {
    const int tid = threadIdx.x, w = __builtin_amdgcn_readfirstlane(tid >> 6), lane = tid & 63;
    const char* xt = ws + WS_XT;
#pragma unroll
    for (int i = 0; i < 2; ++i) {
        const int jj = (w & 1) * 2 + i;
        glds16(xt + ((size_t)(64 * jj + lane) * 512 + c0 + (w >> 1)) * 16, lds + CV_U + (w >> 1) * CV_UCH + 512 + jj * 1024);
    }
    if (tid < 64) *(LAS u32x4*)(lds + CV_U + (tid >> 5) * CV_UCH + (tid & 31) * 16) = u32x4{0, 0, 0, 0};
}

DI void conv_phase(const bf16_t* __restrict__ Dskip, char* __restrict__ ws, int c0, LAS char* lds) {
    const int tid = threadIdx.x, w = tid >> 6, lane = tid & 63;
    const int chl = w >> 1, rh = w & 1;
    const int a = lane & 15, kq = lane >> 4;
    LAS char* ub = lds + CV_U + chl * CV_UCH + 2 * (256 + 16 * (a - (kq >> 1)) + 8 * (kq & 1));
    const int i0 = 2047 - 16 * (kq >> 1) - a + 8 * (kq & 1);
    LAS char* kb = lds + CV_K + chl * CV_KCH + ((a & 1) ? 0 : 4160) + 4 * (i0 >> 1);
    f32x4 acc[8] = {};
#pragma unroll 1
    for (int rr = 0; rr < 4; ++rr) {
        const int rho = 8 * rh + 2 * rr;
        bf16x8 G[8], Fr[8];
#pragma unroll
        for (int m = 0; m < 8; ++m) {
            const LAS unsigned* p = (const LAS unsigned*)(kb - 32 * rho - 512 * m);
            u32x4 t;
            t[0] = p[0]; t[1] = p[1]; t[2] = p[2]; t[3] = p[3];
            G[m] = __builtin_bit_cast(bf16x8, t);
            Fr[m] = *(const LAS bf16x8*)(ub - 32 * rho + 512 * m);
        }
#pragma unroll
        for (int m = 0; m < 8; ++m)
#pragma unroll
            for (int i = m; i < 8; ++i) acc[i] = __builtin_amdgcn_mfma_f32_16x16x32_bf16(G[m], Fr[i - m], acc[i], 0, 0, 0);
    }
    LAS char* red = lds + CV_RED;
#pragma unroll
    for (int i = 0; i < 4; ++i) {
        f32x4 lo = acc[i], hi = acc[4 + i];
        asm volatile("" : "+v"(lo), "+v"(hi));
        *(LAS f32x4*)(red + (w * 4 + i) * 1024 + lane * 16) = rh ? lo : hi;
        acc[i] = rh ? hi : lo;
    }
    __syncthreads();
    const float dsk = bf2f(Dskip[c0 + chl]);
    bf16_t* st = (bf16_t*)(ws + WS_ST) + (size_t)(c0 + chl) * L_;
#pragma unroll
    for (int i = 0; i < 4; ++i) {
        const f32x4 o = *(LAS f32x4*)(red + ((w ^ 1) * 4 + i) * 1024 + lane * 16);
        const f32x4 y = acc[i] + o;
        const int t = 256 * (4 * rh + i) + 16 * a + 4 * kq;
        const u32x2 uu = *(LAS u32x2*)(lds + CV_U + chl * CV_UCH + 2 * (256 + t));
        const float s0 = rbf(y[0]) + rbf(bflo(uu[0]) * dsk), s1 = rbf(y[1]) + rbf(bfhi(uu[0]) * dsk);
        const float s2 = rbf(y[2]) + rbf(bflo(uu[1]) * dsk), s3 = rbf(y[3]) + rbf(bfhi(uu[1]) * dsk);
        *(u32x2*)(st + t) = u32x2{pk2(s0, s1), pk2(s2, s3)};
    }
}

constexpr int G1_SLOT = 28800, G1_UP = 14336 + 128;
DI float silu_f(float g) { return g * __builtin_amdgcn_rcpf(1.0f + __builtin_amdgcn_exp2f(-g * 1.44269504088896341f)); }

DI void g1_stage(const bf16_t* __restrict__ wg, const bf16_t* __restrict__ wu, int f0, int s, int w, const int (&voff)[7], LAS char* slot) {
    const char* base = (const char*)((w & 1) ? wu : wg) + (size_t)(64 * s + 32 * (w >> 1)) * (F_ * 2) + f0 * 2;
    LAS char* d = slot + (w & 1) * G1_UP + (w >> 1) * 7168;
#pragma unroll
    for (int i = 0; i < 7; ++i) glds16(base + voff[i], d + i * 1024);
}

DI void gemm1_phase(const bf16_t* __restrict__ wg, const bf16_t* __restrict__ wu, char* __restrict__ ws, int mt, int chunk, LAS char* lds) {
    const int tid = threadIdx.x, w = __builtin_amdgcn_readfirstlane(tid >> 6), lane = tid & 63;
    const int f0 = chunk * 112;
    int voff[7];
#pragma unroll
    for (int i = 0; i < 7; ++i) { const int sg = 64 * i + lane, k = sg / 14, c = sg - 14 * k; voff[i] = k * (F_ * 2) + c * 16; }
    bf16x8 xf[32];
    const char* xn = ws + WS_XN + ((size_t)(mt * 32) * 128 + 32 * w + (lane & 31)) * 32 + (lane >> 5) * 16;
#pragma unroll
    for (int U = 0; U < 32; ++U) xf[U] = *(const bf16x8*)(xn + (size_t)U * 4096);
    WAITV(0);
#pragma unroll
    for (int s = 0; s < 5; ++s) g1_stage(wg, wu, f0, s, w, voff, lds + s * G1_SLOT);
    const int g16 = lane >> 4, q = (lane & 15) >> 2, pp = lane & 3;
    const int phi = ((pp & 1) << 1) | (pp >> 1);
    const int aoff = (g16 & 1) * G1_UP + (8 * (g16 >> 1) + q) * 224 + phi * 8;
    f32x16 acc[7] = {};
#pragma unroll
    for (int s = 0; s < 8; ++s) {
        if (s <= 0) WAITV(28); else if (s == 1) WAITV(28); else if (s == 2) WAITV(28); else if (s == 3) WAITV(28);
        else if (s == 4) WAITV(21); else if (s == 5) WAITV(14); else if (s == 6) WAITV(7); else WAITV(0);
        BAR();
        LAS char* sl = lds + (s % 5) * G1_SLOT + aoff;
#pragma unroll
        for (int kk = 0; kk < 4; ++kk) {
#pragma unroll
            for (int p = 0; p < 7; ++p) {
                const s16x4 lo = tr_read(sl + kk * 16 * 224 + p * 32);
                const s16x4 hi = tr_read(sl + (kk * 16 + 4) * 224 + p * 32);
                acc[p] = __builtin_amdgcn_mfma_f32_32x32x16_bf16(cat8(lo, hi), xf[4 * s + kk], acc[p], 0, 0, 0);
            }
        }
        if (s + 5 < 8) {
            WAITL(0);
            BAR();
            g1_stage(wg, wu, f0, s + 5, w, voff, lds + (s % 5) * G1_SLOT);
        }
    }
    char* hb = ws + WS_H + ((size_t)(mt * 112 + chunk * 7) * 128 + 32 * w + (lane & 31)) * 32 + (lane >> 5) * 16;
#pragma unroll
    for (int p = 0; p < 7; ++p) {
        float h[8];
#pragma unroll
        for (int r = 0; r < 8; ++r) {
            const float g = rbf(acc[p][r]), u = rbf(acc[p][8 + r]);
            h[r] = rbf(silu_f(g)) * u;
        }
        *(u32x4*)(hb + (size_t)p * 4096) = u32x4{pk2(h[0], h[1]), pk2(h[2], h[3]), pk2(h[4], h[5]), pk2(h[6], h[7])};
    }
}

constexpr int G2_WREG = 28672;
constexpr int G2_PRS = 272;
constexpr int G2_PART = 64 * G2_PRS;
constexpr int G2_ST = 4 * G2_WREG;
DI void gemm2_phase(const bf16_t* __restrict__ wd, const bf16_t* __restrict__ x, bf16_t* __restrict__ out, char* __restrict__ ws,
                    int mt, int th, int dtile, LAS char* lds) {
    const int tid = threadIdx.x, w = __builtin_amdgcn_readfirstlane(tid >> 6), lane = tid & 63;
    const int d0 = dtile * 64, t0 = mt * 128 + th * 64;
#pragma unroll
    for (int i = 0; i < 2; ++i) {
        const int row = 16 * w + 8 * i + (lane >> 3);
        glds16(ws + WS_ST + ((size_t)(d0 + row) * L_ + t0) * 2 + (lane & 7) * 16, lds + G2_ST + (2 * w + i) * 1024);
    }
    bf16x8 wf[28][2];
    const int g16 = lane >> 4, q = (lane & 15) >> 2, pp = lane & 3;
    const int phi = ((pp & 1) << 1) | (pp >> 1);
    LAS char* wl = lds + w * G2_WREG;
    const char* wsrc = (const char*)wd + (size_t)(448 * w + (lane >> 3)) * 1024 + d0 * 2 + (((lane & 7) ^ (4 * ((lane >> 4) & 1))) * 16);
    const int rrow = 8 * (g16 >> 1) + q;
    const int rsw = 4 * ((q >> 1) & 1);
    const int roff0 = rrow * 128 + (((2 * (g16 & 1) + (phi >> 1)) ^ rsw) * 16) + 8 * (phi & 1);
    const int roff1 = rrow * 128 + (((4 + 2 * (g16 & 1) + (phi >> 1)) ^ rsw) * 16) + 8 * (phi & 1);
#pragma unroll
    for (int rnd = 0; rnd < 2; ++rnd) {
#pragma unroll
        for (int i = 0; i < 28; ++i) glds16(wsrc + (size_t)(rnd * 224 + 8 * i) * 1024, wl + i * 1024);
        WAITV(0);
#pragma unroll
        for (int s = 0; s < 14; ++s) {
            wf[rnd * 14 + s][0] = cat8(tr_read(wl + s * 2048 + roff0), tr_read(wl + s * 2048 + 512 + roff0));
            wf[rnd * 14 + s][1] = cat8(tr_read(wl + s * 2048 + roff1), tr_read(wl + s * 2048 + 512 + roff1));
        }
        WAITL(0);
        SCHED();
    }
    const char* hb = ws + WS_H + ((size_t)(mt * 112 + 28 * w) * 128 + 64 * th + (lane & 31)) * 32 + (lane >> 5) * 16;
    constexpr int PD = 8;
    bf16x8 hf[PD][2];
#pragma unroll
    for (int s = 0; s < PD; ++s) { hf[s][0] = *(const bf16x8*)(hb + (size_t)s * 4096); hf[s][1] = *(const bf16x8*)(hb + (size_t)s * 4096 + 1024); }
    f32x16 acc[2][2] = {};
#pragma unroll
    for (int s = 0; s < 28; ++s) {
        const bf16x8 h0 = hf[s % PD][0], h1 = hf[s % PD][1];
        acc[0][0] = __builtin_amdgcn_mfma_f32_32x32x16_bf16(wf[s][0], h0, acc[0][0], 0, 0, 0);
        acc[1][0] = __builtin_amdgcn_mfma_f32_32x32x16_bf16(wf[s][1], h0, acc[1][0], 0, 0, 0);
        acc[0][1] = __builtin_amdgcn_mfma_f32_32x32x16_bf16(wf[s][0], h1, acc[0][1], 0, 0, 0);
        acc[1][1] = __builtin_amdgcn_mfma_f32_32x32x16_bf16(wf[s][1], h1, acc[1][1], 0, 0, 0);
        if (s + PD < 28) {
            hf[s % PD][0] = *(const bf16x8*)(hb + (size_t)(s + PD) * 4096);
            hf[s % PD][1] = *(const bf16x8*)(hb + (size_t)(s + PD) * 4096 + 1024);
        }
    }
    __syncthreads();
    {
        LAS char* pb = lds + w * G2_PART + (lane & 31) * G2_PRS + (lane >> 5) * 32;
#pragma unroll
        for (int tt = 0; tt < 2; ++tt)
#pragma unroll
            for (int dh = 0; dh < 2; ++dh)
#pragma unroll
                for (int g = 0; g < 2; ++g) {
                    LAS char* p = pb + tt * 32 * G2_PRS + dh * 128 + g * 64;
                    const f32x16 a = acc[dh][tt];
                    *(LAS f32x4*)(p) = f32x4{a[8 * g], a[8 * g + 1], a[8 * g + 2], a[8 * g + 3]};
                    *(LAS f32x4*)(p + 16) = f32x4{a[8 * g + 4], a[8 * g + 5], a[8 * g + 6], a[8 * g + 7]};
                }
    }
    __syncthreads();
    const int tk = 16 * w + (lane & 15);
#pragma unroll
    for (int dh = 0; dh < 2; ++dh) {
        const int dd = 32 * dh + 8 * g16;
        f32x4 s0 = {}, s1 = {};
#pragma unroll
        for (int sw = 0; sw < 4; ++sw) {
            LAS char* p = lds + sw * G2_PART + tk * G2_PRS + dd * 4;
            s0 += *(LAS f32x4*)p; s1 += *(LAS f32x4*)(p + 16);
        }
        const s16x4 sa = tr_read(lds + G2_ST + (dd + q) * 128 + (16 * w + 4 * pp) * 2);
        const s16x4 sb = tr_read(lds + G2_ST + (dd + 4 + q) * 128 + (16 * w + 4 * pp) * 2);
        const u32x4 xv = *(const u32x4*)(x + (size_t)(t0 + tk) * D_ + d0 + dd);
        float o[8];
#pragma unroll
        for (int j = 0; j < 8; ++j) {
            const float f = rbf(j < 4 ? s0[j] : s1[j - 4]);
            const float g = rbf(silu_f(f));
            const float sv = bf2f((unsigned short)(j < 4 ? sa[j] : sb[j - 4]));
            const float xx = (j & 1) ? bfhi(xv[j >> 1]) : bflo(xv[j >> 1]);
            o[j] = xx + rbf(sv * g);
        }
        *(u32x4*)(out + (size_t)(t0 + tk) * D_ + d0 + dd) = u32x4{pk2(o[0], o[1]), pk2(o[2], o[3]), pk2(o[4], o[5]), pk2(o[6], o[7])};
    }
}

extern __shared__ __attribute__((aligned(16))) char dyn_smem[];

__global__ __launch_bounds__(256, 1) void k_ln(const bf16_t* __restrict__ x, char* __restrict__ ws) {
    const int b = blockIdx.x;
    ln_phase(x, ws, (b & 7) * 32 + (b >> 3), (LAS char*)dyn_smem);
}

__global__ __launch_bounds__(256, 1) void k_mid(const bf16_t* __restrict__ A_re, const bf16_t* __restrict__ A_im, const bf16_t* __restrict__ B_re,
                                                const bf16_t* __restrict__ B_im, const bf16_t* __restrict__ C_re, const bf16_t* __restrict__ C_im,
                                                const bf16_t* __restrict__ log_dt, const bf16_t* __restrict__ Dskip,
                                                const bf16_t* __restrict__ wg, const bf16_t* __restrict__ wu, char* __restrict__ ws) {
    const int b = blockIdx.x, xcd = b & 7, j = b >> 3;
    LAS char* lds = (LAS char*)dyn_smem;
    const int c0 = 2 * (xcd * 32 + j);
    constexpr int CVB = 69632;
    kgen_phase(A_re, A_im, B_re, B_im, C_re, C_im, log_dt, c0, lds);
    uload_phase(ws, c0, lds + CVB);
    kexpand_phase(lds + KG_KC, lds + CVB);
    WAITV(0);
    __syncthreads();
    conv_phase(Dskip, ws, c0, lds + CVB);
    __syncthreads();
    gemm1_phase(wg, wu, ws, 2 * xcd + (j >> 4), j & 15, lds);
}

__global__ __launch_bounds__(256, 1) void k_out(const bf16_t* __restrict__ wd, const bf16_t* __restrict__ x, bf16_t* __restrict__ out, char* __restrict__ ws) {
    const int b = blockIdx.x, xcd = b & 7, j = b >> 3;
    gemm2_phase(wd, x, out, ws, 2 * xcd + (j >> 4), (j >> 3) & 1, j & 7, (LAS char*)dyn_smem);
}

extern "C" void kernel_launch(void* const* d_in, const int* in_sizes, int n_in, void* d_out, int out_size, void* d_ws, size_t ws_size,
                              hipStream_t stream) {
    const bf16_t* x = (const bf16_t*)d_in[0];
    char* ws = (char*)d_ws;
    constexpr int LDS_MID = 5 * G1_SLOT, LDS_OUT = G2_ST + 8192;
    static_assert(KG_END <= 69632 && 69632 + CV_END <= LDS_MID, "lds");
    static_assert(4 * G2_PART <= G2_ST, "lds");
    (void)hipFuncSetAttribute((const void*)k_mid, hipFuncAttributeMaxDynamicSharedMemorySize, LDS_MID);
    (void)hipFuncSetAttribute((const void*)k_out, hipFuncAttributeMaxDynamicSharedMemorySize, LDS_OUT);
    k_ln<<<256, 256, 8 * 1056, stream>>>(x, ws);
    k_mid<<<256, 256, LDS_MID, stream>>>((const bf16_t*)d_in[1], (const bf16_t*)d_in[2], (const bf16_t*)d_in[3], (const bf16_t*)d_in[4],
                                         (const bf16_t*)d_in[5], (const bf16_t*)d_in[6], (const bf16_t*)d_in[7], (const bf16_t*)d_in[8],
                                         (const bf16_t*)d_in[10], (const bf16_t*)d_in[11], ws);
    k_out<<<256, 256, LDS_OUT, stream>>>((const bf16_t*)d_in[12], x, (bf16_t*)d_out, ws);
}
```

```cpp
#include <hip/hip_runtime.h>
#include <stdint.h>

typedef unsigned short bf16_t;
typedef short bf16x8 __attribute__((ext_vector_type(8)));
typedef short s16x4 __attribute__((ext_vector_type(4)));
typedef float f32x2 __attribute__((ext_vector_type(2)));
typedef float f32x4 __attribute__((ext_vector_type(4)));
typedef float f32x16 __attribute__((ext_vector_type(16)));
typedef unsigned u32x4 __attribute__((ext_vector_type(4)));
typedef unsigned u32x2 __attribute__((ext_vector_type(2)));
typedef __bf16 bf16v2 __attribute__((ext_vector_type(2)));
typedef unsigned long long u64;
#define LAS __attribute__((address_space(3)))
#define GAS __attribute__((address_space(1)))
#define DI __device__ __forceinline__

constexpr int L_ = 2048, D_ = 512, N_ = 128, F_ = 1792;

constexpr unsigned WS_XN = 0;
constexpr unsigned WS_XT = 2u << 20;
constexpr unsigned WS_ST = 4u << 20;
constexpr unsigned WS_H = 6u << 20;
constexpr unsigned WS_FX = 14u << 20;
constexpr unsigned WS_FH = WS_FX + 256 * 128;
constexpr unsigned WS_FS = WS_FH + 256 * 128;
constexpr unsigned WS_FXP = WS_FS + 256 * 128;
constexpr unsigned WS_EP = WS_FXP + 256 * 8;
constexpr unsigned WS_USED = 15u << 20;

constexpr int G1_SLOT = 28800, G1_UP = 14336 + 128;
constexpr int KG_BASE = 3 * G1_SLOT, KG_RS = 528, KG_CH = 53 * KG_RS;
constexpr int L_KC = 144128;
constexpr int L_KIMG = 146816;
constexpr int L_LNS = L_KIMG, LN_RS = 1056;
constexpr int L_PF = L_LNS + 8 * LN_RS;
constexpr int CV_KCH = 8320;
constexpr int L_U = 2 * G1_SLOT, CV_UCH = 4608;
constexpr int L_RED = L_U + 2 * CV_UCH;
constexpr int G2_PRS = 272, G2_PART = 64 * G2_PRS;
constexpr int L_ST = 4 * G2_PART;
constexpr int LDS_TOTAL = 163840;
static_assert(KG_BASE + 2 * KG_CH <= L_KC, "K-gen scratch");
static_assert(L_KIMG + 2 * CV_KCH <= LDS_TOTAL && L_RED + 16384 <= 3 * G1_SLOT && L_ST + 8192 <= 3 * G1_SLOT, "lds map");

DI float bf2f(unsigned u16) { return __uint_as_float(u16 << 16); }
DI float bflo(unsigned w) { return __uint_as_float(w << 16); }
DI float bfhi(unsigned w) { return __uint_as_float(w & 0xffff0000u); }
DI unsigned pk2(float lo, float hi) {
    f32x2 v = {lo, hi};
    bf16v2 b = __builtin_convertvector(v, bf16v2);
    return __builtin_bit_cast(unsigned, b);
}
DI float rbf(float x) { return bflo(pk2(x, x)); }

#define WAITV(n) asm volatile("s_waitcnt vmcnt(" #n ")" ::: "memory")
#define WAITL(n) asm volatile("s_waitcnt lgkmcnt(" #n ")" ::: "memory")
#define BAR() do { asm volatile("" ::: "memory"); __builtin_amdgcn_s_barrier(); asm volatile("" ::: "memory"); } while (0)
#define LBAR() do { WAITL(0); BAR(); } while (0)
#define SCHED() __builtin_amdgcn_sched_barrier(0)

#define TS(i) do {} while (0)
#define TSR(i) do {} while (0)
#define TS_DUMP() do {} while (0)

template <int CTRL, int RMASK> DI float dpp_add(float v) {
    return v + __builtin_bit_cast(float, __builtin_amdgcn_update_dpp(0, __builtin_bit_cast(int, v), CTRL, RMASK, 0xf, true));
}
DI float wave_sum(float v) {
    v = dpp_add<0x111, 0xf>(v); v = dpp_add<0x112, 0xf>(v); v = dpp_add<0x114, 0xf>(v); v = dpp_add<0x118, 0xf>(v);
    v = dpp_add<0x142, 0xa>(v); v = dpp_add<0x143, 0xc>(v);
    return __builtin_bit_cast(float, __builtin_amdgcn_readlane(__builtin_bit_cast(int, v), 63));
}
DI s16x4 tr_read(LAS char* p) { return __builtin_amdgcn_ds_read_tr16_b64_v4i16((LAS s16x4*)p); }
DI bf16x8 cat8(s16x4 a, s16x4 b) { return __builtin_shufflevector(a, b, 0, 1, 2, 3, 4, 5, 6, 7); }
DI void glds16(const void* sbase, unsigned voff, unsigned ldst) {
    asm volatile("s_mov_b32 m0, %2\n\ts_nop 0\n\tglobal_load_lds_dwordx4 %0, %1" ::"v"(voff), "s"(sbase), "s"(ldst) : "memory");
}
DI void glds4(const void* sbase, unsigned voff, unsigned ldst) {
    asm volatile("s_mov_b32 m0, %2\n\ts_nop 0\n\tglobal_load_lds_dword %0, %1" ::"v"(voff), "s"(sbase), "s"(ldst) : "memory");
}
typedef __amdgpu_buffer_rsrc_t rsrc_t;
constexpr int SC1 = 16;
DI u32x4 ld128(rsrc_t r, unsigned voff, unsigned soff) { return __builtin_amdgcn_raw_buffer_load_b128(r, voff, soff, SC1); }
DI void st128(rsrc_t r, u32x4 v, unsigned voff, unsigned soff) {
    asm volatile("buffer_store_dwordx4 %0, %1, %2, %3 offen sc1\n\ts_nop 1" ::"v"(v), "v"(voff), "s"(r), "s"(soff) : "memory");
}
DI void st64(rsrc_t r, u32x2 v, unsigned voff, unsigned soff) { __builtin_amdgcn_raw_buffer_store_b64(v, r, voff, soff, SC1); }
DI bf16x8 asbf(u32x4 v) { return __builtin_bit_cast(bf16x8, v); }

template <int SIMM, int LIMM> DI void bdma16(rsrc_t r, unsigned voff, unsigned sbase, unsigned lbase) {
    unsigned t;
    asm volatile("s_add_u32 m0, %1, %2\n\ts_add_u32 %0, %3, %4\n\tbuffer_load_dwordx4 %5, %6, %0 offen lds"
                 : "=&s"(t) : "s"(lbase), "i"(LIMM), "s"(sbase), "i"(SIMM), "v"(voff), "s"(r) : "memory", "scc");
}
DI void aload16(u32x4& d, const void* sbase, unsigned voff) { asm volatile("global_load_dwordx4 %0, %1, %2" : "=&v"(d) : "v"(voff), "s"(sbase) : "memory"); }
DI void aload2(unsigned& d, const void* sbase, unsigned voff) { asm volatile("global_load_ushort %0, %1, %2" : "=&v"(d) : "v"(voff), "s"(sbase) : "memory"); }
DI void aload8sc1(u64& d, const void* sbase, unsigned voff) { asm volatile("global_load_dwordx2 %0, %1, %2 sc1" : "=&v"(d) : "v"(voff), "s"(sbase) : "memory"); }
template <int SIMM> DI void abload16sc1(bf16x8& d, rsrc_t r, unsigned voff, unsigned sbase) {
    unsigned t;
    asm volatile("s_add_u32 %1, %4, %5\n\tbuffer_load_dwordx4 %0, %2, %3, %1 offen sc1" : "=&a"(d), "=&s"(t) : "v"(voff), "s"(r), "s"(sbase), "i"(SIMM) : "memory", "scc");
}
template <bool FIRST> DI void mfma32_va(f32x16& c, bf16x8 a, bf16x8 b) {
    if constexpr (FIRST) asm volatile("v_mfma_f32_32x32x16_bf16 %0, %1, %2, 0" : "=&v"(c) : "v"(a), "a"(b));
    else asm volatile("v_mfma_f32_32x32x16_bf16 %0, %1, %2, %0" : "+v"(c) : "v"(a), "a"(b));
}
template <bool FIRST> DI void mfma32_av(f32x16& c, bf16x8 a, bf16x8 b) {
    if constexpr (FIRST) asm volatile("v_mfma_f32_32x32x16_bf16 %0, %1, %2, 0" : "=&v"(c) : "a"(a), "v"(b));
    else asm volatile("v_mfma_f32_32x32x16_bf16 %0, %1, %2, %0" : "+v"(c) : "a"(a), "v"(b));
}
template <bool FIRST> DI void mfma16_vv(f32x4& c, bf16x8 a, bf16x8 b) {
    if constexpr (FIRST) asm volatile("v_mfma_f32_16x16x32_bf16 %0, %1, %2, 0" : "=&v"(c) : "v"(a), "v"(b));
    else asm volatile("v_mfma_f32_16x16x32_bf16 %0, %1, %2, %0" : "+v"(c) : "v"(a), "v"(b));
}
#define MFMA_DRAIN() asm volatile("s_nop 15\n\ts_nop 3" ::: "memory")
template <int SIMM> DI void abload16sc1_v(u32x4& d, rsrc_t r, unsigned voff, unsigned sbase) {
    unsigned t;
    asm volatile("s_add_u32 %1, %4, %5\n\tbuffer_load_dwordx4 %0, %2, %3, %1 offen sc1" : "=&v"(d), "=&s"(t) : "v"(voff), "s"(r), "s"(sbase), "i"(SIMM) : "memory", "scc");
}
template <int IMM> DI void aload8sc1_o(u64& d, const void* sbase, unsigned voff) {
    asm volatile("global_load_dwordx2 %0, %1, %2 sc1" : "=&v"(d) : "v"(voff + IMM), "s"(sbase) : "memory");
}
template <int N> DI void waitv() { asm volatile("s_waitcnt vmcnt(%0)" ::"i"(N) : "memory"); }
template <int N> struct IC { static constexpr int v = N; };
template <int B, int E, class F> DI void sfor(F&& f) { if constexpr (B < E) { f(IC<B>{}); sfor<B + 1, E>(f); } }

struct G1Op { int step, gap, kind, a, b; };
struct G1Sched {
    G1Op op[128]; int n;
    constexpr G1Sched() : op{}, n(0) {
        for (int u = 0; u < 8; ++u) op[n++] = G1Op{-1, 0, 0, u, 0};
        for (int s = 0; s < 8; ++s) {
            G1Op list[16] = {}; int m = 0;
            if (s < 3) for (int i = 0; i < 8; ++i) { list[m++] = G1Op{0, 0, 0, 8 + 8 * s + i, 0}; if (i < 7) list[m++] = G1Op{0, 0, 1, 3 + s, i}; }
            if (s == 3 || s == 4) for (int i = 0; i < 7; ++i) list[m++] = G1Op{0, 0, 1, 3 + s, i};
            if (s == 4) for (int i = 0; i < 2; ++i) list[m++] = G1Op{0, 0, 3, i, 0};
            if (s == 6) for (int i = 0; i < 2; ++i) list[m++] = G1Op{0, 0, 4, i, 0};
            if (s >= 4) for (int i = 0; i < 7; ++i) list[m++] = G1Op{0, 0, 2, s - 4, i};
            const int span = s == 7 ? 14 : 28;
            for (int i = 0; i < m; ++i) { const int g = (i * span) / m; list[i].step = 4 * s + g / 7; list[i].gap = g % 7; op[n++] = list[i]; }
        }
    }
    constexpr int younger(int kind, int a, int st) const {
        int last = -1;
        for (int i = 0; i < n; ++i) if (op[i].kind == kind && op[i].a == a) last = i;
        int c = 0;
        for (int i = last + 1; i < n; ++i) if (op[i].step < st) ++c;
        return c;
    }
};
constexpr G1Sched G1S{};

DI u64 ld_flag(const char* ws, unsigned off) { return __hip_atomic_load((GAS u64*)(ws + off), __ATOMIC_RELAXED, __HIP_MEMORY_SCOPE_AGENT); }
DI void st_flag(char* ws, unsigned off, u64 v) { __hip_atomic_store((GAS u64*)(ws + off), v, __ATOMIC_RELAXED, __HIP_MEMORY_SCOPE_AGENT); }
template <int NPL, int STRIDE = 128>
DI void wait_flags(const char* ws, unsigned base, int nl, u64 tag, int lane) {
    const unsigned off = base + (lane < nl ? lane : 0) * STRIDE;
#pragma unroll 1
    for (int spin = 0; spin < 40000; ++spin) {
        bool ok = true;
#pragma unroll
        for (int i = 0; i < NPL; ++i) ok &= ld_flag(ws, off + i * 64 * STRIDE) == tag;
        if (__all(ok)) break;
        __builtin_amdgcn_s_sleep(1);
    }
    asm volatile("" ::: "memory");
}

typedef f32x2 cf;
DI cf cmul(cf a, cf b) {
    cf t, z;
    asm("v_pk_mul_f32 %0, %1, %2 op_sel_hi:[0,1]" : "=v"(t) : "v"(a), "v"(b));
    asm("v_pk_fma_f32 %0, %1, %2, %3 op_sel:[1,1,0] op_sel_hi:[1,0,1] neg_lo:[0,1,0]" : "=v"(z) : "v"(a), "v"(b), "v"(t));
    return z;
}
DI void cmul2(cf& z0, cf a0, cf b0, cf& z1, cf a1, cf b1) {
    cf t0, t1;
    asm("v_pk_mul_f32 %2, %4, %5 op_sel_hi:[0,1]\n\tv_pk_mul_f32 %3, %6, %7 op_sel_hi:[0,1]\n\t"
        "v_pk_fma_f32 %0, %4, %5, %2 op_sel:[1,1,0] op_sel_hi:[1,0,1] neg_lo:[0,1,0]\n\t"
        "v_pk_fma_f32 %1, %6, %7, %3 op_sel:[1,1,0] op_sel_hi:[1,0,1] neg_lo:[0,1,0]"
        : "=&v"(z0), "=&v"(z1), "=&v"(t0), "=&v"(t1) : "v"(a0), "v"(b0), "v"(a1), "v"(b1));
}
DI cf zdir(float a2, float th, float l) {
    const float m = __builtin_amdgcn_exp2f(a2 * l);
    const float r = __builtin_amdgcn_fractf(th * l);
    return cf{m * __builtin_amdgcn_cosf(r), m * __builtin_amdgcn_sinf(r)};
}
DI f32x2 silu2(f32x2 g) {
    const f32x2 t = g * -1.44269504088896341f;
    const f32x2 d = f32x2{__builtin_amdgcn_exp2f(t[0]), __builtin_amdgcn_exp2f(t[1])} + 1.0f;
    return g * f32x2{__builtin_amdgcn_rcpf(d[0]), __builtin_amdgcn_rcpf(d[1])};
}
DI unsigned pk2v(f32x2 v) { return __builtin_bit_cast(unsigned, __builtin_convertvector(v, bf16v2)); }
DI f32x2 unpk(unsigned w) { return f32x2{bflo(w), bfhi(w)}; }
DI float silu_f(float g) { return g * __builtin_amdgcn_rcpf(1.0f + __builtin_amdgcn_exp2f(g * -1.44269504088896341f)); }

extern __shared__ __attribute__((aligned(16))) char dyn_smem[];

__global__ __launch_bounds__(256, 1) void fused_s4_ffn(const bf16_t* __restrict__ x, const bf16_t* __restrict__ A_re, const bf16_t* __restrict__ A_im,
                                                       const bf16_t* __restrict__ B_re, const bf16_t* __restrict__ B_im, const bf16_t* __restrict__ C_re,
                                                       const bf16_t* __restrict__ C_im, const bf16_t* __restrict__ log_dt, const bf16_t* __restrict__ Dskip,
                                                       const bf16_t* __restrict__ wg, const bf16_t* __restrict__ wu, const bf16_t* __restrict__ wd,
                                                       bf16_t* __restrict__ out, char* ws) {
    LAS char* lds = (LAS char*)dyn_smem;
    const unsigned lbase = (unsigned)(size_t)lds;
    const int tid = threadIdx.x, w = __builtin_amdgcn_readfirstlane(tid >> 6), lane = tid & 63;
    const int b = blockIdx.x, xcd = b & 7, j = b >> 3;
    const int tb = xcd * 32 + j;
    const int mt = 8 * (xcd >> 2) + (j >> 2), chunk = 4 * (xcd & 3) + (j & 3);
    const int mt2 = 2 * xcd + (j >> 4);
    const int c0 = 2 * tb;
    TSR(30); TS(0);

    u32x4 raw[2];
#pragma unroll
    for (int r = 0; r < 2; ++r) aload16(raw[r], x + (size_t)(8 * tb + 2 * w + r) * D_, lane * 16);
    {
        u64 pc; unsigned len;
        asm volatile("s_getpc_b64 %0\n.Lfused_pf_here:\n\ts_mov_b32 %1, .Lfused_pf_end-.Lfused_pf_here" : "=s"(pc), "=s"(len));
#pragma unroll
        for (int i = 0; i < 2; ++i) {
            const unsigned o = (unsigned)(i * 256 + tid) * 128u;
            glds4((const void*)pc, o < len ? o : len, lbase + L_PF);
        }
    }
    u64 ep;
    aload8sc1(ep, ws + WS_EP, 0);
    const int kch = c0 + (w >> 1);
    const unsigned kpo = (64 * (w & 1) + lane) * 2;
    unsigned p_are, p_aim, p_br, p_bi, p_cr, p_ci, p_ldt, p_dsk;
    aload2(p_are, A_re + kch * N_, kpo); aload2(p_aim, A_im + kch * N_, kpo); aload2(p_br, B_re + kch * N_, kpo);
    aload2(p_bi, B_im + kch * N_, kpo); aload2(p_cr, C_re + kch * N_, kpo); aload2(p_ci, C_im + kch * N_, kpo);
    aload2(p_ldt, log_dt + kch, 0); aload2(p_dsk, Dskip + kch, 0);
    SCHED();
    const rsrc_t rs = __builtin_amdgcn_make_buffer_rsrc(ws, 0, WS_USED, 0x00020000);
    const rsrc_t rw1 = __builtin_amdgcn_make_buffer_rsrc((void*)((w & 1) ? wu : wg), 0, D_ * F_ * 2, 0x00020000);
    const rsrc_t rwd = __builtin_amdgcn_make_buffer_rsrc((void*)wd, 0, D_ * F_ * 2, 0x00020000);
    unsigned voff[7];
    {
        const int k = (lane * 147) >> 11, c = lane - 14 * k;
        unsigned v = k * (F_ * 2) + c * 16;
        int cc = c;
#pragma unroll
        for (int i = 0; i < 7; ++i) {
            voff[i] = v;
            cc += 8; v += 4 * (F_ * 2) + 128;
            if (cc >= 14) { cc -= 14; v += F_ * 2 - 224; }
        }
    }
    const unsigned w1s = (32 * (w >> 1)) * (F_ * 2) + chunk * 224;
    const unsigned w1l = lbase + (w & 1) * G1_UP + (w >> 1) * 7168;
#define STAGE_W1(S, I) bdma16<64 * (S) * (F_ * 2), ((S) % 5) * G1_SLOT + (I) * 1024>(rw1, voff[I], w1s, w1l)
    sfor<0, 7>([&](auto I) { STAGE_W1(0, I.v); });
    {
        const int r = 56 * (w >> 1) + (lane < 56 ? lane : 55), row = (r * 147) >> 10;
        glds4((w & 1) ? wu : wg, (16 * j + row) * (F_ * 2) + (xcd & 3) * 896 + (r - 7 * row) * 128, lbase + L_PF);
        glds4(wd, (448 * j + tid) * 128, lbase + L_PF);
        glds4(wd, (448 * j + (tid < 192 ? 256 + tid : 447)) * 128, lbase + L_PF);
    }
    SCHED();
    const int g16 = lane >> 4, q4 = (lane & 15) >> 2, pp = lane & 3, l15 = lane & 15;
    unsigned ksc[8];
    {
        const int col8 = 8 * (16 * (w & 1) + l15);
        const unsigned kcb = lbase + L_KC + (w >> 1) * 1312;
#pragma unroll
        for (int r = 0; r < 8; ++r) {
            int l1;
            if (r < 4) l1 = g16 == 0 ? r : g16 == 1 ? 4 + r : g16 == 2 ? 256 + 2 * r : 512 + 256 * (r >> 1) + 4 * (r & 1);
            else l1 = g16 == 0 ? 1024 + 256 * (r - 4) : 2048;
            const int v = l1 + col8;
            int idx = v < 128 ? v : (int)(__float_as_uint((float)v) >> 16) - 17024;
            if (r >= 4 && !(g16 == 0 || (g16 == 1 && r == 4 && col8 == 0))) idx = 648;
            ksc[r] = kcb + idx * 2;
            asm volatile("" : "+v"(ksc[r]));
        }
    }
    unsigned kga[9];
#pragma unroll
    for (int e = 0; e < 9; ++e) {
        const int l = 2047 - 8 * tid - e;
        int idx = l < 256 ? l : (int)(pk2((float)l, 0.f) & 0xffffu) - 17024;
        if (l < 0) idx = 649;
        kga[e] = lbase + L_KC + idx * 2;
        asm volatile("" : "+v"(kga[e]));
    }
    SCHED();
    asm volatile("s_waitcnt vmcnt(10)" : "+v"(ep), "+v"(raw[0]), "+v"(raw[1]), "+v"(p_are), "+v"(p_aim), "+v"(p_br), "+v"(p_bi), "+v"(p_cr), "+v"(p_ci),
                 "+v"(p_ldt), "+v"(p_dsk)::"memory");
    SCHED();
    TS(1);

    {
        f32x2 v[2][4];
        float s1[2], s2[2];
#pragma unroll
        for (int r = 0; r < 2; ++r) {
#pragma unroll
            for (int jj = 0; jj < 4; ++jj) v[r][jj] = unpk(raw[r][jj]);
            const f32x2 t = (v[r][0] + v[r][1]) + (v[r][2] + v[r][3]);
            s1[r] = t[0] + t[1];
        }
        s1[0] = wave_sum(s1[0]); s1[1] = wave_sum(s1[1]);
#pragma unroll
        for (int r = 0; r < 2; ++r) {
            const float mean = s1[r] * (1.0f / 512.0f);
            f32x2 q = {0.f, 0.f};
#pragma unroll
            for (int jj = 0; jj < 4; ++jj) { const f32x2 d = v[r][jj] - mean; q += d * d; }
            s2[r] = q[0] + q[1];
        }
        s2[0] = wave_sum(s2[0]); s2[1] = wave_sum(s2[1]);
#pragma unroll
        for (int r = 0; r < 2; ++r) {
            const float mb = rbf(s1[r] * (1.0f / 512.0f));
            const float sd = rbf(__builtin_amdgcn_sqrtf(rbf(rbf(s2[r] * (1.0f / 512.0f)) + 1e-5f)));
            const float rsd = __builtin_amdgcn_rcpf(sd);
            u32x4 o;
#pragma unroll
            for (int jj = 0; jj < 4; ++jj) o[jj] = pk2v(unpk(pk2v(v[r][jj] - mb)) * rsd);
            *(LAS u32x4*)(lds + L_LNS + (2 * w + r) * LN_RS + lane * 16) = o;
        }
    }
    LBAR();
    TS(2);
    {
        const int lmt = tb >> 4, tok0 = (tb & 15) * 8;
        const int half = tid & 1, tok = (tid >> 1) & 7, U = tid >> 4;
        const u32x4 va = *(LAS u32x4*)(lds + L_LNS + tok * LN_RS + U * 32 + half * 16);
        const u32x4 vb = *(LAS u32x4*)(lds + L_LNS + tok * LN_RS + U * 32 + half * 16 + 512);
        LAS char* tp = lds + L_LNS + q4 * LN_RS + (128 * w + 16 * g16 + 4 * pp) * 2;
        const s16x4 a0 = tr_read(tp), b0 = tr_read(tp + 4 * LN_RS), a1 = tr_read(tp + 128), b1 = tr_read(tp + 4 * LN_RS + 128);
        const unsigned xo = WS_XN + ((lmt * 32 + U) * 128 + tok0 + tok) * 32 + half * 16;
        st128(rs, va, xo, 0);
        st128(rs, vb, xo, 16 * 4096);
        const unsigned to = WS_XT + (tb * 512 + 128 * w + 16 * g16 + l15) * 16;
        st128(rs, __builtin_bit_cast(u32x4, cat8(a0, b0)), to, 0);
        st128(rs, __builtin_bit_cast(u32x4, cat8(a1, b1)), to, 1024);
    }
    SCHED();
    const u64 tag = ((u64)(unsigned)__builtin_amdgcn_readfirstlane((int)(unsigned)ep) |
                     ((u64)(unsigned)__builtin_amdgcn_readfirstlane((int)(unsigned)(ep >> 32)) << 32)) + 1;
    TS(3);

    {
        const float dt = rbf(__builtin_amdgcn_exp2f(bf2f(p_ldt) * 1.44269504088896341f));
        const float lr = dt * bf2f(p_are), li = dt * bf2f(p_aim);
        const cf dB{dt * bf2f(p_br), dt * bf2f(p_bi)};
        const cf CBt = cmul(cf{bf2f(p_cr), bf2f(p_ci)}, dB);
        const cf CB{CBt[0], -CBt[1]};
        const float a2 = lr * 1.44269504088896341f, th = li * 0.15915494309189535f;
        LAS char* qs = lds + KG_BASE + (w >> 1) * KG_CH + (64 * (w & 1) + lane) * 4;
        LAS char* ps = qs + 21 * KG_RS;
        auto putq = [&](int r, cf q) { *(LAS unsigned*)(qs + r * KG_RS) = pk2v(q); };
        auto putp = [&](int r, cf p) { *(LAS unsigned*)(ps + r * KG_RS) = pk2v(p); };
        const cf z1 = zdir(a2, -th, 1.f), Z256 = zdir(a2, -th, 256.f), w8 = zdir(a2, th, 8.f);
        STAGE_W1(1, 0); SCHED();
        cf z2, z3, z4, z5, z6, z7, w16, q256, q512, q768, q1024, q1280, q1536, q1792, q2048, t0, t1;
        cmul2(z2, z1, z1, w16, w8, w8);
        cmul2(q256, CB, Z256, t0, CB, z1);
        putq(0, CB);
        cmul2(z3, z2, z1, z4, z2, z2);
        putq(1, t0);
        cmul2(q512, q256, Z256, t0, CB, z2);
        putq(8, q256);
        STAGE_W1(1, 1); SCHED();
        cmul2(z5, z4, z1, z6, z4, z2);
        putq(2, t0);
        cmul2(z7, z4, z3, t0, CB, z3);
        putq(12, q512);
        cmul2(q768, q512, Z256, t1, CB, z4);
        putq(3, t0);
        STAGE_W1(1, 2); SCHED();
        cmul2(t0, CB, z5, q1024, q768, Z256);
        putq(4, t1); putq(14, q768);
        cmul2(t1, CB, z6, q1280, q1024, Z256);
        putq(5, t0); putq(16, q1024);
        STAGE_W1(1, 3); SCHED();
        cmul2(t0, CB, z7, q1536, q1280, Z256);
        putq(6, t1); putq(17, q1280);
        cmul2(t1, q256, z2, q1792, q1536, Z256);
        putq(7, t0); putq(18, q1536);
        STAGE_W1(1, 4); SCHED();
        cmul2(t0, q256, z4, q2048, q1792, Z256);
        putq(9, t1); putq(19, q1792);
        cmul2(t1, q256, z6, z5, q512, z4);
        putq(10, t0); putq(20, q2048);
        STAGE_W1(1, 5); SCHED();
        cf pa{1.f, 0.f}, pb = w8, na, nb;
        cmul2(t0, q768, z4, na, pa, w16);
        putq(11, t1); putq(13, z5);
        cmul2(nb, pb, w16, t1, pb, w16);
        putq(15, t0);
        STAGE_W1(1, 6); SCHED();
        TS(4);
        sfor<0, 16>([&](auto J) {
            constexpr int jj = J.v;
            putp(2 * jj, pa); putp(2 * jj + 1, pb);
            pa = na; pb = nb;
            if constexpr (jj < 14) cmul2(na, pa, w16, nb, pb, w16);
            if constexpr (jj == 3) {
                SCHED();
                WAITV(7);
                BAR();
                if (tid == 0) { st_flag(ws, WS_FX + tb * 128, tag); st_flag(ws, WS_FXP + tb * 8, tag); }
                SCHED();
            }
            if constexpr (jj >= 4 && jj <= 10) { STAGE_W1(2, jj - 4); SCHED(); }
        });
        if (tid < 2) *(LAS unsigned short*)(lds + L_KC + tid * 1312 + 649 * 2) = 0;
        LBAR();
    }
    TS(5);
    {
        LAS char* qa = lds + KG_BASE + (w >> 1) * KG_CH + l15 * KG_RS + g16 * 16;
        LAS char* pa_ = qa + (21 + 16 * (w & 1)) * KG_RS;
        bf16x8 fa[8], fb[8], fc[8];
#pragma unroll
        for (int s = 0; s < 8; ++s) { fb[s] = *(LAS bf16x8*)(pa_ + s * 64); fa[s] = *(LAS bf16x8*)(qa + s * 64); fc[s] = *(LAS bf16x8*)(qa + 16 * KG_RS + s * 64); }
        SCHED();
        f32x4 k0 = {}, k1 = {};
#pragma unroll
        for (int s = 0; s < 8; ++s) {
            k0 = __builtin_amdgcn_mfma_f32_16x16x32_bf16(fa[s], fb[s], k0, 0, 0, 0);
            k1 = __builtin_amdgcn_mfma_f32_16x16x32_bf16(fc[s], fb[s], k1, 0, 0, 0);
        }
#pragma unroll
        for (int r = 0; r < 4; r += 2) {
            const unsigned a0 = pk2(k0[r], k0[r + 1]), a1 = pk2(k1[r], k1[r + 1]);
            *(LAS unsigned short*)(size_t)ksc[r] = (unsigned short)a0;
            *(LAS unsigned short*)(size_t)ksc[r + 1] = (unsigned short)(a0 >> 16);
            *(LAS unsigned short*)(size_t)ksc[4 + r] = (unsigned short)a1;
            *(LAS unsigned short*)(size_t)ksc[5 + r] = (unsigned short)(a1 >> 16);
        }
    }
    SCHED();
    TS(6);
    wait_flags<1>(ws, WS_FX + (16 * mt + 4 * w) * 128, 4, tag, lane);
    TS(7);
    bf16x8 xf[32];
    const unsigned xvo = WS_XN + (32 * w + (lane & 31)) * 32 + (lane >> 5) * 16, xso = mt * 32 * 4096;
    sfor<0, 8>([&](auto U) { abload16sc1<U.v * 4096>(xf[U.v], rs, xvo, xso); });
    SCHED();
    LBAR();
    TS(8);
    {
        unsigned val[2][9];
#pragma unroll
        for (int ch = 0; ch < 2; ++ch)
#pragma unroll
            for (int e = 0; e < 9; ++e) val[ch][e] = *(LAS unsigned short*)(size_t)(kga[e] + ch * 1312);
        SCHED();
#pragma unroll
        for (int ch = 0; ch < 2; ++ch) {
            u32x4 E, O;
#pragma unroll
            for (int m = 0; m < 4; ++m) { E[m] = val[ch][2 * m] | (val[ch][2 * m + 1] << 16); O[m] = val[ch][2 * m + 1] | (val[ch][2 * m + 2] << 16); }
            *(LAS u32x4*)(lds + L_KIMG + ch * CV_KCH + tid * 16) = E;
            *(LAS u32x4*)(lds + L_KIMG + ch * CV_KCH + 4160 + tid * 16) = O;
        }
        if (tid < 16) *(LAS u32x4*)(lds + L_KIMG + (tid >> 3) * CV_KCH + ((tid >> 2) & 1) * 4160 + 4096 + (tid & 3) * 16) = u32x4{0, 0, 0, 0};
    }
    SCHED();
    TS(9);

    const int phi = ((pp & 1) << 1) | (pp >> 1);
    const int th2 = (j >> 3) & 1, d0 = (j & 7) * 64;
    const unsigned w2voff = (lane >> 3) * 1024 + (((lane & 7) ^ (4 * ((lane >> 4) & 1))) * 16);
    const unsigned w2s = 7 * w * 8 * 1024 + d0 * 2, w2l = lbase + 7 * w * 1024;
    f32x16 acc[7];
    u32x4 uu0, uu1;
    {
        LAS char* ab = lds + (g16 & 1) * G1_UP + (8 * (g16 >> 1) + q4) * 224 + phi * 8;
        bf16x8 cur[7], nxt[7];
        u32x4 fx[2];
        WAITV(8);
        BAR();
#pragma unroll
        for (int p = 0; p < 7; ++p) cur[p] = cat8(tr_read(ab + p * 32), tr_read(ab + 4 * 224 + p * 32));
        SCHED();
        TS(10);
        sfor<0, 32>([&](auto ST) {
            constexpr int st = ST.v, s = st >> 2, kk = st & 3;
            if constexpr (kk == 0 && s > 0) {
                if constexpr (s >= 2 && s <= 6) waitv<G1S.younger(1, s + 1, st)>();
                BAR();
                SCHED();
            }
            if constexpr (st == 23) {
                asm volatile("s_waitcnt vmcnt(%2)" : "+v"(fx[0]), "+v"(fx[1]) : "i"(G1S.younger(3, 1, st)) : "memory");
                const u64 f0 = ((u64)fx[0][1] << 32) | fx[0][0], f1 = ((u64)fx[0][3] << 32) | fx[0][2], f2 = ((u64)fx[1][1] << 32) | fx[1][0], f3 = ((u64)fx[1][3] << 32) | fx[1][2];
                if (!__all(f0 == tag && f1 == tag && f2 == tag && f3 == tag)) wait_flags<4>(ws, WS_FX, 64, tag, lane);
                SCHED();
            }
            waitv<G1S.younger(0, st, st)>();
            SCHED();
            constexpr int n = st + 1, ns = n >> 2, nk = n & 3;
            LAS char* np = ab + (ns % 5) * G1_SLOT + nk * 16 * 224;
            sfor<0, 7>([&](auto P) {
                constexpr int p = P.v;
                mfma32_va<st == 0>(acc[p], cur[p], xf[st]);
                if constexpr (n < 32) nxt[p] = cat8(tr_read(np + p * 32), tr_read(np + 4 * 224 + p * 32));
                sfor<0, G1S.n>([&](auto K) {
                    constexpr G1Op o = G1S.op[K.v];
                    if constexpr (o.step == st && o.gap == p) {
                        if constexpr (o.kind == 0) abload16sc1<o.a * 4096>(xf[o.a], rs, xvo, xso);
                        else if constexpr (o.kind == 1) STAGE_W1(o.a, o.b);
                        else if constexpr (o.kind == 2) bdma16<(448 * o.a + 8 * o.b) * 1024, ((o.a + 3) % 5) * G1_SLOT + o.b * 1024>(rwd, w2voff, w2s, w2l);
                        else if constexpr (o.kind == 3) abload16sc1_v<WS_FXP + o.a * 16>(fx[o.a], rs, lane * 32, 0);
                        else if constexpr (o.a == 0) abload16sc1_v<0>(uu0, rs, WS_XT + (tid * 512 + c0) * 16, 0);
                        else abload16sc1_v<16>(uu1, rs, WS_XT + (tid * 512 + c0) * 16, 0);
                    }
                });
                SCHED();
            });
#pragma unroll
            for (int p = 0; p < 7; ++p) cur[p] = nxt[p];
        });
    }
    MFMA_DRAIN();
    TS(11);
    const int w2slot = (w + 3) % 5;
    LAS char* wl = lds + w2slot * G1_SLOT;
    const int rrow = 8 * (g16 >> 1) + q4, rsw = 4 * ((q4 >> 1) & 1);
    LAS char* wr0 = wl + rrow * 128 + (((2 * (g16 & 1) + (phi >> 1)) ^ rsw) * 16) + 8 * (phi & 1);
    LAS char* wr1 = wl + rrow * 128 + (((4 + 2 * (g16 & 1) + (phi >> 1)) ^ rsw) * 16) + 8 * (phi & 1);
    bf16x8 wf[28][2];
    asm volatile("s_waitcnt vmcnt(0)" : "+v"(uu0), "+v"(uu1)::"memory");
    BAR();
#pragma unroll
    for (int s = 0; s < 14; ++s) {
        wf[s][0] = cat8(tr_read(wr0 + s * 2048), tr_read(wr0 + s * 2048 + 512));
        wf[s][1] = cat8(tr_read(wr1 + s * 2048), tr_read(wr1 + s * 2048 + 512));
        asm volatile("" : "+a"(wf[s][0]), "+a"(wf[s][1]));
    }
    if (tid < 64) *(LAS u32x4*)(lds + L_U + (tid >> 5) * CV_UCH + (tid & 31) * 16) = u32x4{0, 0, 0, 0};
    *(LAS u32x4*)(lds + L_U + 512 + tid * 16) = uu0;
    *(LAS u32x4*)(lds + L_U + CV_UCH + 512 + tid * 16) = uu1;
    WAITL(0);
    SCHED();
    TS(12);
    const unsigned w2s2 = (448 * w + 224) * 1024 + d0 * 2, w2l2 = lbase + w2slot * G1_SLOT;
#define STAGE_W2B(I) bdma16<8 * (I) * 1024, (I) * 1024>(rwd, w2voff, w2s2, w2l2)
    {
        const unsigned hvo = WS_H + (32 * w + (lane & 31)) * 32 + (lane >> 5) * 16, hso = (mt * 112 + chunk * 7) * 4096;
        sfor<0, 7>([&](auto P) {
            constexpr int p = P.v;
            u32x4 o;
            sfor<0, 2>([&](auto R) {
                constexpr int r = 2 * R.v;
                const f32x2 g0 = {acc[p][2 * r], acc[p][2 * r + 1]}, u0 = {acc[p][8 + 2 * r], acc[p][9 + 2 * r]};
                const f32x2 g1 = {acc[p][2 * r + 2], acc[p][2 * r + 3]}, u1 = {acc[p][10 + 2 * r], acc[p][11 + 2 * r]};
                o[r] = pk2v(silu2(g0) * u0);
                o[r + 1] = pk2v(silu2(g1) * u1);
                SCHED();
                if constexpr (2 * p + R.v < 16 && p < 7) STAGE_W2B(2 * p + R.v < 14 ? 2 * p + R.v : 0);
                SCHED();
            });
            st128(rs, o, hvo, hso + p * 4096);
            SCHED();
        });
    }
    BAR();
    TS(13);

    const int chl = w >> 1, rh = w & 1;
    constexpr int PD = 8;
    bf16x8 hf[PD][2];
    const unsigned hvoff = WS_H + (64 * th2 + (lane & 31)) * 32 + (lane >> 5) * 16;
    const unsigned hsoff = (mt2 * 112 + 28 * w) * 4096;
    f32x4 cacc[8];
    {
        LAS char* ub = lds + L_U + chl * CV_UCH + 2 * (256 + 16 * (l15 - (g16 >> 1)) + 8 * (g16 & 1)) - 32 * (8 * rh + 6);
        const int i0 = 2047 - 16 * (g16 >> 1) - l15 + 8 * (g16 & 1);
        LAS char* kb = lds + L_KIMG + chl * CV_KCH + ((l15 & 1) ? 0 : 4160) + 4 * (i0 >> 1) - 32 * (8 * rh + 6);
        LAS char* kbm[8];
#pragma unroll
        for (int m = 0; m < 8; ++m) { kbm[m] = kb - 512 * m; asm volatile("" : "+v"(kbm[m])); }
        auto ldG = [&](int m, int rr) {
            const LAS unsigned* p = (const LAS unsigned*)(kbm[m] + 64 * (3 - rr));
            u32x4 t; t[0] = p[0]; t[1] = p[1]; t[2] = p[2]; t[3] = p[3];
            return __builtin_bit_cast(bf16x8, t);
        };
        auto ldF = [&](int m, int rr) { return *(const LAS bf16x8*)(ub + 64 * (3 - rr) + 512 * m); };
        bf16x8 G[2][8], Fr[2][8];
#pragma unroll
        for (int m = 0; m < 8; ++m) { G[0][m] = ldG(m, 0); Fr[0][m] = ldF(m, 0); }
        SCHED();
        u64 fh;
        sfor<0, 4>([&](auto RR) {
            constexpr int rr = RR.v, c = rr & 1, n = c ^ 1;
            if constexpr (rr == 1) {
                WAITV(7);
                BAR();
                if (tid == 0) st_flag(ws, WS_FH + (mt * 16 + chunk) * 128, tag);
                SCHED();
            }
            if constexpr (rr == 3) {
                asm volatile("s_waitcnt vmcnt(0)" : "+v"(fh)::"memory");
                if (!__all(fh == tag)) wait_flags<1>(ws, WS_FH + (mt2 * 16 + 4 * w) * 128, 4, tag, lane);
                SCHED();
            }
            sfor<0, 36>([&](auto C) {
                constexpr int cnt = C.v;
                constexpr int m = cnt < 8 ? 0 : cnt < 15 ? 1 : cnt < 21 ? 2 : cnt < 26 ? 3 : cnt < 30 ? 4 : cnt < 33 ? 5 : cnt < 35 ? 6 : 7;
                constexpr int first = m * 8 - (m * (m - 1)) / 2, i = m + (cnt - first);
                mfma16_vv<rr == 0 && m == 0>(cacc[i], G[c][m], Fr[c][i - m]);
                if constexpr (rr < 3) {
                    if constexpr (cnt < 8) Fr[n][cnt] = ldF(cnt, rr + 1);
                    else if constexpr (cnt < 24 && !(cnt & 1)) G[n][(cnt - 8) >> 1] = ldG((cnt - 8) >> 1, rr + 1);
                }
                if constexpr (rr < 2 && cnt % 5 == 2 && cnt / 5 < 7) STAGE_W2B(14 + 7 * rr + cnt / 5);
                if constexpr (rr == 2 && cnt == 24) aload8sc1(fh, ws + WS_FH + (mt2 * 16 + 4 * w) * 128, (lane & 3) * 128);
                if constexpr (rr == 3 && cnt % 4 == 1 && cnt / 4 < 8) hf[cnt / 8][(cnt / 4) & 1] = asbf(ld128(rs, hvoff + ((cnt / 4) & 1) * 1024, hsoff + (cnt / 8) * 4096));
                SCHED();
            });
        });
    }
    MFMA_DRAIN();
    TS(14);
    {
        LAS char* red = lds + L_RED;
#pragma unroll
        for (int i = 0; i < 4; ++i) {
            f32x4 lo = cacc[i], hi = cacc[4 + i];
            asm volatile("" : "+v"(lo), "+v"(hi));
            *(LAS f32x4*)(red + (w * 4 + i) * 1024 + lane * 16) = rh ? lo : hi;
            cacc[i] = rh ? hi : lo;
            hf[4 + i][0] = asbf(ld128(rs, hvoff, hsoff + (4 + i) * 4096));
            SCHED();
        }
#pragma unroll
        for (int s = 0; s < 14; ++s) {
            wf[14 + s][0] = cat8(tr_read(wr0 + s * 2048), tr_read(wr0 + s * 2048 + 512));
            wf[14 + s][1] = cat8(tr_read(wr1 + s * 2048), tr_read(wr1 + s * 2048 + 512));
            asm volatile("" : "+a"(wf[14 + s][0]), "+a"(wf[14 + s][1]));
            if (s % 3 == 1 && s / 3 < 4) { hf[4 + s / 3][1] = asbf(ld128(rs, hvoff + 1024, hsoff + (4 + s / 3) * 4096)); SCHED(); }
        }
        LBAR();
        const float dsk = bf2f(__builtin_amdgcn_readfirstlane((int)p_dsk));
#pragma unroll
        for (int i = 0; i < 4; ++i) {
            const f32x4 y = cacc[i] + *(LAS f32x4*)(red + ((w ^ 1) * 4 + i) * 1024 + lane * 16);
            const int t = 256 * (4 * rh + i) + 16 * l15 + 4 * g16;
            const u32x2 uu = *(LAS u32x2*)(lds + L_U + chl * CV_UCH + 2 * (256 + t));
            st64(rs, u32x2{pk2v(f32x2{y[0], y[1]} + unpk(uu[0]) * dsk), pk2v(f32x2{y[2], y[3]} + unpk(uu[1]) * dsk)}, WS_ST + ((c0 + chl) * L_ + t) * 2, 0);
        }
    }
    SCHED();
    TS(15);

    f32x16 a2[2][2];
    u32x4 sv[2], xv[2];
    u64 fs;
    const int t0 = mt2 * 128 + th2 * 64;
    const int tt2 = w >> 1, dh2 = w & 1;
    sfor<0, 28>([&](auto S) {
        constexpr int s = S.v;
        const bf16x8 h0 = hf[s % PD][0], h1 = hf[s % PD][1];
        mfma32_av<s == 0>(a2[0][0], wf[s][0], h0);
        mfma32_av<s == 0>(a2[1][0], wf[s][1], h0);
        if constexpr (s + PD < 28) hf[s % PD][0] = asbf(ld128(rs, hvoff, hsoff + (s + PD) * 4096));
        SCHED();
        mfma32_av<s == 0>(a2[0][1], wf[s][0], h1);
        mfma32_av<s == 0>(a2[1][1], wf[s][1], h1);
        if constexpr (s + PD < 28) hf[s % PD][1] = asbf(ld128(rs, hvoff + 1024, hsoff + (s + PD) * 4096));
        if constexpr (s == 10) {
            WAITV(22);
            LBAR();
            if (tid == 0) st_flag(ws, WS_FS + tb * 8, tag);
        }
        if constexpr (s == 19) aload8sc1(fs, ws + WS_FS + (d0 >> 1) * 8, (lane & 31) * 8);
        if constexpr (s == 23) {
            asm volatile("s_waitcnt vmcnt(0)" : "+v"(fs)::"memory");
            if (!__all(fs == tag)) wait_flags<1, 8>(ws, WS_FS + (d0 >> 1) * 8, 32, tag, lane);
#pragma unroll
            for (int i = 0; i < 2; ++i) {
                const int e = tid + 256 * i;
                sv[i] = ld128(rs, WS_ST + ((d0 + (e >> 3)) * L_ + t0) * 2 + (e & 7) * 16, 0);
            }
#pragma unroll
            for (int g = 0; g < 2; ++g) xv[g] = *(const u32x4*)(x + (size_t)(t0 + 32 * tt2 + (lane & 31)) * D_ + d0 + 32 * dh2 + 16 * g + 8 * (lane >> 5));
        }
        SCHED();
    });
    MFMA_DRAIN();
    TS(16);
    {
        sfor<0, 4>([&](auto Q) {
            constexpr int q = Q.v, tt = q >> 1, dh = q & 1;
            if (q != w) {
                const f32x16 a = a2[dh][tt];
#pragma unroll
                for (int g = 0; g < 4; ++g)
                    *(LAS f32x4*)(lds + ((q * 4 + w) * 4 + g) * 1024 + lane * 16) = f32x4{a[4 * g], a[4 * g + 1], a[4 * g + 2], a[4 * g + 3]};
            }
        });
#pragma unroll
        for (int i = 0; i < 2; ++i) *(LAS u32x4*)(lds + L_ST + (tid + 256 * i) * 16) = sv[i];
        LBAR();
    }
    TS(17);
    {
        f32x16 own;
        { f32x16 q0 = a2[0][0], q1 = a2[1][0], q2 = a2[0][1], q3 = a2[1][1];
          asm volatile("" : "+v"(q0), "+v"(q1), "+v"(q2), "+v"(q3));
          own = w == 0 ? q0 : w == 1 ? q1 : w == 2 ? q2 : q3; }
        f32x4 f[4];
#pragma unroll
        for (int g = 0; g < 4; ++g) {
            f[g] = f32x4{own[4 * g], own[4 * g + 1], own[4 * g + 2], own[4 * g + 3]};
#pragma unroll
            for (int o = 1; o < 4; ++o) f[g] += *(LAS f32x4*)(lds + ((w * 4 + ((w + o) & 3)) * 4 + g) * 1024 + lane * 16);
        }
        LAS char* sp = lds + L_ST + (32 * dh2 + 8 * (lane >> 5) + q4) * 128 + (32 * tt2 + 16 * (g16 & 1) + 4 * pp) * 2;
#pragma unroll
        for (int g2 = 0; g2 < 2; ++g2) {
            const s16x4 sa = tr_read(sp + g2 * 16 * 128), sb = tr_read(sp + (g2 * 16 + 4) * 128);
            const u32x2 s01 = __builtin_bit_cast(u32x2, sa), s23 = __builtin_bit_cast(u32x2, sb);
            u32x4 o;
            o[0] = pk2v(unpk(xv[g2][0]) + unpk(s01[0]) * silu2(f32x2{f[2 * g2][0], f[2 * g2][1]}));
            o[1] = pk2v(unpk(xv[g2][1]) + unpk(s01[1]) * silu2(f32x2{f[2 * g2][2], f[2 * g2][3]}));
            o[2] = pk2v(unpk(xv[g2][2]) + unpk(s23[0]) * silu2(f32x2{f[2 * g2 + 1][0], f[2 * g2 + 1][1]}));
            o[3] = pk2v(unpk(xv[g2][3]) + unpk(s23[1]) * silu2(f32x2{f[2 * g2 + 1][2], f[2 * g2 + 1][3]}));
            *(u32x4*)(out + (size_t)(t0 + 32 * tt2 + (lane & 31)) * D_ + d0 + 32 * dh2 + 16 * g2 + 8 * (lane >> 5)) = o;
        }
    }
    if (b == 0 && tid == 0) st_flag(ws, WS_EP, tag);
    TS(18);
    TSR(31);
    TS_DUMP();
    asm volatile(".Lfused_pf_end:" ::: "memory");
}

extern "C" void kernel_launch(void* const* d_in, const int* in_sizes, int n_in, void* d_out, int out_size, void* d_ws, size_t ws_size,
                              hipStream_t stream) {
    (void)hipFuncSetAttribute((const void*)fused_s4_ffn, hipFuncAttributeMaxDynamicSharedMemorySize, LDS_TOTAL);
    fused_s4_ffn<<<256, 256, LDS_TOTAL, stream>>>((const bf16_t*)d_in[0], (const bf16_t*)d_in[1], (const bf16_t*)d_in[2], (const bf16_t*)d_in[3],
                                                  (const bf16_t*)d_in[4], (const bf16_t*)d_in[5], (const bf16_t*)d_in[6], (const bf16_t*)d_in[7],
                                                  (const bf16_t*)d_in[8], (const bf16_t*)d_in[10], (const bf16_t*)d_in[11], (const bf16_t*)d_in[12],
                                                  (bf16_t*)d_out, (char*)d_ws);
}
```

```cpp
#include <hip/hip_runtime.h>
#include <stdint.h>

typedef unsigned short bf16_t;
typedef short bf16x8 __attribute__((ext_vector_type(8)));
typedef short s16x4 __attribute__((ext_vector_type(4)));
typedef float f32x2 __attribute__((ext_vector_type(2)));
typedef float f32x4 __attribute__((ext_vector_type(4)));
typedef float f32x16 __attribute__((ext_vector_type(16)));
typedef unsigned u32x4 __attribute__((ext_vector_type(4)));
typedef unsigned u32x2 __attribute__((ext_vector_type(2)));
typedef __bf16 bf16v2 __attribute__((ext_vector_type(2)));
typedef unsigned long long u64;
#define LAS __attribute__((address_space(3)))
#define GAS __attribute__((address_space(1)))
#define DI __device__ __forceinline__

constexpr int L_ = 2048, D_ = 512, N_ = 128, F_ = 1792;

constexpr unsigned WS_XN = 0;
constexpr unsigned WS_XT = 2u << 20;
constexpr unsigned WS_ST = 4u << 20;
constexpr unsigned WS_H = 6u << 20;
constexpr unsigned WS_FX = 14u << 20;
constexpr unsigned WS_FH = WS_FX + 256 * 128;
constexpr unsigned WS_FS = WS_FH + 256 * 128;
constexpr unsigned WS_FXP = WS_FS + 256 * 128;
constexpr unsigned WS_EP = WS_FXP + 256 * 8;
constexpr unsigned WS_USED = 15u << 20;

constexpr int G1_SLOT = 28800, G1_UP = 14336 + 128;
constexpr int KG_BASE = 3 * G1_SLOT, KG_RS = 528, KG_CH = 53 * KG_RS;
constexpr int L_KC = 144128;
constexpr int L_KIMG = 146816;
constexpr int L_LNS = L_KIMG, LN_RS = 1056;
constexpr int L_PF = L_LNS + 8 * LN_RS;
constexpr int CV_KCH = 8320;
constexpr int L_U = 2 * G1_SLOT, CV_UCH = 4608;
constexpr int L_RED = L_U + 2 * CV_UCH;
constexpr int G2_PRS = 272, G2_PART = 64 * G2_PRS;
constexpr int L_ST = 4 * G2_PART;
constexpr int LDS_TOTAL = 163840;
static_assert(KG_BASE + 2 * KG_CH <= L_KC, "K-gen scratch");
static_assert(L_KIMG + 2 * CV_KCH <= LDS_TOTAL && L_RED + 16384 <= 3 * G1_SLOT && L_ST + 8192 <= 3 * G1_SLOT, "lds map");

DI float bf2f(unsigned u16) { return __uint_as_float(u16 << 16); }
DI float bflo(unsigned w) { return __uint_as_float(w << 16); }
DI float bfhi(unsigned w) { return __uint_as_float(w & 0xffff0000u); }
DI unsigned pk2(float lo, float hi) {
    f32x2 v = {lo, hi};
    bf16v2 b = __builtin_convertvector(v, bf16v2);
    return __builtin_bit_cast(unsigned, b);
}
DI float rbf(float x) { return bflo(pk2(x, x)); }

#define WAITV(n) asm volatile("s_waitcnt vmcnt(" #n ")" ::: "memory")
#define WAITL(n) asm volatile("s_waitcnt lgkmcnt(" #n ")" ::: "memory")
#define BAR() do { asm volatile("" ::: "memory"); __builtin_amdgcn_s_barrier(); asm volatile("" ::: "memory"); } while (0)
#define LBAR() do { WAITL(0); BAR(); } while (0)
#define SCHED() __builtin_amdgcn_sched_barrier(0)

#define TS(i) do {} while (0)
#define TSR(i) do {} while (0)
#define TS_DUMP() do {} while (0)

template <int CTRL, int RMASK> DI float dpp_add(float v) {
    return v + __builtin_bit_cast(float, __builtin_amdgcn_update_dpp(0, __builtin_bit_cast(int, v), CTRL, RMASK, 0xf, true));
}
DI float wave_sum(float v) {
    v = dpp_add<0x111, 0xf>(v); v = dpp_add<0x112, 0xf>(v); v = dpp_add<0x114, 0xf>(v); v = dpp_add<0x118, 0xf>(v);
    v = dpp_add<0x142, 0xa>(v); v = dpp_add<0x143, 0xc>(v);
    return __builtin_bit_cast(float, __builtin_amdgcn_readlane(__builtin_bit_cast(int, v), 63));
}
DI s16x4 tr_read(LAS char* p) { return __builtin_amdgcn_ds_read_tr16_b64_v4i16((LAS s16x4*)p); }
DI bf16x8 cat8(s16x4 a, s16x4 b) { return __builtin_shufflevector(a, b, 0, 1, 2, 3, 4, 5, 6, 7); }
DI void glds16(const void* sbase, unsigned voff, unsigned ldst) {
    asm volatile("s_mov_b32 m0, %2\n\ts_nop 0\n\tglobal_load_lds_dwordx4 %0, %1" ::"v"(voff), "s"(sbase), "s"(ldst) : "memory");
}
DI void glds4(const void* sbase, unsigned voff, unsigned ldst) {
    asm volatile("s_mov_b32 m0, %2\n\ts_nop 0\n\tglobal_load_lds_dword %0, %1" ::"v"(voff), "s"(sbase), "s"(ldst) : "memory");
}
typedef __amdgpu_buffer_rsrc_t rsrc_t;
constexpr int SC1 = 16;
DI u32x4 ld128(rsrc_t r, unsigned voff, unsigned soff) { return __builtin_amdgcn_raw_buffer_load_b128(r, voff, soff, SC1); }
DI void st128(rsrc_t r, u32x4 v, unsigned voff, unsigned soff) {
    asm volatile("buffer_store_dwordx4 %0, %1, %2, %3 offen sc1\n\ts_nop 1" ::"v"(v), "v"(voff), "s"(r), "s"(soff) : "memory");
}
DI void st64(rsrc_t r, u32x2 v, unsigned voff, unsigned soff) { __builtin_amdgcn_raw_buffer_store_b64(v, r, voff, soff, SC1); }
DI bf16x8 asbf(u32x4 v) { return __builtin_bit_cast(bf16x8, v); }

template <int SIMM, int LIMM> DI void bdma16(rsrc_t r, unsigned voff, unsigned sbase, unsigned lbase) {
    unsigned t;
    asm volatile("s_add_u32 m0, %1, %2\n\ts_add_u32 %0, %3, %4\n\tbuffer_load_dwordx4 %5, %6, %0 offen lds"
                 : "=&s"(t) : "s"(lbase), "i"(LIMM), "s"(sbase), "i"(SIMM), "v"(voff), "s"(r) : "memory", "scc");
}
DI void warm4(unsigned& pf, const void* sbase, unsigned voff) { asm volatile("global_load_dword %0, %1, %2" : "+v"(pf) : "v"(voff), "s"(sbase) : "memory"); }
template <int SIMM> DI void bwarm4(unsigned& pf, rsrc_t r, unsigned voff) {
    unsigned t;
    asm volatile("s_mov_b32 %1, %4\n\tbuffer_load_dword %0, %2, %3, %1 offen" : "+v"(pf), "=&s"(t) : "v"(voff), "s"(r), "i"(SIMM) : "memory");
}
DI void aload16(u32x4& d, const void* sbase, unsigned voff) { asm volatile("global_load_dwordx4 %0, %1, %2" : "=&v"(d) : "v"(voff), "s"(sbase) : "memory"); }
DI void aload2(unsigned& d, const void* sbase, unsigned voff) { asm volatile("global_load_ushort %0, %1, %2" : "=&v"(d) : "v"(voff), "s"(sbase) : "memory"); }
DI void aload8sc1(u64& d, const void* sbase, unsigned voff) { asm volatile("global_load_dwordx2 %0, %1, %2 sc1" : "=&v"(d) : "v"(voff), "s"(sbase) : "memory"); }
template <int SIMM> DI void abload16sc1(bf16x8& d, rsrc_t r, unsigned voff, unsigned sbase) {
    unsigned t;
    asm volatile("s_add_u32 %1, %4, %5\n\tbuffer_load_dwordx4 %0, %2, %3, %1 offen sc1" : "=&a"(d), "=&s"(t) : "v"(voff), "s"(r), "s"(sbase), "i"(SIMM) : "memory", "scc");
}
template <bool FIRST> DI void mfma32_va(f32x16& c, bf16x8 a, bf16x8 b) {
    if constexpr (FIRST) asm volatile("v_mfma_f32_32x32x16_bf16 %0, %1, %2, 0" : "=&v"(c) : "v"(a), "a"(b));
    else asm volatile("v_mfma_f32_32x32x16_bf16 %0, %1, %2, %0" : "+v"(c) : "v"(a), "a"(b));
}
template <bool FIRST> DI void mfma32_av(f32x16& c, bf16x8 a, bf16x8 b) {
    if constexpr (FIRST) asm volatile("v_mfma_f32_32x32x16_bf16 %0, %1, %2, 0" : "=&v"(c) : "a"(a), "v"(b));
    else asm volatile("v_mfma_f32_32x32x16_bf16 %0, %1, %2, %0" : "+v"(c) : "a"(a), "v"(b));
}
template <bool FIRST> DI void mfma16_vv(f32x4& c, bf16x8 a, bf16x8 b) {
    if constexpr (FIRST) asm volatile("v_mfma_f32_16x16x32_bf16 %0, %1, %2, 0" : "=&v"(c) : "v"(a), "v"(b));
    else asm volatile("v_mfma_f32_16x16x32_bf16 %0, %1, %2, %0" : "+v"(c) : "v"(a), "v"(b));
}
#define MFMA_DRAIN() asm volatile("s_nop 15\n\ts_nop 3" ::: "memory")
template <int SIMM> DI void abload16sc1_v(u32x4& d, rsrc_t r, unsigned voff, unsigned sbase) {
    unsigned t;
    asm volatile("s_add_u32 %1, %4, %5\n\tbuffer_load_dwordx4 %0, %2, %3, %1 offen sc1" : "=&v"(d), "=&s"(t) : "v"(voff), "s"(r), "s"(sbase), "i"(SIMM) : "memory", "scc");
}
template <int IMM> DI void aload8sc1_o(u64& d, const void* sbase, unsigned voff) {
    asm volatile("global_load_dwordx2 %0, %1, %2 sc1" : "=&v"(d) : "v"(voff + IMM), "s"(sbase) : "memory");
}
template <int N> DI void waitv() { asm volatile("s_waitcnt vmcnt(%0)" ::"i"(N) : "memory"); }
template <int N> struct IC { static constexpr int v = N; };
template <int B, int E, class F> DI void sfor(F&& f) { if constexpr (B < E) { f(IC<B>{}); sfor<B + 1, E>(f); } }

struct G1Op { int step, gap, kind, a, b; };
struct G1Sched {
    G1Op op[128]; int n;
    constexpr G1Sched() : op{}, n(0) {
        for (int u = 0; u < 8; ++u) op[n++] = G1Op{-1, 0, 0, u, 0};
        for (int i = 0; i < 7; ++i) { op[n++] = G1Op{-1, 0, 1, 1, i}; op[n++] = G1Op{-1, 0, 1, 2, i}; }
        op[n++] = G1Op{-1, 0, 6, 0, 0};
        for (int s = 0; s < 8; ++s) {
            G1Op list[20] = {}; int m = 0;
            if (s < 3) for (int i = 0; i < 8; ++i) { list[m++] = G1Op{0, 0, 0, 8 + 8 * s + i, 0}; if (i < 7) list[m++] = G1Op{0, 0, 1, 3 + s, i}; }
            if (s == 3 || s == 4) for (int i = 0; i < 7; ++i) list[m++] = G1Op{0, 0, 1, 3 + s, i};
            if (s < 4) for (int i = 0; i < 2; ++i) list[m++] = G1Op{0, 0, 5, 2 * s + i, 0};
            if (s == 4) for (int i = 0; i < 2; ++i) list[m++] = G1Op{0, 0, 3, i, 0};
            if (s == 6) for (int i = 0; i < 2; ++i) list[m++] = G1Op{0, 0, 4, i, 0};
            if (s >= 4) for (int i = 0; i < 7; ++i) list[m++] = G1Op{0, 0, 2, s - 4, i};
            const int span = s == 7 ? 14 : 28;
            for (int i = 0; i < m; ++i) { const int g = (i * span) / m; list[i].step = 4 * s + g / 7; list[i].gap = g % 7; op[n++] = list[i]; }
        }
    }
    constexpr int younger(int kind, int a, int st) const {
        int last = -1;
        for (int i = 0; i < n; ++i) if (op[i].kind == kind && op[i].a == a) last = i;
        int c = 0;
        for (int i = last + 1; i < n; ++i) if (op[i].step < st) ++c;
        return c;
    }
};
constexpr G1Sched G1S{};

DI u64 ld_flag(const char* ws, unsigned off) { return __hip_atomic_load((GAS u64*)(ws + off), __ATOMIC_RELAXED, __HIP_MEMORY_SCOPE_AGENT); }
DI void st_flag(char* ws, unsigned off, u64 v) { __hip_atomic_store((GAS u64*)(ws + off), v, __ATOMIC_RELAXED, __HIP_MEMORY_SCOPE_AGENT); }
template <int NPL, int STRIDE = 128>
DI void wait_flags(const char* ws, unsigned base, int nl, u64 tag, int lane) {
    const unsigned off = base + (lane < nl ? lane : 0) * STRIDE;
#pragma unroll 1
    for (int spin = 0; spin < 40000; ++spin) {
        bool ok = true;
#pragma unroll
        for (int i = 0; i < NPL; ++i) ok &= ld_flag(ws, off + i * 64 * STRIDE) == tag;
        if (__all(ok)) break;
        __builtin_amdgcn_s_sleep(1);
    }
    asm volatile("" ::: "memory");
}

typedef f32x2 cf;
DI cf cmul(cf a, cf b) {
    cf t, z;
    asm("v_pk_mul_f32 %0, %1, %2 op_sel_hi:[0,1]" : "=v"(t) : "v"(a), "v"(b));
    asm("v_pk_fma_f32 %0, %1, %2, %3 op_sel:[1,1,0] op_sel_hi:[1,0,1] neg_lo:[0,1,0]" : "=v"(z) : "v"(a), "v"(b), "v"(t));
    return z;
}
DI void cmul2(cf& z0, cf a0, cf b0, cf& z1, cf a1, cf b1) {
    cf t0, t1;
    asm("v_pk_mul_f32 %2, %4, %5 op_sel_hi:[0,1]\n\tv_pk_mul_f32 %3, %6, %7 op_sel_hi:[0,1]\n\t"
        "v_pk_fma_f32 %0, %4, %5, %2 op_sel:[1,1,0] op_sel_hi:[1,0,1] neg_lo:[0,1,0]\n\t"
        "v_pk_fma_f32 %1, %6, %7, %3 op_sel:[1,1,0] op_sel_hi:[1,0,1] neg_lo:[0,1,0]"
        : "=&v"(z0), "=&v"(z1), "=&v"(t0), "=&v"(t1) : "v"(a0), "v"(b0), "v"(a1), "v"(b1));
}
DI cf zdir(float a2, float th, float l) {
    const float m = __builtin_amdgcn_exp2f(a2 * l);
    const float r = __builtin_amdgcn_fractf(th * l);
    return cf{m * __builtin_amdgcn_cosf(r), m * __builtin_amdgcn_sinf(r)};
}
DI f32x2 silu2(f32x2 g) {
    const f32x2 t = g * -1.44269504088896341f;
    const f32x2 d = f32x2{__builtin_amdgcn_exp2f(t[0]), __builtin_amdgcn_exp2f(t[1])} + 1.0f;
    return g * f32x2{__builtin_amdgcn_rcpf(d[0]), __builtin_amdgcn_rcpf(d[1])};
}
DI unsigned pk2v(f32x2 v) { return __builtin_bit_cast(unsigned, __builtin_convertvector(v, bf16v2)); }
DI f32x2 unpk(unsigned w) { return f32x2{bflo(w), bfhi(w)}; }
DI float silu_f(float g) { return g * __builtin_amdgcn_rcpf(1.0f + __builtin_amdgcn_exp2f(g * -1.44269504088896341f)); }

extern __shared__ __attribute__((aligned(16))) char dyn_smem[];

__global__ __launch_bounds__(256, 1) void fused_s4_ffn(const bf16_t* __restrict__ x, const bf16_t* __restrict__ A_re, const bf16_t* __restrict__ A_im,
                                                       const bf16_t* __restrict__ B_re, const bf16_t* __restrict__ B_im, const bf16_t* __restrict__ C_re,
                                                       const bf16_t* __restrict__ C_im, const bf16_t* __restrict__ log_dt, const bf16_t* __restrict__ Dskip,
                                                       const bf16_t* __restrict__ wg, const bf16_t* __restrict__ wu, const bf16_t* __restrict__ wd,
                                                       bf16_t* __restrict__ out, char* ws) {
    LAS char* lds = (LAS char*)dyn_smem;
    const unsigned lbase = (unsigned)(size_t)lds;
    const int tid = threadIdx.x, w = __builtin_amdgcn_readfirstlane(tid >> 6), lane = tid & 63;
    const int b = blockIdx.x, xcd = b & 7, j = b >> 3;
    const int tb = xcd * 32 + j;
    const int mt = 8 * (xcd >> 2) + (j >> 2), chunk = 4 * (xcd & 3) + (j & 3);
    const int mt2 = 2 * xcd + (j >> 4);
    const int c0 = 2 * tb;
    TSR(30); TS(0);

    u32x4 raw[2];
#pragma unroll
    for (int r = 0; r < 2; ++r) aload16(raw[r], x + (size_t)(8 * tb + 2 * w + r) * D_, lane * 16);
    unsigned pf = 0;
    {
        u64 pc; unsigned len;
        asm volatile("s_getpc_b64 %0\n.Lfused_pf_here:\n\ts_mov_b32 %1, .Lfused_pf_end-.Lfused_pf_here" : "=s"(pc), "=s"(len));
        const unsigned o = (unsigned)(12 * j + (lane < 12 ? lane : 11)) * 128u;
        warm4(pf, (const void*)pc, o < len ? o : len);
    }
    u64 ep;
    aload8sc1(ep, ws + WS_EP, 0);
    const int kch = c0 + (w >> 1);
    const unsigned kpo = (64 * (w & 1) + lane) * 2;
    unsigned p_are, p_aim, p_br, p_bi, p_cr, p_ci, p_ldt, p_dsk;
    aload2(p_are, A_re + kch * N_, kpo); aload2(p_aim, A_im + kch * N_, kpo); aload2(p_br, B_re + kch * N_, kpo);
    aload2(p_bi, B_im + kch * N_, kpo); aload2(p_cr, C_re + kch * N_, kpo); aload2(p_ci, C_im + kch * N_, kpo);
    aload2(p_ldt, log_dt + kch, 0); aload2(p_dsk, Dskip + kch, 0);
    SCHED();
    const rsrc_t rs = __builtin_amdgcn_make_buffer_rsrc(ws, 0, WS_USED, 0x00020000);
    const rsrc_t rw1 = __builtin_amdgcn_make_buffer_rsrc((void*)((w & 1) ? wu : wg), 0, D_ * F_ * 2, 0x00020000);
    const rsrc_t rwd = __builtin_amdgcn_make_buffer_rsrc((void*)wd, 0, D_ * F_ * 2, 0x00020000);
    unsigned voff[7];
    {
        const int k = (lane * 147) >> 11, c = lane - 14 * k;
        unsigned v = k * (F_ * 2) + c * 16;
        int cc = c;
#pragma unroll
        for (int i = 0; i < 7; ++i) {
            voff[i] = v;
            cc += 8; v += 4 * (F_ * 2) + 128;
            if (cc >= 14) { cc -= 14; v += F_ * 2 - 224; }
        }
    }
    const unsigned w1s = (32 * (w >> 1)) * (F_ * 2) + chunk * 224;
    const unsigned w1l = lbase + (w & 1) * G1_UP + (w >> 1) * 7168;
#define STAGE_W1(S, I) bdma16<64 * (S) * (F_ * 2), ((S) % 5) * G1_SLOT + (I) * 1024>(rw1, voff[I], w1s, w1l)
    sfor<0, 7>([&](auto I) { STAGE_W1(0, I.v); });
    {
        const int r = 14 * (w >> 1) + (lane < 14 ? lane : 13), row = (r * 147) >> 10;
        warm4(pf, (w & 1) ? wu : wg, (64 + 4 * j + row) * (F_ * 2) + (xcd & 3) * 896 + (r - 7 * row) * 128);
    }
    const int wdt = 14 * w + (lane < 14 ? lane : 13);
    const unsigned wdpf = (448 * (j >> 3) + 28 * (j & 7) + (wdt >> 3)) * 1024 + (wdt & 7) * 128;
    SCHED();
    const int g16 = lane >> 4, q4 = (lane & 15) >> 2, pp = lane & 3, l15 = lane & 15;
    unsigned ksc[8];
    {
        const int col8 = 8 * (16 * (w & 1) + l15);
        const unsigned kcb = lbase + L_KC + (w >> 1) * 1312;
#pragma unroll
        for (int r = 0; r < 8; ++r) {
            int l1;
            if (r < 4) l1 = g16 == 0 ? r : g16 == 1 ? 4 + r : g16 == 2 ? 256 + 2 * r : 512 + 256 * (r >> 1) + 4 * (r & 1);
            else l1 = g16 == 0 ? 1024 + 256 * (r - 4) : 2048;
            const int v = l1 + col8;
            int idx = v < 128 ? v : (int)(__float_as_uint((float)v) >> 16) - 17024;
            if (r >= 4 && !(g16 == 0 || (g16 == 1 && r == 4 && col8 == 0))) idx = 648;
            ksc[r] = kcb + idx * 2;
            asm volatile("" : "+v"(ksc[r]));
        }
    }
    unsigned kga[9];
#pragma unroll
    for (int e = 0; e < 9; ++e) {
        const int l = 2047 - 8 * tid - e;
        int idx = l < 256 ? l : (int)(pk2((float)l, 0.f) & 0xffffu) - 17024;
        if (l < 0) idx = 649;
        kga[e] = lbase + L_KC + idx * 2;
        asm volatile("" : "+v"(kga[e]));
    }
    SCHED();
    asm volatile("s_waitcnt vmcnt(8)" : "+v"(ep), "+v"(raw[0]), "+v"(raw[1]), "+v"(p_are), "+v"(p_aim), "+v"(p_br), "+v"(p_bi), "+v"(p_cr), "+v"(p_ci),
                 "+v"(p_ldt), "+v"(p_dsk)::"memory");
    SCHED();
    TS(1);

    {
        f32x2 v[2][4];
        float s1[2], s2[2];
#pragma unroll
        for (int r = 0; r < 2; ++r) {
#pragma unroll
            for (int jj = 0; jj < 4; ++jj) v[r][jj] = unpk(raw[r][jj]);
            const f32x2 t = (v[r][0] + v[r][1]) + (v[r][2] + v[r][3]);
            s1[r] = t[0] + t[1];
        }
        s1[0] = wave_sum(s1[0]); s1[1] = wave_sum(s1[1]);
#pragma unroll
        for (int r = 0; r < 2; ++r) {
            const float mean = s1[r] * (1.0f / 512.0f);
            f32x2 q = {0.f, 0.f};
#pragma unroll
            for (int jj = 0; jj < 4; ++jj) { const f32x2 d = v[r][jj] - mean; q += d * d; }
            s2[r] = q[0] + q[1];
        }
        s2[0] = wave_sum(s2[0]); s2[1] = wave_sum(s2[1]);
#pragma unroll
        for (int r = 0; r < 2; ++r) {
            const float mb = rbf(s1[r] * (1.0f / 512.0f));
            const float sd = rbf(__builtin_amdgcn_sqrtf(rbf(rbf(s2[r] * (1.0f / 512.0f)) + 1e-5f)));
            const float rsd = __builtin_amdgcn_rcpf(sd);
            u32x4 o;
#pragma unroll
            for (int jj = 0; jj < 4; ++jj) o[jj] = pk2v(unpk(pk2v(v[r][jj] - mb)) * rsd);
            *(LAS u32x4*)(lds + L_LNS + (2 * w + r) * LN_RS + lane * 16) = o;
        }
    }
    LBAR();
    TS(2);
    {
        const int lmt = tb >> 4, tok0 = (tb & 15) * 8;
        const int half = tid & 1, tok = (tid >> 1) & 7, U = tid >> 4;
        const u32x4 va = *(LAS u32x4*)(lds + L_LNS + tok * LN_RS + U * 32 + half * 16);
        const u32x4 vb = *(LAS u32x4*)(lds + L_LNS + tok * LN_RS + U * 32 + half * 16 + 512);
        LAS char* tp = lds + L_LNS + q4 * LN_RS + (128 * w + 16 * g16 + 4 * pp) * 2;
        const s16x4 a0 = tr_read(tp), b0 = tr_read(tp + 4 * LN_RS), a1 = tr_read(tp + 128), b1 = tr_read(tp + 4 * LN_RS + 128);
        const unsigned xo = WS_XN + ((lmt * 32 + U) * 128 + tok0 + tok) * 32 + half * 16;
        st128(rs, va, xo, 0);
        st128(rs, vb, xo, 16 * 4096);
        const unsigned to = WS_XT + (tb * 512 + 128 * w + 16 * g16 + l15) * 16;
        st128(rs, __builtin_bit_cast(u32x4, cat8(a0, b0)), to, 0);
        st128(rs, __builtin_bit_cast(u32x4, cat8(a1, b1)), to, 1024);
    }
    SCHED();
    const u64 tag = ((u64)(unsigned)__builtin_amdgcn_readfirstlane((int)(unsigned)ep) |
                     ((u64)(unsigned)__builtin_amdgcn_readfirstlane((int)(unsigned)(ep >> 32)) << 32)) + 1;
    TS(3);

    {
        const float dt = rbf(__builtin_amdgcn_exp2f(bf2f(p_ldt) * 1.44269504088896341f));
        const float lr = dt * bf2f(p_are), li = dt * bf2f(p_aim);
        const cf dB{dt * bf2f(p_br), dt * bf2f(p_bi)};
        const cf CBt = cmul(cf{bf2f(p_cr), bf2f(p_ci)}, dB);
        const cf CB{CBt[0], -CBt[1]};
        const float a2 = lr * 1.44269504088896341f, th = li * 0.15915494309189535f;
        LAS char* qs = lds + KG_BASE + (w >> 1) * KG_CH + (64 * (w & 1) + lane) * 4;
        LAS char* ps = qs + 21 * KG_RS;
        auto putq = [&](int r, cf q) { *(LAS unsigned*)(qs + r * KG_RS) = pk2v(q); };
        auto putp = [&](int r, cf p) { *(LAS unsigned*)(ps + r * KG_RS) = pk2v(p); };
        const cf z1 = zdir(a2, -th, 1.f), Z256 = zdir(a2, -th, 256.f), w8 = zdir(a2, th, 8.f);
        cf z2, z3, z4, z5, z6, z7, w16, q256, q512, q768, q1024, q1280, q1536, q1792, q2048, t0, t1;
        cmul2(z2, z1, z1, w16, w8, w8);
        cmul2(q256, CB, Z256, t0, CB, z1);
        putq(0, CB);
        cmul2(z3, z2, z1, z4, z2, z2);
        putq(1, t0);
        cmul2(q512, q256, Z256, t0, CB, z2);
        putq(8, q256);
        cmul2(z5, z4, z1, z6, z4, z2);
        putq(2, t0);
        cmul2(z7, z4, z3, t0, CB, z3);
        putq(12, q512);
        cmul2(q768, q512, Z256, t1, CB, z4);
        putq(3, t0);
        cmul2(t0, CB, z5, q1024, q768, Z256);
        putq(4, t1); putq(14, q768);
        cmul2(t1, CB, z6, q1280, q1024, Z256);
        putq(5, t0); putq(16, q1024);
        cmul2(t0, CB, z7, q1536, q1280, Z256);
        putq(6, t1); putq(17, q1280);
        cmul2(t1, q256, z2, q1792, q1536, Z256);
        putq(7, t0); putq(18, q1536);
        cmul2(t0, q256, z4, q2048, q1792, Z256);
        putq(9, t1); putq(19, q1792);
        cmul2(t1, q256, z6, z5, q512, z4);
        putq(10, t0); putq(20, q2048);
        cf pa{1.f, 0.f}, pb = w8, na, nb;
        cmul2(t0, q768, z4, na, pa, w16);
        putq(11, t1); putq(13, z5);
        cmul2(nb, pb, w16, t1, pb, w16);
        putq(15, t0);
        TS(4);
        sfor<0, 16>([&](auto J) {
            constexpr int jj = J.v;
            putp(2 * jj, pa); putp(2 * jj + 1, pb);
            pa = na; pb = nb;
            if constexpr (jj < 14) cmul2(na, pa, w16, nb, pb, w16);
            if constexpr (jj == 5) {
                SCHED();
                WAITV(0);
                BAR();
                if (tid == 0) { st_flag(ws, WS_FX + tb * 128, tag); st_flag(ws, WS_FXP + tb * 8, tag); }
                SCHED();
            }
        });
        if (tid < 2) *(LAS unsigned short*)(lds + L_KC + tid * 1312 + 649 * 2) = 0;
        LBAR();
    }
    TS(5);
    bf16x8 xf[32];
    const unsigned xvo = WS_XN + (32 * w + (lane & 31)) * 32 + (lane >> 5) * 16, xso = mt * 32 * 4096;
    {
        LAS char* qa = lds + KG_BASE + (w >> 1) * KG_CH + l15 * KG_RS + g16 * 16;
        LAS char* pa_ = qa + (21 + 16 * (w & 1)) * KG_RS;
        bf16x8 fa[8], fb[8], fc[8];
#pragma unroll
        for (int s = 0; s < 8; ++s) { fb[s] = *(LAS bf16x8*)(pa_ + s * 64); fa[s] = *(LAS bf16x8*)(qa + s * 64); fc[s] = *(LAS bf16x8*)(qa + 16 * KG_RS + s * 64); }
        SCHED();
        TS(6);
        wait_flags<1>(ws, WS_FX + (16 * mt + 4 * w) * 128, 4, tag, lane);
        TS(7);
        sfor<0, 8>([&](auto U) { abload16sc1<U.v * 4096>(xf[U.v], rs, xvo, xso); });
        SCHED();
        f32x4 k0, k1;
        sfor<0, 8>([&](auto S) {
            mfma16_vv<S.v == 0>(k0, fa[S.v], fb[S.v]);
            if constexpr (S.v < 7) STAGE_W1(1, S.v);
            SCHED();
            mfma16_vv<S.v == 0>(k1, fc[S.v], fb[S.v]);
            if constexpr (S.v < 7) STAGE_W1(2, S.v);
            SCHED();
        });
        {
            const int r = 35 * (w >> 1) + (lane < 35 ? lane : 34), row = (r * 147) >> 10;
            warm4(pf, (w & 1) ? wu : wg, (192 + 10 * j + row) * (F_ * 2) + (xcd & 3) * 896 + (r - 7 * row) * 128);
        }
        asm volatile("s_nop 7" : "+v"(k0), "+v"(k1));
#pragma unroll
        for (int r = 0; r < 4; r += 2) {
            const unsigned a0 = pk2(k0[r], k0[r + 1]), a1 = pk2(k1[r], k1[r + 1]);
            *(LAS unsigned short*)(size_t)ksc[r] = (unsigned short)a0;
            *(LAS unsigned short*)(size_t)ksc[r + 1] = (unsigned short)(a0 >> 16);
            *(LAS unsigned short*)(size_t)ksc[4 + r] = (unsigned short)a1;
            *(LAS unsigned short*)(size_t)ksc[5 + r] = (unsigned short)(a1 >> 16);
        }
    }
    SCHED();
    LBAR();
    TS(8);
    {
        unsigned val[2][9];
#pragma unroll
        for (int ch = 0; ch < 2; ++ch)
#pragma unroll
            for (int e = 0; e < 9; ++e) val[ch][e] = *(LAS unsigned short*)(size_t)(kga[e] + ch * 1312);
        SCHED();
#pragma unroll
        for (int ch = 0; ch < 2; ++ch) {
            u32x4 E, O;
#pragma unroll
            for (int m = 0; m < 4; ++m) { E[m] = val[ch][2 * m] | (val[ch][2 * m + 1] << 16); O[m] = val[ch][2 * m + 1] | (val[ch][2 * m + 2] << 16); }
            *(LAS u32x4*)(lds + L_KIMG + ch * CV_KCH + tid * 16) = E;
            *(LAS u32x4*)(lds + L_KIMG + ch * CV_KCH + 4160 + tid * 16) = O;
        }
        if (tid < 16) *(LAS u32x4*)(lds + L_KIMG + (tid >> 3) * CV_KCH + ((tid >> 2) & 1) * 4160 + 4096 + (tid & 3) * 16) = u32x4{0, 0, 0, 0};
    }
    SCHED();
    TS(9);

    const int phi = ((pp & 1) << 1) | (pp >> 1);
    const int th2 = (j >> 3) & 1, d0 = (j & 7) * 64;
    const unsigned w2voff = (lane >> 3) * 1024 + (((lane & 7) ^ (4 * ((lane >> 4) & 1))) * 16);
    const unsigned w2s = 7 * w * 8 * 1024 + d0 * 2, w2l = lbase + 7 * w * 1024;
    f32x16 acc[7];
    u32x4 uu0, uu1;
    {
        LAS char* ab = lds + (g16 & 1) * G1_UP + (8 * (g16 >> 1) + q4) * 224 + phi * 8;
        bf16x8 cur[7], nxt[7];
        u32x4 fx[2];
        waitv<G1S.younger(1, 1, 0)>();
        BAR();
#pragma unroll
        for (int p = 0; p < 7; ++p) cur[p] = cat8(tr_read(ab + p * 32), tr_read(ab + 4 * 224 + p * 32));
        SCHED();
        TS(10);
        sfor<0, 32>([&](auto ST) {
            constexpr int st = ST.v, s = st >> 2, kk = st & 3;
            if constexpr (kk == 0 && s > 0) {
                if constexpr (s <= 6) waitv<G1S.younger(1, s + 1, st)>();
                BAR();
                SCHED();
            }
            if constexpr (st == 23) {
                asm volatile("s_waitcnt vmcnt(%2)" : "+v"(fx[0]), "+v"(fx[1]) : "i"(G1S.younger(3, 1, st)) : "memory");
                const u64 f0 = ((u64)fx[0][1] << 32) | fx[0][0], f1 = ((u64)fx[0][3] << 32) | fx[0][2], f2 = ((u64)fx[1][1] << 32) | fx[1][0], f3 = ((u64)fx[1][3] << 32) | fx[1][2];
                if (!__all(f0 == tag && f1 == tag && f2 == tag && f3 == tag)) wait_flags<4>(ws, WS_FX, 64, tag, lane);
                SCHED();
            }
            waitv<G1S.younger(0, st, st)>();
            SCHED();
            constexpr int n = st + 1, ns = n >> 2, nk = n & 3;
            LAS char* np = ab + (ns % 5) * G1_SLOT + nk * 16 * 224;
            sfor<0, 7>([&](auto P) {
                constexpr int p = P.v;
                mfma32_va<st == 0>(acc[p], cur[p], xf[st]);
                if constexpr (n < 32) nxt[p] = cat8(tr_read(np + p * 32), tr_read(np + 4 * 224 + p * 32));
                sfor<0, G1S.n>([&](auto K) {
                    constexpr G1Op o = G1S.op[K.v];
                    if constexpr (o.step == st && o.gap == p) {
                        if constexpr (o.kind == 0) abload16sc1<o.a * 4096>(xf[o.a], rs, xvo, xso);
                        else if constexpr (o.kind == 1) STAGE_W1(o.a, o.b);
                        else if constexpr (o.kind == 2) bdma16<(448 * o.a + 8 * o.b) * 1024, ((o.a + 3) % 5) * G1_SLOT + o.b * 1024>(rwd, w2voff, w2s, w2l);
                        else if constexpr (o.kind == 5) bwarm4<(o.a & 3) * 7168 + (o.a >> 2) * 224 * 1024>(pf, rwd, wdpf);
                        else if constexpr (o.kind == 3) abload16sc1_v<WS_FXP + o.a * 16>(fx[o.a], rs, lane * 32, 0);
                        else if constexpr (o.a == 0) abload16sc1_v<0>(uu0, rs, WS_XT + (tid * 512 + c0) * 16, 0);
                        else abload16sc1_v<16>(uu1, rs, WS_XT + (tid * 512 + c0) * 16, 0);
                    }
                });
                SCHED();
            });
#pragma unroll
            for (int p = 0; p < 7; ++p) cur[p] = nxt[p];
        });
    }
    MFMA_DRAIN();
    TS(11);
    const int w2slot = (w + 3) % 5;
    LAS char* wl = lds + w2slot * G1_SLOT;
    const int rrow = 8 * (g16 >> 1) + q4, rsw = 4 * ((q4 >> 1) & 1);
    LAS char* wr0 = wl + rrow * 128 + (((2 * (g16 & 1) + (phi >> 1)) ^ rsw) * 16) + 8 * (phi & 1);
    LAS char* wr1 = wl + rrow * 128 + (((4 + 2 * (g16 & 1) + (phi >> 1)) ^ rsw) * 16) + 8 * (phi & 1);
    bf16x8 wf[28][2];
    asm volatile("s_waitcnt vmcnt(0)" : "+v"(uu0), "+v"(uu1), "+v"(pf)::"memory");
    BAR();
#pragma unroll
    for (int s = 0; s < 14; ++s) {
        wf[s][0] = cat8(tr_read(wr0 + s * 2048), tr_read(wr0 + s * 2048 + 512));
        wf[s][1] = cat8(tr_read(wr1 + s * 2048), tr_read(wr1 + s * 2048 + 512));
        asm volatile("" : "+a"(wf[s][0]), "+a"(wf[s][1]));
    }
    if (tid < 64) *(LAS u32x4*)(lds + L_U + (tid >> 5) * CV_UCH + (tid & 31) * 16) = u32x4{0, 0, 0, 0};
    *(LAS u32x4*)(lds + L_U + 512 + tid * 16) = uu0;
    *(LAS u32x4*)(lds + L_U + CV_UCH + 512 + tid * 16) = uu1;
    WAITL(0);
    SCHED();
    TS(12);
    const unsigned w2s2 = (448 * w + 224) * 1024 + d0 * 2, w2l2 = lbase + w2slot * G1_SLOT;
#define STAGE_W2B(I) bdma16<8 * (I) * 1024, (I) * 1024>(rwd, w2voff, w2s2, w2l2)
    {
        const unsigned hvo = WS_H + (32 * w + (lane & 31)) * 32 + (lane >> 5) * 16, hso = (mt * 112 + chunk * 7) * 4096;
        sfor<0, 7>([&](auto P) {
            constexpr int p = P.v;
            u32x4 o;
            sfor<0, 2>([&](auto R) {
                constexpr int r = 2 * R.v;
                const f32x2 g0 = {acc[p][2 * r], acc[p][2 * r + 1]}, u0 = {acc[p][8 + 2 * r], acc[p][9 + 2 * r]};
                const f32x2 g1 = {acc[p][2 * r + 2], acc[p][2 * r + 3]}, u1 = {acc[p][10 + 2 * r], acc[p][11 + 2 * r]};
                o[r] = pk2v(silu2(g0) * u0);
                o[r + 1] = pk2v(silu2(g1) * u1);
                SCHED();
                if constexpr (2 * p + R.v < 16 && p < 7) STAGE_W2B(2 * p + R.v < 14 ? 2 * p + R.v : 0);
                SCHED();
            });
            st128(rs, o, hvo, hso + p * 4096);
            SCHED();
        });
    }
    BAR();
    TS(13);

    const int chl = w >> 1, rh = w & 1;
    constexpr int PD = 8;
    bf16x8 hf[PD][2];
    const unsigned hvoff = WS_H + (64 * th2 + (lane & 31)) * 32 + (lane >> 5) * 16;
    const unsigned hsoff = (mt2 * 112 + 28 * w) * 4096;
    f32x4 cacc[8];
    {
        LAS char* ub = lds + L_U + chl * CV_UCH + 2 * (256 + 16 * (l15 - (g16 >> 1)) + 8 * (g16 & 1)) - 32 * (8 * rh + 6);
        const int i0 = 2047 - 16 * (g16 >> 1) - l15 + 8 * (g16 & 1);
        LAS char* kb = lds + L_KIMG + chl * CV_KCH + ((l15 & 1) ? 0 : 4160) + 4 * (i0 >> 1) - 32 * (8 * rh + 6);
        LAS char* kbm[8];
#pragma unroll
        for (int m = 0; m < 8; ++m) { kbm[m] = kb - 512 * m; asm volatile("" : "+v"(kbm[m])); }
        auto ldG = [&](int m, int rr) {
            const LAS unsigned* p = (const LAS unsigned*)(kbm[m] + 64 * (3 - rr));
            u32x4 t; t[0] = p[0]; t[1] = p[1]; t[2] = p[2]; t[3] = p[3];
            return __builtin_bit_cast(bf16x8, t);
        };
        auto ldF = [&](int m, int rr) { return *(const LAS bf16x8*)(ub + 64 * (3 - rr) + 512 * m); };
        bf16x8 G[2][8], Fr[2][8];
#pragma unroll
        for (int m = 0; m < 8; ++m) { G[0][m] = ldG(m, 0); Fr[0][m] = ldF(m, 0); }
        SCHED();
        u64 fh;
        sfor<0, 4>([&](auto RR) {
            constexpr int rr = RR.v, c = rr & 1, n = c ^ 1;
            if constexpr (rr == 1) {
                WAITV(7);
                BAR();
                if (tid == 0) st_flag(ws, WS_FH + (mt * 16 + chunk) * 128, tag);
                SCHED();
            }
            if constexpr (rr == 3) {
                asm volatile("s_waitcnt vmcnt(0)" : "+v"(fh)::"memory");
                if (!__all(fh == tag)) wait_flags<1>(ws, WS_FH + (mt2 * 16 + 4 * w) * 128, 4, tag, lane);
                SCHED();
            }
            sfor<0, 36>([&](auto C) {
                constexpr int cnt = C.v;
                constexpr int m = cnt < 8 ? 0 : cnt < 15 ? 1 : cnt < 21 ? 2 : cnt < 26 ? 3 : cnt < 30 ? 4 : cnt < 33 ? 5 : cnt < 35 ? 6 : 7;
                constexpr int first = m * 8 - (m * (m - 1)) / 2, i = m + (cnt - first);
                mfma16_vv<rr == 0 && m == 0>(cacc[i], G[c][m], Fr[c][i - m]);
                if constexpr (rr < 3) {
                    if constexpr (cnt < 8) Fr[n][cnt] = ldF(cnt, rr + 1);
                    else if constexpr (cnt < 24 && !(cnt & 1)) G[n][(cnt - 8) >> 1] = ldG((cnt - 8) >> 1, rr + 1);
                }
                if constexpr (rr < 2 && cnt % 5 == 2 && cnt / 5 < 7) STAGE_W2B(14 + 7 * rr + cnt / 5);
                if constexpr (rr == 2 && cnt == 24) aload8sc1(fh, ws + WS_FH + (mt2 * 16 + 4 * w) * 128, (lane & 3) * 128);
                if constexpr (rr == 3 && cnt % 4 == 1 && cnt / 4 < 8) hf[cnt / 8][(cnt / 4) & 1] = asbf(ld128(rs, hvoff + ((cnt / 4) & 1) * 1024, hsoff + (cnt / 8) * 4096));
                SCHED();
            });
        });
    }
    MFMA_DRAIN();
    TS(14);
    {
        LAS char* red = lds + L_RED;
#pragma unroll
        for (int i = 0; i < 4; ++i) {
            f32x4 lo = cacc[i], hi = cacc[4 + i];
            asm volatile("" : "+v"(lo), "+v"(hi));
            *(LAS f32x4*)(red + (w * 4 + i) * 1024 + lane * 16) = rh ? lo : hi;
            cacc[i] = rh ? hi : lo;
            hf[4 + i][0] = asbf(ld128(rs, hvoff, hsoff + (4 + i) * 4096));
            SCHED();
        }
#pragma unroll
        for (int s = 0; s < 14; ++s) {
            wf[14 + s][0] = cat8(tr_read(wr0 + s * 2048), tr_read(wr0 + s * 2048 + 512));
            wf[14 + s][1] = cat8(tr_read(wr1 + s * 2048), tr_read(wr1 + s * 2048 + 512));
            asm volatile("" : "+a"(wf[14 + s][0]), "+a"(wf[14 + s][1]));
            if (s % 3 == 1 && s / 3 < 4) { hf[4 + s / 3][1] = asbf(ld128(rs, hvoff + 1024, hsoff + (4 + s / 3) * 4096)); SCHED(); }
        }
        LBAR();
        const float dsk = bf2f(__builtin_amdgcn_readfirstlane((int)p_dsk));
#pragma unroll
        for (int i = 0; i < 4; ++i) {
            const f32x4 y = cacc[i] + *(LAS f32x4*)(red + ((w ^ 1) * 4 + i) * 1024 + lane * 16);
            const int t = 256 * (4 * rh + i) + 16 * l15 + 4 * g16;
            const u32x2 uu = *(LAS u32x2*)(lds + L_U + chl * CV_UCH + 2 * (256 + t));
            st64(rs, u32x2{pk2v(f32x2{y[0], y[1]} + unpk(uu[0]) * dsk), pk2v(f32x2{y[2], y[3]} + unpk(uu[1]) * dsk)}, WS_ST + ((c0 + chl) * L_ + t) * 2, 0);
        }
    }
    SCHED();
    TS(15);

    f32x16 a2[2][2];
    u32x4 sv[2], xv[2];
    u64 fs;
    const int t0 = mt2 * 128 + th2 * 64;
    const int tt2 = w >> 1, dh2 = w & 1;
    sfor<0, 28>([&](auto S) {
        constexpr int s = S.v;
        const bf16x8 h0 = hf[s % PD][0], h1 = hf[s % PD][1];
        mfma32_av<s == 0>(a2[0][0], wf[s][0], h0);
        mfma32_av<s == 0>(a2[1][0], wf[s][1], h0);
        if constexpr (s + PD < 28) hf[s % PD][0] = asbf(ld128(rs, hvoff, hsoff + (s + PD) * 4096));
        SCHED();
        mfma32_av<s == 0>(a2[0][1], wf[s][0], h1);
        mfma32_av<s == 0>(a2[1][1], wf[s][1], h1);
        if constexpr (s + PD < 28) hf[s % PD][1] = asbf(ld128(rs, hvoff + 1024, hsoff + (s + PD) * 4096));
        if constexpr (s == 10) {
            WAITV(22);
            LBAR();
            if (tid == 0) st_flag(ws, WS_FS + tb * 8, tag);
        }
        if constexpr (s == 19) aload8sc1(fs, ws + WS_FS + (d0 >> 1) * 8, (lane & 31) * 8);
        if constexpr (s == 23) {
            asm volatile("s_waitcnt vmcnt(0)" : "+v"(fs)::"memory");
            if (!__all(fs == tag)) wait_flags<1, 8>(ws, WS_FS + (d0 >> 1) * 8, 32, tag, lane);
#pragma unroll
            for (int i = 0; i < 2; ++i) {
                const int e = tid + 256 * i;
                sv[i] = ld128(rs, WS_ST + ((d0 + (e >> 3)) * L_ + t0) * 2 + (e & 7) * 16, 0);
            }
#pragma unroll
            for (int g = 0; g < 2; ++g) xv[g] = *(const u32x4*)(x + (size_t)(t0 + 32 * tt2 + (lane & 31)) * D_ + d0 + 32 * dh2 + 16 * g + 8 * (lane >> 5));
        }
        SCHED();
    });
    MFMA_DRAIN();
    TS(16);
    {
        sfor<0, 4>([&](auto Q) {
            constexpr int q = Q.v, tt = q >> 1, dh = q & 1;
            if (q != w) {
                const f32x16 a = a2[dh][tt];
#pragma unroll
                for (int g = 0; g < 4; ++g)
                    *(LAS f32x4*)(lds + ((q * 4 + w) * 4 + g) * 1024 + lane * 16) = f32x4{a[4 * g], a[4 * g + 1], a[4 * g + 2], a[4 * g + 3]};
            }
        });
#pragma unroll
        for (int i = 0; i < 2; ++i) *(LAS u32x4*)(lds + L_ST + (tid + 256 * i) * 16) = sv[i];
        LBAR();
    }
    TS(17);
    {
        f32x16 own;
        { f32x16 q0 = a2[0][0], q1 = a2[1][0], q2 = a2[0][1], q3 = a2[1][1];
          asm volatile("" : "+v"(q0), "+v"(q1), "+v"(q2), "+v"(q3));
          own = w == 0 ? q0 : w == 1 ? q1 : w == 2 ? q2 : q3; }
        f32x4 f[4];
#pragma unroll
        for (int g = 0; g < 4; ++g) {
            f[g] = f32x4{own[4 * g], own[4 * g + 1], own[4 * g + 2], own[4 * g + 3]};
#pragma unroll
            for (int o = 1; o < 4; ++o) f[g] += *(LAS f32x4*)(lds + ((w * 4 + ((w + o) & 3)) * 4 + g) * 1024 + lane * 16);
        }
        LAS char* sp = lds + L_ST + (32 * dh2 + 8 * (lane >> 5) + q4) * 128 + (32 * tt2 + 16 * (g16 & 1) + 4 * pp) * 2;
#pragma unroll
        for (int g2 = 0; g2 < 2; ++g2) {
            const s16x4 sa = tr_read(sp + g2 * 16 * 128), sb = tr_read(sp + (g2 * 16 + 4) * 128);
            const u32x2 s01 = __builtin_bit_cast(u32x2, sa), s23 = __builtin_bit_cast(u32x2, sb);
            u32x4 o;
            o[0] = pk2v(unpk(xv[g2][0]) + unpk(s01[0]) * silu2(f32x2{f[2 * g2][0], f[2 * g2][1]}));
            o[1] = pk2v(unpk(xv[g2][1]) + unpk(s01[1]) * silu2(f32x2{f[2 * g2][2], f[2 * g2][3]}));
            o[2] = pk2v(unpk(xv[g2][2]) + unpk(s23[0]) * silu2(f32x2{f[2 * g2 + 1][0], f[2 * g2 + 1][1]}));
            o[3] = pk2v(unpk(xv[g2][3]) + unpk(s23[1]) * silu2(f32x2{f[2 * g2 + 1][2], f[2 * g2 + 1][3]}));
            *(u32x4*)(out + (size_t)(t0 + 32 * tt2 + (lane & 31)) * D_ + d0 + 32 * dh2 + 16 * g2 + 8 * (lane >> 5)) = o;
        }
    }
    if (b == 0 && tid == 0) st_flag(ws, WS_EP, tag);
    TS(18);
    TSR(31);
    TS_DUMP();
    asm volatile(".Lfused_pf_end:" ::: "memory");
}

extern "C" void kernel_launch(void* const* d_in, const int* in_sizes, int n_in, void* d_out, int out_size, void* d_ws, size_t ws_size,
                              hipStream_t stream) {
    (void)hipFuncSetAttribute((const void*)fused_s4_ffn, hipFuncAttributeMaxDynamicSharedMemorySize, LDS_TOTAL);
    fused_s4_ffn<<<256, 256, LDS_TOTAL, stream>>>((const bf16_t*)d_in[0], (const bf16_t*)d_in[1], (const bf16_t*)d_in[2], (const bf16_t*)d_in[3],
                                                  (const bf16_t*)d_in[4], (const bf16_t*)d_in[5], (const bf16_t*)d_in[6], (const bf16_t*)d_in[7],
                                                  (const bf16_t*)d_in[8], (const bf16_t*)d_in[10], (const bf16_t*)d_in[11], (const bf16_t*)d_in[12],
                                                  (bf16_t*)d_out, (char*)d_ws);
}
```

```cpp
#include <hip/hip_runtime.h>
#include <stdint.h>

typedef unsigned short bf16_t;
typedef short bf16x8 __attribute__((ext_vector_type(8)));
typedef short s16x4 __attribute__((ext_vector_type(4)));
typedef float f32x2 __attribute__((ext_vector_type(2)));
typedef float f32x4 __attribute__((ext_vector_type(4)));
typedef float f32x16 __attribute__((ext_vector_type(16)));
typedef unsigned u32x4 __attribute__((ext_vector_type(4)));
typedef unsigned u32x2 __attribute__((ext_vector_type(2)));
typedef __bf16 bf16v2 __attribute__((ext_vector_type(2)));
typedef unsigned long long u64;
#define LAS __attribute__((address_space(3)))
#define GAS __attribute__((address_space(1)))
#define DI __device__ __forceinline__

constexpr int L_ = 2048, D_ = 512, N_ = 128, F_ = 1792;

constexpr unsigned WS_XN = 0;
constexpr unsigned WS_XT = 2u << 20;
constexpr unsigned WS_ST = 4u << 20;
constexpr unsigned WS_H = 6u << 20;
constexpr unsigned WS_FX = 14u << 20;
constexpr unsigned WS_FH = WS_FX + 256 * 128;
constexpr unsigned WS_FS = WS_FH + 256 * 128;
constexpr unsigned WS_FXP = WS_FS + 256 * 128;
constexpr unsigned WS_EP = WS_FXP + 256 * 8;
constexpr unsigned WS_USED = 15u << 20;

constexpr int G1_SLOT = 28800, G1_UP = 14336 + 128;
constexpr int KG_BASE = 3 * G1_SLOT, KG_RS = 528, KG_CH = 53 * KG_RS;
constexpr int L_KC = 144128;
constexpr int L_KIMG = 146816;
constexpr int L_LNS = L_KIMG, LN_RS = 1056;
constexpr int L_PF = L_LNS + 8 * LN_RS;
constexpr int CV_KCH = 8320;
constexpr int L_U = 2 * G1_SLOT, CV_UCH = 4608;
constexpr int L_RED = L_U + 2 * CV_UCH;
constexpr int G2_PRS = 272, G2_PART = 64 * G2_PRS;
constexpr int L_ST = 4 * G2_PART;
constexpr int LDS_TOTAL = 163840;
static_assert(KG_BASE + 2 * KG_CH <= L_KC, "K-gen scratch");
static_assert(L_KIMG + 2 * CV_KCH <= LDS_TOTAL && L_RED + 16384 <= 3 * G1_SLOT && L_ST + 8192 <= 3 * G1_SLOT, "lds map");

DI float bf2f(unsigned u16) { return __uint_as_float(u16 << 16); }
DI float bflo(unsigned w) { return __uint_as_float(w << 16); }
DI float bfhi(unsigned w) { return __uint_as_float(w & 0xffff0000u); }
DI unsigned pk2(float lo, float hi) {
    f32x2 v = {lo, hi};
    bf16v2 b = __builtin_convertvector(v, bf16v2);
    return __builtin_bit_cast(unsigned, b);
}
DI float rbf(float x) { return bflo(pk2(x, x)); }

#define WAITV(n) asm volatile("s_waitcnt vmcnt(" #n ")" ::: "memory")
#define WAITL(n) asm volatile("s_waitcnt lgkmcnt(" #n ")" ::: "memory")
#define BAR() do { asm volatile("" ::: "memory"); __builtin_amdgcn_s_barrier(); asm volatile("" ::: "memory"); } while (0)
#define LBAR() do { WAITL(0); BAR(); } while (0)
#define SCHED() __builtin_amdgcn_sched_barrier(0)

#define TS(i) do {} while (0)
#define TSR(i) do {} while (0)
#define TS_DUMP() do {} while (0)

template <int CTRL, int RMASK> DI float dpp_add(float v) {
    return v + __builtin_bit_cast(float, __builtin_amdgcn_update_dpp(0, __builtin_bit_cast(int, v), CTRL, RMASK, 0xf, true));
}
DI float wave_sum(float v) {
    v = dpp_add<0x111, 0xf>(v); v = dpp_add<0x112, 0xf>(v); v = dpp_add<0x114, 0xf>(v); v = dpp_add<0x118, 0xf>(v);
    v = dpp_add<0x142, 0xa>(v); v = dpp_add<0x143, 0xc>(v);
    return __builtin_bit_cast(float, __builtin_amdgcn_readlane(__builtin_bit_cast(int, v), 63));
}
DI s16x4 tr_read(LAS char* p) { return __builtin_amdgcn_ds_read_tr16_b64_v4i16((LAS s16x4*)p); }
DI bf16x8 cat8(s16x4 a, s16x4 b) { return __builtin_shufflevector(a, b, 0, 1, 2, 3, 4, 5, 6, 7); }
DI void glds16(const void* sbase, unsigned voff, unsigned ldst) {
    asm volatile("s_mov_b32 m0, %2\n\ts_nop 0\n\tglobal_load_lds_dwordx4 %0, %1" ::"v"(voff), "s"(sbase), "s"(ldst) : "memory");
}
DI void glds4(const void* sbase, unsigned voff, unsigned ldst) {
    asm volatile("s_mov_b32 m0, %2\n\ts_nop 0\n\tglobal_load_lds_dword %0, %1" ::"v"(voff), "s"(sbase), "s"(ldst) : "memory");
}
typedef __amdgpu_buffer_rsrc_t rsrc_t;
constexpr int SC1 = 16;
DI u32x4 ld128(rsrc_t r, unsigned voff, unsigned soff) { return __builtin_amdgcn_raw_buffer_load_b128(r, voff, soff, SC1); }
DI void st128(rsrc_t r, u32x4 v, unsigned voff, unsigned soff) {
    asm volatile("buffer_store_dwordx4 %0, %1, %2, %3 offen sc1\n\ts_nop 1" ::"v"(v), "v"(voff), "s"(r), "s"(soff) : "memory");
}
DI void st64(rsrc_t r, u32x2 v, unsigned voff, unsigned soff) { __builtin_amdgcn_raw_buffer_store_b64(v, r, voff, soff, SC1); }
DI bf16x8 asbf(u32x4 v) { return __builtin_bit_cast(bf16x8, v); }

template <int SIMM, int LIMM> DI void bdma16(rsrc_t r, unsigned voff, unsigned sbase, unsigned lbase) {
    unsigned t;
    asm volatile("s_add_u32 m0, %1, %2\n\ts_add_u32 %0, %3, %4\n\tbuffer_load_dwordx4 %5, %6, %0 offen lds"
                 : "=&s"(t) : "s"(lbase), "i"(LIMM), "s"(sbase), "i"(SIMM), "v"(voff), "s"(r) : "memory", "scc");
}
DI void warm4(unsigned& pf, const void* sbase, unsigned voff) { asm volatile("global_load_dword %0, %1, %2" : "+v"(pf) : "v"(voff), "s"(sbase) : "memory"); }
template <int SIMM> DI void bwarm4(unsigned& pf, rsrc_t r, unsigned voff) {
    unsigned t;
    asm volatile("s_mov_b32 %1, %4\n\tbuffer_load_dword %0, %2, %3, %1 offen" : "+v"(pf), "=&s"(t) : "v"(voff), "s"(r), "i"(SIMM) : "memory");
}
DI void aload16(u32x4& d, const void* sbase, unsigned voff) { asm volatile("global_load_dwordx4 %0, %1, %2" : "=&v"(d) : "v"(voff), "s"(sbase) : "memory"); }
DI void aload2(unsigned& d, const void* sbase, unsigned voff) { asm volatile("global_load_ushort %0, %1, %2" : "=&v"(d) : "v"(voff), "s"(sbase) : "memory"); }
DI void aload8sc1(u64& d, const void* sbase, unsigned voff) { asm volatile("global_load_dwordx2 %0, %1, %2 sc1" : "=&v"(d) : "v"(voff), "s"(sbase) : "memory"); }
template <int SIMM> DI void abload16sc1(bf16x8& d, rsrc_t r, unsigned voff, unsigned sbase) {
    unsigned t;
    asm volatile("s_add_u32 %1, %4, %5\n\tbuffer_load_dwordx4 %0, %2, %3, %1 offen sc1" : "=&a"(d), "=&s"(t) : "v"(voff), "s"(r), "s"(sbase), "i"(SIMM) : "memory", "scc");
}
template <bool FIRST> DI void mfma32_va(f32x16& c, bf16x8 a, bf16x8 b) {
    if constexpr (FIRST) asm volatile("v_mfma_f32_32x32x16_bf16 %0, %1, %2, 0" : "=&v"(c) : "v"(a), "a"(b));
    else asm volatile("v_mfma_f32_32x32x16_bf16 %0, %1, %2, %0" : "+v"(c) : "v"(a), "a"(b));
}
template <bool FIRST> DI void mfma32_av(f32x16& c, bf16x8 a, bf16x8 b) {
    if constexpr (FIRST) asm volatile("v_mfma_f32_32x32x16_bf16 %0, %1, %2, 0" : "=&v"(c) : "a"(a), "v"(b));
    else asm volatile("v_mfma_f32_32x32x16_bf16 %0, %1, %2, %0" : "+v"(c) : "a"(a), "v"(b));
}
template <bool FIRST> DI void mfma16_vv(f32x4& c, bf16x8 a, bf16x8 b) {
    if constexpr (FIRST) asm volatile("v_mfma_f32_16x16x32_bf16 %0, %1, %2, 0" : "=&v"(c) : "v"(a), "v"(b));
    else asm volatile("v_mfma_f32_16x16x32_bf16 %0, %1, %2, %0" : "+v"(c) : "v"(a), "v"(b));
}
#define MFMA_DRAIN() asm volatile("s_nop 15\n\ts_nop 3" ::: "memory")
template <int SIMM> DI void abload16sc1_v(u32x4& d, rsrc_t r, unsigned voff, unsigned sbase) {
    unsigned t;
    asm volatile("s_add_u32 %1, %4, %5\n\tbuffer_load_dwordx4 %0, %2, %3, %1 offen sc1" : "=&v"(d), "=&s"(t) : "v"(voff), "s"(r), "s"(sbase), "i"(SIMM) : "memory", "scc");
}
template <int IMM> DI void aload8sc1_o(u64& d, const void* sbase, unsigned voff) {
    asm volatile("global_load_dwordx2 %0, %1, %2 sc1" : "=&v"(d) : "v"(voff + IMM), "s"(sbase) : "memory");
}
template <int N> DI void waitv() { asm volatile("s_waitcnt vmcnt(%0)" ::"i"(N) : "memory"); }
template <int N> struct IC { static constexpr int v = N; };
template <int B, int E, class F> DI void sfor(F&& f) { if constexpr (B < E) { f(IC<B>{}); sfor<B + 1, E>(f); } }

struct G1Op { int step, gap, kind, a, b; };
struct G1Sched {
    G1Op op[128]; int n;
    constexpr G1Sched() : op{}, n(0) {
        for (int u = 0; u < 8; ++u) op[n++] = G1Op{-1, 0, 0, u, 0};
        for (int i = 0; i < 7; ++i) { op[n++] = G1Op{-1, 0, 1, 1, i}; op[n++] = G1Op{-1, 0, 1, 2, i}; }
        op[n++] = G1Op{-1, 0, 6, 0, 0};
        for (int s = 0; s < 8; ++s) {
            G1Op list[20] = {}; int m = 0;
            if (s < 3) for (int i = 0; i < 8; ++i) { list[m++] = G1Op{0, 0, 0, 8 + 8 * s + i, 0}; if (i < 7) list[m++] = G1Op{0, 0, 1, 3 + s, i}; }
            if (s == 3 || s == 4) for (int i = 0; i < 7; ++i) list[m++] = G1Op{0, 0, 1, 3 + s, i};
            if (s < 4) for (int i = 0; i < 2; ++i) list[m++] = G1Op{0, 0, 5, 2 * s + i, 0};
            if (s == 4) for (int i = 0; i < 2; ++i) list[m++] = G1Op{0, 0, 3, i, 0};
            if (s == 6) for (int i = 0; i < 2; ++i) list[m++] = G1Op{0, 0, 4, i, 0};
            if (s >= 4) for (int i = 0; i < 7; ++i) list[m++] = G1Op{0, 0, 2, s - 4, i};
            const int span = s == 7 ? 14 : 28;
            for (int i = 0; i < m; ++i) { const int g = (i * span) / m; list[i].step = 4 * s + g / 7; list[i].gap = g % 7; op[n++] = list[i]; }
        }
    }
    constexpr int younger(int kind, int a, int st) const {
        int last = -1;
        for (int i = 0; i < n; ++i) if (op[i].kind == kind && op[i].a == a) last = i;
        int c = 0;
        for (int i = last + 1; i < n; ++i) if (op[i].step < st) ++c;
        return c;
    }
};
constexpr G1Sched G1S{};

DI u64 ld_flag(const char* ws, unsigned off) { return __hip_atomic_load((GAS u64*)(ws + off), __ATOMIC_RELAXED, __HIP_MEMORY_SCOPE_AGENT); }
DI void st_flag(char* ws, unsigned off, u64 v) { __hip_atomic_store((GAS u64*)(ws + off), v, __ATOMIC_RELAXED, __HIP_MEMORY_SCOPE_AGENT); }
template <int NPL, int STRIDE = 128>
DI void wait_flags(const char* ws, unsigned base, int nl, u64 tag, int lane) {
    const unsigned off = base + (lane < nl ? lane : 0) * STRIDE;
#pragma unroll 1
    for (int spin = 0; spin < 40000; ++spin) {
        bool ok = true;
#pragma unroll
        for (int i = 0; i < NPL; ++i) ok &= ld_flag(ws, off + i * 64 * STRIDE) == tag;
        if (__all(ok)) break;
        __builtin_amdgcn_s_sleep(1);
    }
    asm volatile("" ::: "memory");
}

typedef f32x2 cf;
DI cf cmul(cf a, cf b) {
    cf t, z;
    asm("v_pk_mul_f32 %0, %1, %2 op_sel_hi:[0,1]" : "=v"(t) : "v"(a), "v"(b));
    asm("v_pk_fma_f32 %0, %1, %2, %3 op_sel:[1,1,0] op_sel_hi:[1,0,1] neg_lo:[0,1,0]" : "=v"(z) : "v"(a), "v"(b), "v"(t));
    return z;
}
DI void cmul2(cf& z0, cf a0, cf b0, cf& z1, cf a1, cf b1) {
    cf t0, t1;
    asm("v_pk_mul_f32 %2, %4, %5 op_sel_hi:[0,1]\n\tv_pk_mul_f32 %3, %6, %7 op_sel_hi:[0,1]\n\t"
        "v_pk_fma_f32 %0, %4, %5, %2 op_sel:[1,1,0] op_sel_hi:[1,0,1] neg_lo:[0,1,0]\n\t"
        "v_pk_fma_f32 %1, %6, %7, %3 op_sel:[1,1,0] op_sel_hi:[1,0,1] neg_lo:[0,1,0]"
        : "=&v"(z0), "=&v"(z1), "=&v"(t0), "=&v"(t1) : "v"(a0), "v"(b0), "v"(a1), "v"(b1));
}
DI cf zdir(float a2, float th, float l) {
    const float m = __builtin_amdgcn_exp2f(a2 * l);
    const float r = __builtin_amdgcn_fractf(th * l);
    return cf{m * __builtin_amdgcn_cosf(r), m * __builtin_amdgcn_sinf(r)};
}
DI f32x2 silu2(f32x2 g) {
    const f32x2 t = g * -1.44269504088896341f;
    const f32x2 d = f32x2{__builtin_amdgcn_exp2f(t[0]), __builtin_amdgcn_exp2f(t[1])} + 1.0f;
    return g * f32x2{__builtin_amdgcn_rcpf(d[0]), __builtin_amdgcn_rcpf(d[1])};
}
DI unsigned pk2v(f32x2 v) { return __builtin_bit_cast(unsigned, __builtin_convertvector(v, bf16v2)); }
DI f32x2 unpk(unsigned w) { return f32x2{bflo(w), bfhi(w)}; }
DI float silu_f(float g) { return g * __builtin_amdgcn_rcpf(1.0f + __builtin_amdgcn_exp2f(g * -1.44269504088896341f)); }

extern __shared__ __attribute__((aligned(16))) char dyn_smem[];

__global__ __launch_bounds__(256, 1) void fused_s4_ffn(const bf16_t* __restrict__ x, const bf16_t* __restrict__ A_re, const bf16_t* __restrict__ A_im,
                                                       const bf16_t* __restrict__ B_re, const bf16_t* __restrict__ B_im, const bf16_t* __restrict__ C_re,
                                                       const bf16_t* __restrict__ C_im, const bf16_t* __restrict__ log_dt, const bf16_t* __restrict__ Dskip,
                                                       const bf16_t* __restrict__ wg, const bf16_t* __restrict__ wu, const bf16_t* __restrict__ wd,
                                                       bf16_t* __restrict__ out, char* ws) {
    LAS char* lds = (LAS char*)dyn_smem;
    const unsigned lbase = (unsigned)(size_t)lds;
    const int tid = threadIdx.x, w = __builtin_amdgcn_readfirstlane(tid >> 6), lane = tid & 63;
    const int b = blockIdx.x, xcd = b & 7, j = b >> 3;
    const int tb = xcd * 32 + j;
    const int mt = 8 * (xcd >> 2) + (j >> 2), chunk = 4 * (xcd & 3) + (j & 3);
    const int mt2 = 2 * xcd + (j >> 4);
    const int c0 = 2 * tb;
    const unsigned long long probe_t0 = __builtin_amdgcn_s_memrealtime();
    unsigned long long probe_a = 0;
    if (threadIdx.x == 0) __hip_atomic_store((GAS u64*)((char*)ws + (20u << 20) + blockIdx.x * 8), probe_t0, __ATOMIC_RELAXED, __HIP_MEMORY_SCOPE_AGENT);
    TSR(30); TS(0);

    u32x4 raw[2];
#pragma unroll
    for (int r = 0; r < 2; ++r) aload16(raw[r], x + (size_t)(8 * tb + 2 * w + r) * D_, lane * 16);
    unsigned pf = 0;
    {
        u64 pc; unsigned len;
        asm volatile("s_getpc_b64 %0\n.Lfused_pf_here:\n\ts_mov_b32 %1, .Lfused_pf_end-.Lfused_pf_here" : "=s"(pc), "=s"(len));
        const unsigned o = (unsigned)(12 * j + (lane < 12 ? lane : 11)) * 128u;
        warm4(pf, (const void*)pc, o < len ? o : len);
    }
    u64 ep;
    aload8sc1(ep, ws + WS_EP, 0);
    const int kch = c0 + (w >> 1);
    const unsigned kpo = (64 * (w & 1) + lane) * 2;
    unsigned p_are, p_aim, p_br, p_bi, p_cr, p_ci, p_ldt, p_dsk;
    aload2(p_are, A_re + kch * N_, kpo); aload2(p_aim, A_im + kch * N_, kpo); aload2(p_br, B_re + kch * N_, kpo);
    aload2(p_bi, B_im + kch * N_, kpo); aload2(p_cr, C_re + kch * N_, kpo); aload2(p_ci, C_im + kch * N_, kpo);
    aload2(p_ldt, log_dt + kch, 0); aload2(p_dsk, Dskip + kch, 0);
    SCHED();
    const rsrc_t rs = __builtin_amdgcn_make_buffer_rsrc(ws, 0, WS_USED, 0x00020000);
    const rsrc_t rw1 = __builtin_amdgcn_make_buffer_rsrc((void*)((w & 1) ? wu : wg), 0, D_ * F_ * 2, 0x00020000);
    const rsrc_t rwd = __builtin_amdgcn_make_buffer_rsrc((void*)wd, 0, D_ * F_ * 2, 0x00020000);
    unsigned voff[7];
    {
        const int k = (lane * 147) >> 11, c = lane - 14 * k;
        unsigned v = k * (F_ * 2) + c * 16;
        int cc = c;
#pragma unroll
        for (int i = 0; i < 7; ++i) {
            voff[i] = v;
            cc += 8; v += 4 * (F_ * 2) + 128;
            if (cc >= 14) { cc -= 14; v += F_ * 2 - 224; }
        }
    }
    const unsigned w1s = (32 * (w >> 1)) * (F_ * 2) + chunk * 224;
    const unsigned w1l = lbase + (w & 1) * G1_UP + (w >> 1) * 7168;
#define STAGE_W1(S, I) bdma16<64 * (S) * (F_ * 2), ((S) % 5) * G1_SLOT + (I) * 1024>(rw1, voff[I], w1s, w1l)
    sfor<0, 7>([&](auto I) { STAGE_W1(0, I.v); });
    {
        const int r = 14 * (w >> 1) + (lane < 14 ? lane : 13), row = (r * 147) >> 10;
        warm4(pf, (w & 1) ? wu : wg, (64 + 4 * j + row) * (F_ * 2) + (xcd & 3) * 896 + (r - 7 * row) * 128);
    }
    const int wdt = 14 * w + (lane < 14 ? lane : 13);
    const unsigned wdpf = (448 * (j >> 3) + 28 * (j & 7) + (wdt >> 3)) * 1024 + (wdt & 7) * 128;
    SCHED();
    const int g16 = lane >> 4, q4 = (lane & 15) >> 2, pp = lane & 3, l15 = lane & 15;
    unsigned ksc[8];
    {
        const int col8 = 8 * (16 * (w & 1) + l15);
        const unsigned kcb = lbase + L_KC + (w >> 1) * 1312;
#pragma unroll
        for (int r = 0; r < 8; ++r) {
            int l1;
            if (r < 4) l1 = g16 == 0 ? r : g16 == 1 ? 4 + r : g16 == 2 ? 256 + 2 * r : 512 + 256 * (r >> 1) + 4 * (r & 1);
            else l1 = g16 == 0 ? 1024 + 256 * (r - 4) : 2048;
            const int v = l1 + col8;
            int idx = v < 128 ? v : (int)(__float_as_uint((float)v) >> 16) - 17024;
            if (r >= 4 && !(g16 == 0 || (g16 == 1 && r == 4 && col8 == 0))) idx = 648;
            ksc[r] = kcb + idx * 2;
            asm volatile("" : "+v"(ksc[r]));
        }
    }
    unsigned kga[9];
#pragma unroll
    for (int e = 0; e < 9; ++e) {
        const int l = 2047 - 8 * tid - e;
        int idx = l < 256 ? l : (int)(pk2((float)l, 0.f) & 0xffffu) - 17024;
        if (l < 0) idx = 649;
        kga[e] = lbase + L_KC + idx * 2;
        asm volatile("" : "+v"(kga[e]));
    }
    SCHED();
    asm volatile("s_waitcnt vmcnt(8)" : "+v"(ep), "+v"(raw[0]), "+v"(raw[1]), "+v"(p_are), "+v"(p_aim), "+v"(p_br), "+v"(p_bi), "+v"(p_cr), "+v"(p_ci),
                 "+v"(p_ldt), "+v"(p_dsk)::"memory");
    SCHED();
    TS(1);

    {
        f32x2 v[2][4];
        float s1[2], s2[2];
#pragma unroll
        for (int r = 0; r < 2; ++r) {
#pragma unroll
            for (int jj = 0; jj < 4; ++jj) v[r][jj] = unpk(raw[r][jj]);
            const f32x2 t = (v[r][0] + v[r][1]) + (v[r][2] + v[r][3]);
            s1[r] = t[0] + t[1];
        }
        s1[0] = wave_sum(s1[0]); s1[1] = wave_sum(s1[1]);
#pragma unroll
        for (int r = 0; r < 2; ++r) {
            const float mean = s1[r] * (1.0f / 512.0f);
            f32x2 q = {0.f, 0.f};
#pragma unroll
            for (int jj = 0; jj < 4; ++jj) { const f32x2 d = v[r][jj] - mean; q += d * d; }
            s2[r] = q[0] + q[1];
        }
        s2[0] = wave_sum(s2[0]); s2[1] = wave_sum(s2[1]);
#pragma unroll
        for (int r = 0; r < 2; ++r) {
            const float mb = rbf(s1[r] * (1.0f / 512.0f));
            const float sd = rbf(__builtin_amdgcn_sqrtf(rbf(rbf(s2[r] * (1.0f / 512.0f)) + 1e-5f)));
            const float rsd = __builtin_amdgcn_rcpf(sd);
            u32x4 o;
#pragma unroll
            for (int jj = 0; jj < 4; ++jj) o[jj] = pk2v(unpk(pk2v(v[r][jj] - mb)) * rsd);
            *(LAS u32x4*)(lds + L_LNS + (2 * w + r) * LN_RS + lane * 16) = o;
        }
    }
    LBAR();
    TS(2);
    {
        const int lmt = tb >> 4, tok0 = (tb & 15) * 8;
        const int half = tid & 1, tok = (tid >> 1) & 7, U = tid >> 4;
        const u32x4 va = *(LAS u32x4*)(lds + L_LNS + tok * LN_RS + U * 32 + half * 16);
        const u32x4 vb = *(LAS u32x4*)(lds + L_LNS + tok * LN_RS + U * 32 + half * 16 + 512);
        LAS char* tp = lds + L_LNS + q4 * LN_RS + (128 * w + 16 * g16 + 4 * pp) * 2;
        const s16x4 a0 = tr_read(tp), b0 = tr_read(tp + 4 * LN_RS), a1 = tr_read(tp + 128), b1 = tr_read(tp + 4 * LN_RS + 128);
        const unsigned xo = WS_XN + ((lmt * 32 + U) * 128 + tok0 + tok) * 32 + half * 16;
        st128(rs, va, xo, 0);
        st128(rs, vb, xo, 16 * 4096);
        const unsigned to = WS_XT + (tb * 512 + 128 * w + 16 * g16 + l15) * 16;
        st128(rs, __builtin_bit_cast(u32x4, cat8(a0, b0)), to, 0);
        st128(rs, __builtin_bit_cast(u32x4, cat8(a1, b1)), to, 1024);
    }
    SCHED();
    const u64 tag = ((u64)(unsigned)__builtin_amdgcn_readfirstlane((int)(unsigned)ep) |
                     ((u64)(unsigned)__builtin_amdgcn_readfirstlane((int)(unsigned)(ep >> 32)) << 32)) + 1;
    TS(3);

    {
        const float dt = rbf(__builtin_amdgcn_exp2f(bf2f(p_ldt) * 1.44269504088896341f));
        const float lr = dt * bf2f(p_are), li = dt * bf2f(p_aim);
        const cf dB{dt * bf2f(p_br), dt * bf2f(p_bi)};
        const cf CBt = cmul(cf{bf2f(p_cr), bf2f(p_ci)}, dB);
        const cf CB{CBt[0], -CBt[1]};
        const float a2 = lr * 1.44269504088896341f, th = li * 0.15915494309189535f;
        LAS char* qs = lds + KG_BASE + (w >> 1) * KG_CH + (64 * (w & 1) + lane) * 4;
        LAS char* ps = qs + 21 * KG_RS;
        auto putq = [&](int r, cf q) { *(LAS unsigned*)(qs + r * KG_RS) = pk2v(q); };
        auto putp = [&](int r, cf p) { *(LAS unsigned*)(ps + r * KG_RS) = pk2v(p); };
        const cf z1 = zdir(a2, -th, 1.f), Z256 = zdir(a2, -th, 256.f), w8 = zdir(a2, th, 8.f);
        cf z2, z3, z4, z5, z6, z7, w16, q256, q512, q768, q1024, q1280, q1536, q1792, q2048, t0, t1;
        cmul2(z2, z1, z1, w16, w8, w8);
        cmul2(q256, CB, Z256, t0, CB, z1);
        putq(0, CB);
        cmul2(z3, z2, z1, z4, z2, z2);
        putq(1, t0);
        cmul2(q512, q256, Z256, t0, CB, z2);
        putq(8, q256);
        cmul2(z5, z4, z1, z6, z4, z2);
        putq(2, t0);
        cmul2(z7, z4, z3, t0, CB, z3);
        putq(12, q512);
        cmul2(q768, q512, Z256, t1, CB, z4);
        putq(3, t0);
        cmul2(t0, CB, z5, q1024, q768, Z256);
        putq(4, t1); putq(14, q768);
        cmul2(t1, CB, z6, q1280, q1024, Z256);
        putq(5, t0); putq(16, q1024);
        cmul2(t0, CB, z7, q1536, q1280, Z256);
        putq(6, t1); putq(17, q1280);
        cmul2(t1, q256, z2, q1792, q1536, Z256);
        putq(7, t0); putq(18, q1536);
        cmul2(t0, q256, z4, q2048, q1792, Z256);
        putq(9, t1); putq(19, q1792);
        cmul2(t1, q256, z6, z5, q512, z4);
        putq(10, t0); putq(20, q2048);
        cf pa{1.f, 0.f}, pb = w8, na, nb;
        cmul2(t0, q768, z4, na, pa, w16);
        putq(11, t1); putq(13, z5);
        cmul2(nb, pb, w16, t1, pb, w16);
        putq(15, t0);
        TS(4);
        sfor<0, 16>([&](auto J) {
            constexpr int jj = J.v;
            putp(2 * jj, pa); putp(2 * jj + 1, pb);
            pa = na; pb = nb;
            if constexpr (jj < 14) cmul2(na, pa, w16, nb, pb, w16);
            if constexpr (jj == 5) {
                SCHED();
                WAITV(0);
                BAR();
                if (tid == 0) { st_flag(ws, WS_FX + tb * 128, tag); st_flag(ws, WS_FXP + tb * 8, tag); }
                SCHED();
            }
        });
        if (tid < 2) *(LAS unsigned short*)(lds + L_KC + tid * 1312 + 649 * 2) = 0;
        LBAR();
    }
    TS(5);
    bf16x8 xf[32];
    const unsigned xvo = WS_XN + (32 * w + (lane & 31)) * 32 + (lane >> 5) * 16, xso = mt * 32 * 4096;
    {
        LAS char* qa = lds + KG_BASE + (w >> 1) * KG_CH + l15 * KG_RS + g16 * 16;
        LAS char* pa_ = qa + (21 + 16 * (w & 1)) * KG_RS;
        bf16x8 fa[8], fb[8], fc[8];
#pragma unroll
        for (int s = 0; s < 8; ++s) { fb[s] = *(LAS bf16x8*)(pa_ + s * 64); fa[s] = *(LAS bf16x8*)(qa + s * 64); fc[s] = *(LAS bf16x8*)(qa + 16 * KG_RS + s * 64); }
        SCHED();
        TS(6);
        wait_flags<1>(ws, WS_FX + (16 * mt + 4 * w) * 128, 4, tag, lane);
        TS(7);
        sfor<0, 8>([&](auto U) { abload16sc1<U.v * 4096>(xf[U.v], rs, xvo, xso); });
        SCHED();
        f32x4 k0, k1;
        sfor<0, 8>([&](auto S) {
            mfma16_vv<S.v == 0>(k0, fa[S.v], fb[S.v]);
            if constexpr (S.v < 7) STAGE_W1(1, S.v);
            SCHED();
            mfma16_vv<S.v == 0>(k1, fc[S.v], fb[S.v]);
            if constexpr (S.v < 7) STAGE_W1(2, S.v);
            SCHED();
        });
        {
            const int r = 35 * (w >> 1) + (lane < 35 ? lane : 34), row = (r * 147) >> 10;
            warm4(pf, (w & 1) ? wu : wg, (192 + 10 * j + row) * (F_ * 2) + (xcd & 3) * 896 + (r - 7 * row) * 128);
        }
        asm volatile("s_nop 7" : "+v"(k0), "+v"(k1));
#pragma unroll
        for (int r = 0; r < 4; r += 2) {
            const unsigned a0 = pk2(k0[r], k0[r + 1]), a1 = pk2(k1[r], k1[r + 1]);
            *(LAS unsigned short*)(size_t)ksc[r] = (unsigned short)a0;
            *(LAS unsigned short*)(size_t)ksc[r + 1] = (unsigned short)(a0 >> 16);
            *(LAS unsigned short*)(size_t)ksc[4 + r] = (unsigned short)a1;
            *(LAS unsigned short*)(size_t)ksc[5 + r] = (unsigned short)(a1 >> 16);
        }
    }
    SCHED();
    LBAR();
    TS(8);
    {
        unsigned val[2][9];
#pragma unroll
        for (int ch = 0; ch < 2; ++ch)
#pragma unroll
            for (int e = 0; e < 9; ++e) val[ch][e] = *(LAS unsigned short*)(size_t)(kga[e] + ch * 1312);
        SCHED();
#pragma unroll
        for (int ch = 0; ch < 2; ++ch) {
            u32x4 E, O;
#pragma unroll
            for (int m = 0; m < 4; ++m) { E[m] = val[ch][2 * m] | (val[ch][2 * m + 1] << 16); O[m] = val[ch][2 * m + 1] | (val[ch][2 * m + 2] << 16); }
            *(LAS u32x4*)(lds + L_KIMG + ch * CV_KCH + tid * 16) = E;
            *(LAS u32x4*)(lds + L_KIMG + ch * CV_KCH + 4160 + tid * 16) = O;
        }
        if (tid < 16) *(LAS u32x4*)(lds + L_KIMG + (tid >> 3) * CV_KCH + ((tid >> 2) & 1) * 4160 + 4096 + (tid & 3) * 16) = u32x4{0, 0, 0, 0};
    }
    SCHED();
    TS(9);

    const int phi = ((pp & 1) << 1) | (pp >> 1);
    const int th2 = (j >> 3) & 1, d0 = (j & 7) * 64;
    const unsigned w2voff = (lane >> 3) * 1024 + (((lane & 7) ^ (4 * ((lane >> 4) & 1))) * 16);
    const unsigned w2s = 7 * w * 8 * 1024 + d0 * 2, w2l = lbase + 7 * w * 1024;
    f32x16 acc[7];
    u32x4 uu0, uu1;
    {
        LAS char* ab = lds + (g16 & 1) * G1_UP + (8 * (g16 >> 1) + q4) * 224 + phi * 8;
        bf16x8 cur[7], nxt[7];
        u32x4 fx[2];
        waitv<G1S.younger(1, 1, 0)>();
        BAR();
#pragma unroll
        for (int p = 0; p < 7; ++p) cur[p] = cat8(tr_read(ab + p * 32), tr_read(ab + 4 * 224 + p * 32));
        SCHED();
        TS(10);
        sfor<0, 32>([&](auto ST) {
            constexpr int st = ST.v, s = st >> 2, kk = st & 3;
            if constexpr (kk == 0 && s > 0) {
                if constexpr (s <= 6) waitv<G1S.younger(1, s + 1, st)>();
                BAR();
                SCHED();
            }
            if constexpr (st == 23) {
                asm volatile("s_waitcnt vmcnt(%2)" : "+v"(fx[0]), "+v"(fx[1]) : "i"(G1S.younger(3, 1, st)) : "memory");
                const u64 f0 = ((u64)fx[0][1] << 32) | fx[0][0], f1 = ((u64)fx[0][3] << 32) | fx[0][2], f2 = ((u64)fx[1][1] << 32) | fx[1][0], f3 = ((u64)fx[1][3] << 32) | fx[1][2];
                if (!__all(f0 == tag && f1 == tag && f2 == tag && f3 == tag)) wait_flags<4>(ws, WS_FX, 64, tag, lane);
                SCHED();
            }
            waitv<G1S.younger(0, st, st)>();
            SCHED();
            constexpr int n = st + 1, ns = n >> 2, nk = n & 3;
            LAS char* np = ab + (ns % 5) * G1_SLOT + nk * 16 * 224;
            sfor<0, 7>([&](auto P) {
                constexpr int p = P.v;
                mfma32_va<st == 0>(acc[p], cur[p], xf[st]);
                if constexpr (n < 32) nxt[p] = cat8(tr_read(np + p * 32), tr_read(np + 4 * 224 + p * 32));
                sfor<0, G1S.n>([&](auto K) {
                    constexpr G1Op o = G1S.op[K.v];
                    if constexpr (o.step == st && o.gap == p) {
                        if constexpr (o.kind == 0) abload16sc1<o.a * 4096>(xf[o.a], rs, xvo, xso);
                        else if constexpr (o.kind == 1) STAGE_W1(o.a, o.b);
                        else if constexpr (o.kind == 2) bdma16<(448 * o.a + 8 * o.b) * 1024, ((o.a + 3) % 5) * G1_SLOT + o.b * 1024>(rwd, w2voff, w2s, w2l);
                        else if constexpr (o.kind == 5) bwarm4<(o.a & 3) * 7168 + (o.a >> 2) * 224 * 1024>(pf, rwd, wdpf);
                        else if constexpr (o.kind == 3) abload16sc1_v<WS_FXP + o.a * 16>(fx[o.a], rs, lane * 32, 0);
                        else if constexpr (o.a == 0) abload16sc1_v<0>(uu0, rs, WS_XT + (tid * 512 + c0) * 16, 0);
                        else abload16sc1_v<16>(uu1, rs, WS_XT + (tid * 512 + c0) * 16, 0);
                    }
                });
                SCHED();
            });
#pragma unroll
            for (int p = 0; p < 7; ++p) cur[p] = nxt[p];
        });
    }
    MFMA_DRAIN();
    TS(11);
    const int w2slot = (w + 3) % 5;
    LAS char* wl = lds + w2slot * G1_SLOT;
    const int rrow = 8 * (g16 >> 1) + q4, rsw = 4 * ((q4 >> 1) & 1);
    LAS char* wr0 = wl + rrow * 128 + (((2 * (g16 & 1) + (phi >> 1)) ^ rsw) * 16) + 8 * (phi & 1);
    LAS char* wr1 = wl + rrow * 128 + (((4 + 2 * (g16 & 1) + (phi >> 1)) ^ rsw) * 16) + 8 * (phi & 1);
    bf16x8 wf[28][2];
    asm volatile("s_waitcnt vmcnt(0)" : "+v"(uu0), "+v"(uu1), "+v"(pf)::"memory");
    BAR();
#pragma unroll
    for (int s = 0; s < 14; ++s) {
        wf[s][0] = cat8(tr_read(wr0 + s * 2048), tr_read(wr0 + s * 2048 + 512));
        wf[s][1] = cat8(tr_read(wr1 + s * 2048), tr_read(wr1 + s * 2048 + 512));
        asm volatile("" : "+a"(wf[s][0]), "+a"(wf[s][1]));
    }
    if (tid < 64) *(LAS u32x4*)(lds + L_U + (tid >> 5) * CV_UCH + (tid & 31) * 16) = u32x4{0, 0, 0, 0};
    *(LAS u32x4*)(lds + L_U + 512 + tid * 16) = uu0;
    *(LAS u32x4*)(lds + L_U + CV_UCH + 512 + tid * 16) = uu1;
    WAITL(0);
    SCHED();
    TS(12);
    const unsigned w2s2 = (448 * w + 224) * 1024 + d0 * 2, w2l2 = lbase + w2slot * G1_SLOT;
#define STAGE_W2B(I) bdma16<8 * (I) * 1024, (I) * 1024>(rwd, w2voff, w2s2, w2l2)
    {
        const unsigned hvo = WS_H + (32 * w + (lane & 31)) * 32 + (lane >> 5) * 16, hso = (mt * 112 + chunk * 7) * 4096;
        sfor<0, 7>([&](auto P) {
            constexpr int p = P.v;
            u32x4 o;
            sfor<0, 2>([&](auto R) {
                constexpr int r = 2 * R.v;
                const f32x2 g0 = {acc[p][2 * r], acc[p][2 * r + 1]}, u0 = {acc[p][8 + 2 * r], acc[p][9 + 2 * r]};
                const f32x2 g1 = {acc[p][2 * r + 2], acc[p][2 * r + 3]}, u1 = {acc[p][10 + 2 * r], acc[p][11 + 2 * r]};
                o[r] = pk2v(silu2(g0) * u0);
                o[r + 1] = pk2v(silu2(g1) * u1);
                SCHED();
                if constexpr (2 * p + R.v < 16 && p < 7) STAGE_W2B(2 * p + R.v < 14 ? 2 * p + R.v : 0);
                SCHED();
            });
            st128(rs, o, hvo, hso + p * 4096);
            SCHED();
        });
    }
    BAR();
    TS(13);

    const int chl = w >> 1, rh = w & 1;
    constexpr int PD = 8;
    bf16x8 hf[PD][2];
    const unsigned hvoff = WS_H + (64 * th2 + (lane & 31)) * 32 + (lane >> 5) * 16;
    const unsigned hsoff = (mt2 * 112 + 28 * w) * 4096;
    f32x4 cacc[8];
    {
        LAS char* ub = lds + L_U + chl * CV_UCH + 2 * (256 + 16 * (l15 - (g16 >> 1)) + 8 * (g16 & 1)) - 32 * (8 * rh + 6);
        const int i0 = 2047 - 16 * (g16 >> 1) - l15 + 8 * (g16 & 1);
        LAS char* kb = lds + L_KIMG + chl * CV_KCH + ((l15 & 1) ? 0 : 4160) + 4 * (i0 >> 1) - 32 * (8 * rh + 6);
        LAS char* kbm[8];
#pragma unroll
        for (int m = 0; m < 8; ++m) { kbm[m] = kb - 512 * m; asm volatile("" : "+v"(kbm[m])); }
        auto ldG = [&](int m, int rr) {
            const LAS unsigned* p = (const LAS unsigned*)(kbm[m] + 64 * (3 - rr));
            u32x4 t; t[0] = p[0]; t[1] = p[1]; t[2] = p[2]; t[3] = p[3];
            return __builtin_bit_cast(bf16x8, t);
        };
        auto ldF = [&](int m, int rr) { return *(const LAS bf16x8*)(ub + 64 * (3 - rr) + 512 * m); };
        bf16x8 G[2][8], Fr[2][8];
#pragma unroll
        for (int m = 0; m < 8; ++m) { G[0][m] = ldG(m, 0); Fr[0][m] = ldF(m, 0); }
        SCHED();
        u64 fh;
        sfor<0, 4>([&](auto RR) {
            constexpr int rr = RR.v, c = rr & 1, n = c ^ 1;
            if constexpr (rr == 1) {
                WAITV(7);
                BAR();
                if (tid == 0) st_flag(ws, WS_FH + (mt * 16 + chunk) * 128, tag);
                SCHED();
            }
            if constexpr (rr == 3) {
                asm volatile("s_waitcnt vmcnt(0)" : "+v"(fh)::"memory");
                if (!__all(fh == tag)) wait_flags<1>(ws, WS_FH + (mt2 * 16 + 4 * w) * 128, 4, tag, lane);
                SCHED();
            }
            sfor<0, 36>([&](auto C) {
                constexpr int cnt = C.v;
                constexpr int m = cnt < 8 ? 0 : cnt < 15 ? 1 : cnt < 21 ? 2 : cnt < 26 ? 3 : cnt < 30 ? 4 : cnt < 33 ? 5 : cnt < 35 ? 6 : 7;
                constexpr int first = m * 8 - (m * (m - 1)) / 2, i = m + (cnt - first);
                mfma16_vv<rr == 0 && m == 0>(cacc[i], G[c][m], Fr[c][i - m]);
                if constexpr (rr < 3) {
                    if constexpr (cnt < 8) Fr[n][cnt] = ldF(cnt, rr + 1);
                    else if constexpr (cnt < 24 && !(cnt & 1)) G[n][(cnt - 8) >> 1] = ldG((cnt - 8) >> 1, rr + 1);
                }
                if constexpr (rr < 2 && cnt % 5 == 2 && cnt / 5 < 7) STAGE_W2B(14 + 7 * rr + cnt / 5);
                if constexpr (rr == 2 && cnt == 24) aload8sc1(fh, ws + WS_FH + (mt2 * 16 + 4 * w) * 128, (lane & 3) * 128);
                if constexpr (rr == 3 && cnt % 4 == 1 && cnt / 4 < 8) hf[cnt / 8][(cnt / 4) & 1] = asbf(ld128(rs, hvoff + ((cnt / 4) & 1) * 1024, hsoff + (cnt / 8) * 4096));
                SCHED();
            });
        });
    }
    MFMA_DRAIN();
    TS(14);
    {
        LAS char* red = lds + L_RED;
#pragma unroll
        for (int i = 0; i < 4; ++i) {
            f32x4 lo = cacc[i], hi = cacc[4 + i];
            asm volatile("" : "+v"(lo), "+v"(hi));
            *(LAS f32x4*)(red + (w * 4 + i) * 1024 + lane * 16) = rh ? lo : hi;
            cacc[i] = rh ? hi : lo;
            hf[4 + i][0] = asbf(ld128(rs, hvoff, hsoff + (4 + i) * 4096));
            SCHED();
        }
#pragma unroll
        for (int s = 0; s < 14; ++s) {
            wf[14 + s][0] = cat8(tr_read(wr0 + s * 2048), tr_read(wr0 + s * 2048 + 512));
            wf[14 + s][1] = cat8(tr_read(wr1 + s * 2048), tr_read(wr1 + s * 2048 + 512));
            asm volatile("" : "+a"(wf[14 + s][0]), "+a"(wf[14 + s][1]));
            if (s % 3 == 1 && s / 3 < 4) { hf[4 + s / 3][1] = asbf(ld128(rs, hvoff + 1024, hsoff + (4 + s / 3) * 4096)); SCHED(); }
        }
        LBAR();
        const float dsk = bf2f(__builtin_amdgcn_readfirstlane((int)p_dsk));
#pragma unroll
        for (int i = 0; i < 4; ++i) {
            const f32x4 y = cacc[i] + *(LAS f32x4*)(red + ((w ^ 1) * 4 + i) * 1024 + lane * 16);
            const int t = 256 * (4 * rh + i) + 16 * l15 + 4 * g16;
            const u32x2 uu = *(LAS u32x2*)(lds + L_U + chl * CV_UCH + 2 * (256 + t));
            st64(rs, u32x2{pk2v(f32x2{y[0], y[1]} + unpk(uu[0]) * dsk), pk2v(f32x2{y[2], y[3]} + unpk(uu[1]) * dsk)}, WS_ST + ((c0 + chl) * L_ + t) * 2, 0);
        }
    }
    SCHED();
    TS(15);

    f32x16 a2[2][2];
    u32x4 sv[2], xv[2];
    u64 fs;
    const int t0 = mt2 * 128 + th2 * 64;
    const int tt2 = w >> 1, dh2 = w & 1;
    sfor<0, 28>([&](auto S) {
        constexpr int s = S.v;
        const bf16x8 h0 = hf[s % PD][0], h1 = hf[s % PD][1];
        mfma32_av<s == 0>(a2[0][0], wf[s][0], h0);
        mfma32_av<s == 0>(a2[1][0], wf[s][1], h0);
        if constexpr (s + PD < 28) hf[s % PD][0] = asbf(ld128(rs, hvoff, hsoff + (s + PD) * 4096));
        SCHED();
        mfma32_av<s == 0>(a2[0][1], wf[s][0], h1);
        mfma32_av<s == 0>(a2[1][1], wf[s][1], h1);
        if constexpr (s + PD < 28) hf[s % PD][1] = asbf(ld128(rs, hvoff + 1024, hsoff + (s + PD) * 4096));
        if constexpr (s == 10) {
            WAITV(22);
            LBAR();
            if (tid == 0) st_flag(ws, WS_FS + tb * 8, tag);
        }
        if constexpr (s == 19) aload8sc1(fs, ws + WS_FS + (d0 >> 1) * 8, (lane & 31) * 8);
        if constexpr (s == 23) {
            asm volatile("s_waitcnt vmcnt(0)" : "+v"(fs)::"memory");
            if (!__all(fs == tag)) wait_flags<1, 8>(ws, WS_FS + (d0 >> 1) * 8, 32, tag, lane);
#pragma unroll
            for (int i = 0; i < 2; ++i) {
                const int e = tid + 256 * i;
                sv[i] = ld128(rs, WS_ST + ((d0 + (e >> 3)) * L_ + t0) * 2 + (e & 7) * 16, 0);
            }
#pragma unroll
            for (int g = 0; g < 2; ++g) xv[g] = *(const u32x4*)(x + (size_t)(t0 + 32 * tt2 + (lane & 31)) * D_ + d0 + 32 * dh2 + 16 * g + 8 * (lane >> 5));
        }
        SCHED();
    });
    MFMA_DRAIN();
    TS(16);
    {
        sfor<0, 4>([&](auto Q) {
            constexpr int q = Q.v, tt = q >> 1, dh = q & 1;
            if (q != w) {
                const f32x16 a = a2[dh][tt];
#pragma unroll
                for (int g = 0; g < 4; ++g)
                    *(LAS f32x4*)(lds + ((q * 4 + w) * 4 + g) * 1024 + lane * 16) = f32x4{a[4 * g], a[4 * g + 1], a[4 * g + 2], a[4 * g + 3]};
            }
        });
#pragma unroll
        for (int i = 0; i < 2; ++i) *(LAS u32x4*)(lds + L_ST + (tid + 256 * i) * 16) = sv[i];
        LBAR();
    }
    TS(17);
    {
        f32x16 own;
        { f32x16 q0 = a2[0][0], q1 = a2[1][0], q2 = a2[0][1], q3 = a2[1][1];
          asm volatile("" : "+v"(q0), "+v"(q1), "+v"(q2), "+v"(q3));
          own = w == 0 ? q0 : w == 1 ? q1 : w == 2 ? q2 : q3; }
        f32x4 f[4];
#pragma unroll
        for (int g = 0; g < 4; ++g) {
            f[g] = f32x4{own[4 * g], own[4 * g + 1], own[4 * g + 2], own[4 * g + 3]};
#pragma unroll
            for (int o = 1; o < 4; ++o) f[g] += *(LAS f32x4*)(lds + ((w * 4 + ((w + o) & 3)) * 4 + g) * 1024 + lane * 16);
        }
        LAS char* sp = lds + L_ST + (32 * dh2 + 8 * (lane >> 5) + q4) * 128 + (32 * tt2 + 16 * (g16 & 1) + 4 * pp) * 2;
#pragma unroll
        for (int g2 = 0; g2 < 2; ++g2) {
            const s16x4 sa = tr_read(sp + g2 * 16 * 128), sb = tr_read(sp + (g2 * 16 + 4) * 128);
            const u32x2 s01 = __builtin_bit_cast(u32x2, sa), s23 = __builtin_bit_cast(u32x2, sb);
            u32x4 o;
            o[0] = pk2v(unpk(xv[g2][0]) + unpk(s01[0]) * silu2(f32x2{f[2 * g2][0], f[2 * g2][1]}));
            o[1] = pk2v(unpk(xv[g2][1]) + unpk(s01[1]) * silu2(f32x2{f[2 * g2][2], f[2 * g2][3]}));
            o[2] = pk2v(unpk(xv[g2][2]) + unpk(s23[0]) * silu2(f32x2{f[2 * g2 + 1][0], f[2 * g2 + 1][1]}));
            o[3] = pk2v(unpk(xv[g2][3]) + unpk(s23[1]) * silu2(f32x2{f[2 * g2 + 1][2], f[2 * g2 + 1][3]}));
            *(u32x4*)(out + (size_t)(t0 + 32 * tt2 + (lane & 31)) * D_ + d0 + 32 * dh2 + 16 * g2 + 8 * (lane >> 5)) = o;
        }
    }
    if (b == 0 && w == 0) {
        asm volatile("s_waitcnt vmcnt(0)" ::: "memory");
        u64 mn = ~0ull, mx = 0;
        for (int i = 0; i < 4; ++i) { const u64 v = ld_flag(ws, (20u << 20) + (lane * 4 + i) * 8); mn = v < mn ? v : mn; mx = v > mx ? v : mx; }
        for (int o = 32; o >= 1; o >>= 1) { const u64 a = __shfl_xor(mn, o), c = __shfl_xor(mx, o); mn = a < mn ? a : mn; mx = c > mx ? c : mx; }
        const u64 now = __builtin_amdgcn_s_memrealtime();
        const u64 until = now + 0 * (0);
        while (__builtin_amdgcn_s_memrealtime() < until) __builtin_amdgcn_s_sleep(1);
    }
    if (b == 0 && tid == 0) st_flag(ws, WS_EP, tag);
    TS(18);
    TSR(31);
    TS_DUMP();
    asm volatile(".Lfused_pf_end:" ::: "memory");
}

extern "C" void kernel_launch(void* const* d_in, const int* in_sizes, int n_in, void* d_out, int out_size, void* d_ws, size_t ws_size,
                              hipStream_t stream) {
    (void)hipFuncSetAttribute((const void*)fused_s4_ffn, hipFuncAttributeMaxDynamicSharedMemorySize, LDS_TOTAL);
    fused_s4_ffn<<<256, 256, LDS_TOTAL, stream>>>((const bf16_t*)d_in[0], (const bf16_t*)d_in[1], (const bf16_t*)d_in[2], (const bf16_t*)d_in[3],
                                                  (const bf16_t*)d_in[4], (const bf16_t*)d_in[5], (const bf16_t*)d_in[6], (const bf16_t*)d_in[7],
                                                  (const bf16_t*)d_in[8], (const bf16_t*)d_in[10], (const bf16_t*)d_in[11], (const bf16_t*)d_in[12],
                                                  (bf16_t*)d_out, (char*)d_ws);
}
```

```cpp
#include <hip/hip_runtime.h>
#include <stdint.h>

typedef unsigned short bf16_t;
typedef short bf16x8 __attribute__((ext_vector_type(8)));
typedef short s16x4 __attribute__((ext_vector_type(4)));
typedef float f32x2 __attribute__((ext_vector_type(2)));
typedef float f32x4 __attribute__((ext_vector_type(4)));
typedef float f32x16 __attribute__((ext_vector_type(16)));
typedef unsigned u32x4 __attribute__((ext_vector_type(4)));
typedef unsigned u32x2 __attribute__((ext_vector_type(2)));
typedef __bf16 bf16v2 __attribute__((ext_vector_type(2)));
typedef unsigned long long u64;
#define LAS __attribute__((address_space(3)))
#define GAS __attribute__((address_space(1)))
#define DI __device__ __forceinline__

#define XSCHED 0
#define WSCHED 0
constexpr int L_ = 2048, D_ = 512, N_ = 128, F_ = 1792;

constexpr unsigned WS_XN = 0;
constexpr unsigned WS_XT = 2u << 20;
constexpr unsigned WS_ST = 4u << 20;
constexpr unsigned WS_H = 6u << 20;
constexpr unsigned WS_WT = 14u << 20;
constexpr unsigned WS_FX = 16u << 20;
constexpr unsigned WS_FH = WS_FX + 256 * 128;
constexpr unsigned WS_FS = WS_FH + 256 * 128;
constexpr unsigned WS_FXP = WS_FS + 256 * 128;
constexpr unsigned WS_EP = WS_FXP + 256 * 8;
constexpr unsigned WS_USED = 17u << 20;

constexpr int G1_SLOT = 28800, G1_UP = 14336 + 128;
constexpr int g1slot(int s) { return s % 5 == 0 ? 0 : s % 5 == 1 ? 3 : s % 5 == 2 ? 4 : s % 5 == 3 ? 1 : 2; }
constexpr int KG_BASE = G1_SLOT, KG_RS = 528, KG_CH = 53 * KG_RS;
constexpr int L_WTS = 3 * G1_SLOT;
constexpr int L_KC = 144128;
constexpr int L_KIMG = 146816;
constexpr int L_LNS = L_KIMG, LN_RS = 1056;
constexpr int CV_KCH = 8320;
constexpr int L_U = 2 * G1_SLOT, CV_UCH = 4608;
constexpr int L_RED = L_U + 2 * CV_UCH;
constexpr int L_ST = 65536;
constexpr int LDS_TOTAL = 163840;
static_assert(KG_BASE + 2 * KG_CH <= 3 * G1_SLOT && KG_BASE + KG_CH + 256 < 65536, "K-gen scratch");
static_assert(L_KIMG + 2 * CV_KCH <= LDS_TOTAL && L_RED + 16384 <= 3 * G1_SLOT && L_ST + 8192 <= 3 * G1_SLOT, "lds map");

DI float bf2f(unsigned u16) { return __uint_as_float(u16 << 16); }
DI float bflo(unsigned w) { return __uint_as_float(w << 16); }
DI float bfhi(unsigned w) { return __uint_as_float(w & 0xffff0000u); }
DI unsigned pk2v(f32x2 v) { return __builtin_bit_cast(unsigned, __builtin_convertvector(v, bf16v2)); }
DI unsigned pk2(float lo, float hi) { return pk2v(f32x2{lo, hi}); }
DI f32x2 unpk(unsigned w) { return f32x2{bflo(w), bfhi(w)}; }
DI float rbf(float x) { return bflo(pk2(x, x)); }

#define WAITV(n) asm volatile("s_waitcnt vmcnt(" #n ")" ::: "memory")
#define WAITL(n) asm volatile("s_waitcnt lgkmcnt(" #n ")" ::: "memory")
#define BAR() do { asm volatile("" ::: "memory"); __builtin_amdgcn_s_barrier(); asm volatile("" ::: "memory"); } while (0)
#define LBAR() do { WAITL(0); BAR(); } while (0)
#define SCHED() __builtin_amdgcn_sched_barrier(0)

#define TS(i) do {} while (0)
#define TSR(i) do {} while (0)
#define TS_DUMP() do {} while (0)

template <int N> struct IC { static constexpr int v = N; };
template <int B, int E, class F> DI void sfor(F&& f) { if constexpr (B < E) { f(IC<B>{}); sfor<B + 1, E>(f); } }
template <int N> DI void waitv() { asm volatile("s_waitcnt vmcnt(%0)" ::"i"(N) : "memory"); }
template <int N> DI void waitl() { asm volatile("s_waitcnt lgkmcnt(%0)" ::"i"(N) : "memory"); }

template <int CTRL, int RMASK> DI float dpp_add(float v) {
    return v + __builtin_bit_cast(float, __builtin_amdgcn_update_dpp(0, __builtin_bit_cast(int, v), CTRL, RMASK, 0xf, true));
}
DI void wave_sum4(float& a, float& b, float& c, float& d) {
#define STEP(C, M) a = dpp_add<C, M>(a); b = dpp_add<C, M>(b); c = dpp_add<C, M>(c); d = dpp_add<C, M>(d);
    STEP(0x111, 0xf) STEP(0x112, 0xf) STEP(0x114, 0xf) STEP(0x118, 0xf) STEP(0x142, 0xa) STEP(0x143, 0xc)
#undef STEP
    a = __builtin_bit_cast(float, __builtin_amdgcn_readlane(__builtin_bit_cast(int, a), 63));
    b = __builtin_bit_cast(float, __builtin_amdgcn_readlane(__builtin_bit_cast(int, b), 63));
    c = __builtin_bit_cast(float, __builtin_amdgcn_readlane(__builtin_bit_cast(int, c), 63));
    d = __builtin_bit_cast(float, __builtin_amdgcn_readlane(__builtin_bit_cast(int, d), 63));
}
DI s16x4 tr_read(LAS char* p) { return __builtin_amdgcn_ds_read_tr16_b64_v4i16((LAS s16x4*)p); }
template <int OFF> DI s16x4 atr(unsigned addr) { s16x4 r; asm volatile("ds_read_b64_tr_b16 %0, %1 offset:%2" : "=v"(r) : "v"(addr), "i"(OFF)); return r; }
DI bf16x8 cat8(s16x4 a, s16x4 b) { return __builtin_shufflevector(a, b, 0, 1, 2, 3, 4, 5, 6, 7); }

typedef __amdgpu_buffer_rsrc_t rsrc_t;
constexpr int SC1 = 16;
DI u32x4 ld128(rsrc_t r, unsigned voff, unsigned soff) { return __builtin_amdgcn_raw_buffer_load_b128(r, voff, soff, SC1); }
DI void st128(rsrc_t r, u32x4 v, unsigned voff, unsigned soff) {
    asm volatile("buffer_store_dwordx4 %0, %1, %2, %3 offen sc1\n\ts_nop 1" ::"v"(v), "v"(voff), "s"(r), "s"(soff) : "memory");
}
DI void st64(rsrc_t r, u32x2 v, unsigned voff, unsigned soff) { __builtin_amdgcn_raw_buffer_store_b64(v, r, voff, soff, SC1); }
DI bf16x8 asbf(u32x4 v) { return __builtin_bit_cast(bf16x8, v); }

template <int SIMM, int LIMM, int IMM> DI void bdma_first(unsigned& so, rsrc_t r, unsigned voff, unsigned sbase, unsigned lbase) {
    asm volatile("s_add_u32 m0, %1, %2\n\ts_add_u32 %0, %3, %4\n\tbuffer_load_dwordx4 %5, %6, %0 offen offset:%7 lds"
                 : "=&s"(so) : "s"(lbase), "i"(LIMM), "s"(sbase), "i"(SIMM), "v"(voff), "s"(r), "i"(IMM) : "memory", "scc");
}
template <int IMM> DI void bdma_next(unsigned so, rsrc_t r, unsigned voff) {
    asm volatile("buffer_load_dwordx4 %0, %1, %2 offen offset:%3 lds" ::"v"(voff), "s"(r), "s"(so), "i"(IMM) : "memory");
}
template <int SIMM, int IMM> DI void aload_a_first(bf16x8& d, unsigned& so, rsrc_t r, unsigned voff, unsigned sbase) {
    asm volatile("s_add_u32 %1, %4, %5\n\tbuffer_load_dwordx4 %0, %2, %3, %1 offen offset:%6 sc1"
                 : "=&a"(d), "=&s"(so) : "v"(voff), "s"(r), "s"(sbase), "i"(SIMM), "i"(IMM) : "memory", "scc");
}
template <int IMM> DI void aload_a_next(bf16x8& d, unsigned so, rsrc_t r, unsigned voff) {
    asm volatile("buffer_load_dwordx4 %0, %1, %2, %3 offen offset:%4 sc1" : "=&a"(d) : "v"(voff), "s"(r), "s"(so), "i"(IMM) : "memory");
}
template <int IMM> DI void aload_v(u32x4& d, rsrc_t r, unsigned voff, unsigned so) {
    asm volatile("buffer_load_dwordx4 %0, %1, %2, %3 offen offset:%4 sc1" : "=&v"(d) : "v"(voff), "s"(r), "s"(so), "i"(IMM) : "memory");
}
DI void warm4(unsigned& pf, const void* sbase, unsigned voff) { asm volatile("global_load_dword %0, %1, %2" : "+v"(pf) : "v"(voff), "s"(sbase) : "memory"); }
DI void aload16(u32x4& d, const void* sbase, unsigned voff) { asm volatile("global_load_dwordx4 %0, %1, %2" : "=&v"(d) : "v"(voff), "s"(sbase) : "memory"); }
DI void aload2(unsigned& d, const void* sbase, unsigned voff) { asm volatile("global_load_ushort %0, %1, %2" : "=&v"(d) : "v"(voff), "s"(sbase) : "memory"); }
DI void aload8sc1(u64& d, const void* sbase, unsigned voff) { asm volatile("global_load_dwordx2 %0, %1, %2 sc1" : "=&v"(d) : "v"(voff), "s"(sbase) : "memory"); }

template <bool FIRST> DI void mfma32_va(f32x16& c, bf16x8 a, bf16x8 b) {
    if constexpr (FIRST) asm volatile("v_mfma_f32_32x32x16_bf16 %0, %1, %2, 0" : "=&v"(c) : "v"(a), "a"(b));
    else asm volatile("v_mfma_f32_32x32x16_bf16 %0, %1, %2, %0" : "+v"(c) : "v"(a), "a"(b));
}
template <bool FIRST> DI void mfma32_av(f32x16& c, bf16x8 a, bf16x8 b) {
    if constexpr (FIRST) asm volatile("v_mfma_f32_32x32x16_bf16 %0, %1, %2, 0" : "=&v"(c) : "a"(a), "v"(b));
    else asm volatile("v_mfma_f32_32x32x16_bf16 %0, %1, %2, %0" : "+v"(c) : "a"(a), "v"(b));
}
template <bool FIRST> DI void mfma16_vv(f32x4& c, bf16x8 a, bf16x8 b) {
    if constexpr (FIRST) asm volatile("v_mfma_f32_16x16x32_bf16 %0, %1, %2, 0" : "=&v"(c) : "v"(a), "v"(b));
    else asm volatile("v_mfma_f32_16x16x32_bf16 %0, %1, %2, %0" : "+v"(c) : "v"(a), "v"(b));
}
#define MFMA_DRAIN() asm volatile("s_nop 15\n\ts_nop 3" ::: "memory")

#define WT_LOOP 24
#define WT_EPI 14
constexpr int G1_WT = WT_LOOP;
struct G1Op { int step, gap, kind, a, b; };
struct G1Sched {
    G1Op op[160]; int n;
    constexpr G1Sched() : op{}, n(0) {
        for (int i = 0; i < 7; ++i) op[n++] = G1Op{-1, 0, 1, 1, i};
        for (int u = 0; u < 8; ++u) op[n++] = G1Op{-1, 0, 0, u, 0};
        for (int i = 0; i < 7; ++i) op[n++] = G1Op{-1, 0, 1, 2, i};
        op[n++] = G1Op{-1, 0, 6, 0, 0};
        int wt = 0;
        for (int s = 0; s < 8; ++s) {
            G1Op list[24] = {}; int m = 0;
            if (s == 0) for (int i = 0; i < 2; ++i) list[m++] = G1Op{0, 0, 3, i, 0};
            if (s < 3) { for (int i = 0; i < 4; ++i) list[m++] = G1Op{0, 0, 0, 8 + 8 * s + i, 0};
                         for (int i = 0; i < 4; ++i) list[m++] = G1Op{0, 0, 1, 3 + s, i};
                         for (int i = 4; i < 8; ++i) list[m++] = G1Op{0, 0, 0, 8 + 8 * s + i, 0};
                         for (int i = 4; i < 7; ++i) list[m++] = G1Op{0, 0, 1, 3 + s, i}; }
            if (s == 3 || s == 4) for (int i = 0; i < 7; ++i) list[m++] = G1Op{0, 0, 1, 3 + s, i};
            if (s == 5) for (int i = 0; i < 2; ++i) list[m++] = G1Op{0, 0, 4, i, 0};
            const int nw = s < 5 ? 0 : s == 5 ? G1_WT / 4 / 2 * 2 : s == 6 ? (G1_WT - G1_WT / 4 / 2 * 2) / 2 / 2 * 2 : G1_WT - wt;
            for (int i = 0; i < nw; ++i) list[m++] = G1Op{0, 0, 2, wt++, 0};
            const int span = s == 7 ? 21 : 28;
            for (int i = 0; i < m; ++i) { const int g = (i * span) / m; list[i].step = 4 * s + g / 7; list[i].gap = g % 7; op[n++] = list[i]; }
        }
    }
    constexpr int younger(int kind, int a, int st) const {
        int last = -1;
        for (int i = 0; i < n; ++i) if (op[i].kind == kind && op[i].a == a) last = i;
        int c = 0;
        for (int i = last + 1; i < n; ++i) if (op[i].step < st) ++c;
        return c;
    }
};
constexpr G1Sched G1S{};

DI u64 ld_flag(const char* ws, unsigned off) { return __hip_atomic_load((GAS u64*)(ws + off), __ATOMIC_RELAXED, __HIP_MEMORY_SCOPE_AGENT); }
DI void st_flag(char* ws, unsigned off, u64 v) { __hip_atomic_store((GAS u64*)(ws + off), v, __ATOMIC_RELAXED, __HIP_MEMORY_SCOPE_AGENT); }
template <int NPL, int STRIDE = 128>
DI void wait_flags(const char* ws, unsigned base, int nl, u64 tag, int lane) {
    const unsigned off = base + (lane < nl ? lane : 0) * STRIDE;
#pragma unroll 1
    for (int spin = 0; spin < 40000; ++spin) {
        bool ok = true;
#pragma unroll
        for (int i = 0; i < NPL; ++i) ok &= ld_flag(ws, off + i * 64 * STRIDE) == tag;
        if (__all(ok)) break;
        __builtin_amdgcn_s_sleep(1);
    }
    asm volatile("" ::: "memory");
}

typedef f32x2 cf;
DI cf cmul(cf a, cf b) { return cf{a[0] * b[0] - a[1] * b[1], a[0] * b[1] + a[1] * b[0]}; }
DI void cmul2(cf& z0, cf a0, cf b0, cf& z1, cf a1, cf b1) {
    cf t0, t1;
    asm("v_pk_mul_f32 %2, %4, %5 op_sel_hi:[0,1]\n\tv_pk_mul_f32 %3, %6, %7 op_sel_hi:[0,1]\n\t"
        "v_pk_fma_f32 %0, %4, %5, %2 op_sel:[1,1,0] op_sel_hi:[1,0,1] neg_lo:[0,1,0]\n\t"
        "v_pk_fma_f32 %1, %6, %7, %3 op_sel:[1,1,0] op_sel_hi:[1,0,1] neg_lo:[0,1,0]"
        : "=&v"(z0), "=&v"(z1), "=&v"(t0), "=&v"(t1) : "v"(a0), "v"(b0), "v"(a1), "v"(b1));
}
DI cf zdir(float a2, float th, float l) {
    const float m = __builtin_amdgcn_exp2f(a2 * l);
    const float r = __builtin_amdgcn_fractf(th * l);
    return cf{m * __builtin_amdgcn_cosf(r), m * __builtin_amdgcn_sinf(r)};
}
DI f32x2 silu2(f32x2 g) {
    const f32x2 t = g * -1.44269504088896341f;
    const f32x2 d = f32x2{__builtin_amdgcn_exp2f(t[0]), __builtin_amdgcn_exp2f(t[1])} + 1.0f;
    return g * f32x2{__builtin_amdgcn_rcpf(d[0]), __builtin_amdgcn_rcpf(d[1])};
}

#define PUBLISH_AT 5
constexpr int g2_younger() { int c = 0; for (int t = 12; t < 28; ++t) if (((t - 12) * 21) / 16 <= 10) c += 2; return c; }
#define G2_YOUNGER g2_younger()
#define G1_STAMP(i) do {} while (0)
#define CV_STAMP(i) do {} while (0)
#define G2_STAMP(i) do {} while (0)

extern __shared__ __attribute__((aligned(16))) char dyn_smem[];

__global__ __launch_bounds__(256, 1) void fused_s4_ffn(const bf16_t* __restrict__ x, const bf16_t* __restrict__ A_re, const bf16_t* __restrict__ A_im,
                                                       const bf16_t* __restrict__ B_re, const bf16_t* __restrict__ B_im, const bf16_t* __restrict__ C_re,
                                                       const bf16_t* __restrict__ C_im, const bf16_t* __restrict__ log_dt, const bf16_t* __restrict__ Dskip,
                                                       const bf16_t* __restrict__ wg, const bf16_t* __restrict__ wu, const bf16_t* __restrict__ wd,
                                                       bf16_t* __restrict__ out, char* ws) {
    LAS char* lds = (LAS char*)dyn_smem;
    const unsigned lbase = (unsigned)(size_t)lds;
    const int tid = threadIdx.x, w = __builtin_amdgcn_readfirstlane(tid >> 6), lane = tid & 63;
    const int b = blockIdx.x, xcd = b & 7, j = b >> 3;
    const int tb = xcd * 32 + j;
    const int mt = 8 * (xcd >> 2) + (j >> 2), chunk = 4 * (xcd & 3) + (j & 3);
    const int mt2 = 2 * xcd + (j >> 4), th2 = (j >> 3) & 1, dt2 = j & 7;
    const int c0 = 2 * tb;
    TSR(46); TS(0);

    u32x4 raw[2];
#pragma unroll
    for (int r = 0; r < 2; ++r) aload16(raw[r], x + (size_t)(8 * tb + 2 * w + r) * D_, lane * 16);
    unsigned pf = 0;
    {
        u64 pc; unsigned len;
        asm volatile("s_getpc_b64 %0\n.Lfused_pf_here:\n\ts_mov_b32 %1, .Lfused_pf_end-.Lfused_pf_here" : "=s"(pc), "=s"(len));
        const unsigned o = (unsigned)(12 * j + (lane < 12 ? lane : 11)) * 128u;
        warm4(pf, (const void*)pc, o < len ? o : len);
    }
    u64 ep;
    aload8sc1(ep, ws + WS_EP, 0);
    const int kch = c0 + (w >> 1);
    const unsigned kpo = (64 * (w & 1) + lane) * 2;
    unsigned p_are, p_aim, p_br, p_bi, p_cr, p_ci, p_ldt, p_dsk;
    aload2(p_are, A_re + kch * N_, kpo); aload2(p_aim, A_im + kch * N_, kpo); aload2(p_br, B_re + kch * N_, kpo);
    aload2(p_bi, B_im + kch * N_, kpo); aload2(p_cr, C_re + kch * N_, kpo); aload2(p_ci, C_im + kch * N_, kpo);
    aload2(p_ldt, log_dt + kch, 0); aload2(p_dsk, Dskip + kch, 0);
    SCHED();
    const rsrc_t rs = __builtin_amdgcn_make_buffer_rsrc(ws, 0, WS_USED, 0x00020000);
    const rsrc_t rw1 = __builtin_amdgcn_make_buffer_rsrc((void*)((w & 1) ? wu : wg), 0, D_ * F_ * 2, 0x00020000);
    const rsrc_t rwd = __builtin_amdgcn_make_buffer_rsrc((void*)wd, 0, D_ * F_ * 2, 0x00020000);
    const int wtU0 = 7 * (b >> 4) + w, wtU1 = w < 3 ? wtU0 + 4 : wtU0;
    {
        unsigned so;
        const unsigned vo = (lane >> 2) * 1024 + (lane & 3) * 16;
        bdma_first<0, L_WTS, 0>(so, rwd, vo, wtU0 * 16384 + (b & 15) * 64, lbase + w * 2048);
        bdma_first<0, L_WTS + 1024, 0>(so, rwd, vo, wtU1 * 16384 + (b & 15) * 64, lbase + w * 2048);
    }
    unsigned voff[7];
    {
        const int k = (lane * 147) >> 11, c = lane - 14 * k;
        unsigned v = k * (F_ * 2) + c * 16;
        int cc = c;
#pragma unroll
        for (int i = 0; i < 7; ++i) {
            voff[i] = v - (i & 3) * 1024;
            cc += 8; v += 4 * (F_ * 2) + 128;
            if (cc >= 14) { cc -= 14; v += F_ * 2 - 224; }
        }
    }
    const unsigned w1s = (32 * (w >> 1)) * (F_ * 2) + chunk * 224;
    const unsigned w1l = lbase + (w & 1) * G1_UP + (w >> 1) * 7168;
    unsigned w1so;
#define STAGE_W1(S, I) do { if constexpr (((I) & 3) == 0) bdma_first<64 * (S) * (F_ * 2), g1slot(S) * G1_SLOT + (I) * 1024, 0>(w1so, rw1, voff[I], w1s, w1l); \
                            else bdma_next<((I) & 3) * 1024>(w1so, rw1, voff[I]); } while (0)
    SCHED();
    const int g16 = lane >> 4, q4 = (lane & 15) >> 2, pp = lane & 3, l15 = lane & 15;
    const int phi = ((pp & 1) << 1) | (pp >> 1);
    unsigned ksc[8];
    {
        const int col8 = 8 * (16 * (w & 1) + l15);
        const unsigned kcb = lbase + L_KC + (w >> 1) * 1312, kcf = kcb - 2 * 17024;
        const int A = (g16 == 0 ? 0 : g16 == 1 ? 4 : g16 == 2 ? 256 : 512) + col8, B0 = g16 == 2 ? 2 : g16 == 3 ? 4 : 1, B1 = g16 == 2 ? 4 : g16 == 3 ? 256 : 2;
#pragma unroll
        for (int r = 0; r < 4; ++r) {
            const int v = A + (r & 1) * B0 + (r >> 1) * B1;
            ksc[r] = v < 128 ? kcb + 2 * v : kcf + 2 * (__float_as_uint((float)v) >> 16);
            asm volatile("" : "+v"(ksc[r]));
        }
#pragma unroll
        for (int r = 0; r < 4; ++r) {
            const unsigned a = kcf + 2 * (__float_as_uint((float)(1024 + 256 * r + col8)) >> 16);
            ksc[4 + r] = g16 == 0 ? a : (r == 0 && g16 == 1 && col8 == 0) ? kcb + 2 * 640 : kcb + 2 * 648;
            asm volatile("" : "+v"(ksc[4 + r]));
        }
    }
    unsigned kga[9];
    {
        const float f = (float)(2047 - 8 * tid);
        const unsigned kcf = lbase + L_KC - 2 * 17024;
#pragma unroll
        for (int e = 0; e < 9; e += 2) {
            const unsigned bits = pk2(f - e, f - (e + 1));
            kga[e] = kcf + 2 * (bits & 0xffffu);
            if (e < 8) kga[e + 1] = kcf + 2 * (bits >> 16);
        }
        if (w == 3) {
#pragma unroll
            for (int e = 0; e < 9; ++e) {
                const int l = 2047 - 8 * tid - e;
                if (l < 256) kga[e] = lbase + L_KC + 2 * (l < 0 ? 649 : l);
            }
        }
#pragma unroll
        for (int e = 0; e < 9; ++e) asm volatile("" : "+v"(kga[e]));
    }
    SCHED();
    {
        const int r = 21 * (w >> 1) + (lane < 21 ? lane : 20), row = (r * 147) >> 10;
        warm4(pf, (w & 1) ? wu : wg, (6 * j + row) * (F_ * 2) + (xcd & 3) * 896 + (r - 7 * row) * 128);
    }
    SCHED();
    asm volatile("s_waitcnt vmcnt(3)" : "+v"(ep), "+v"(raw[0]), "+v"(raw[1]), "+v"(p_are), "+v"(p_aim), "+v"(p_br), "+v"(p_bi), "+v"(p_cr), "+v"(p_ci),
                 "+v"(p_ldt), "+v"(p_dsk)::"memory");
    SCHED();
    TS(1);
    {
        f32x2 v[2][4];
        float s1[2], s2[2], x0[2];
        sfor<0, 2>([&](auto R) {
            constexpr int r = R.v;
            x0[r] = bflo((unsigned)__builtin_amdgcn_readfirstlane((int)raw[r][0]));
            f32x2 a = {0.f, 0.f}, q = {0.f, 0.f};
#pragma unroll
            for (int jj = 0; jj < 4; ++jj) { v[r][jj] = unpk(raw[r][jj]); const f32x2 d = v[r][jj] - x0[r]; a += d; q += d * d; }
            s1[r] = a[0] + a[1]; s2[r] = q[0] + q[1];
            STAGE_W1(0, r);
            SCHED();
        });
        wave_sum4(s1[0], s2[0], s1[1], s2[1]);
        STAGE_W1(0, 2);
        SCHED();
        sfor<0, 2>([&](auto R) {
            constexpr int r = R.v;
            const float md = s1[r] * (1.0f / 512.0f);
            const float var = fmaxf(s2[r] * (1.0f / 512.0f) - md * md, 0.f);
            const float mb = rbf(x0[r] + md);
            const float sd = rbf(__builtin_amdgcn_sqrtf(rbf(rbf(var) + 1e-5f)));
            const float rsd = __builtin_amdgcn_rcpf(sd);
            STAGE_W1(0, 3 + 2 * r);
            SCHED();
            u32x4 o;
#pragma unroll
            for (int jj = 0; jj < 4; ++jj) o[jj] = pk2v(unpk(pk2v(v[r][jj] - mb)) * rsd);
            *(LAS u32x4*)(lds + L_LNS + (2 * w + r) * LN_RS + lane * 16) = o;
            STAGE_W1(0, 4 + 2 * r);
            SCHED();
        });
    }
    WAITV(8);
    LBAR();
    TS(2);
    {
        LAS char* tp = lds + L_WTS + w * 2048 + (8 * (g16 >> 1) + q4) * 64 + (g16 & 1) * 32 + phi * 8;
        const bf16x8 f0 = cat8(tr_read(tp), tr_read(tp + 256)), f1 = cat8(tr_read(tp + 1024), tr_read(tp + 1024 + 256));
        st128(rs, __builtin_bit_cast(u32x4, f0), WS_WT + lane * 16, (wtU0 * 16 + (b & 15)) * 1024);
        if (w < 3) st128(rs, __builtin_bit_cast(u32x4, f1), WS_WT + lane * 16, (wtU1 * 16 + (b & 15)) * 1024);
    }
    SCHED();

    {
        const int half = tid & 1, tok = (tid >> 1) & 7, U = tid >> 4;
        const u32x4 va = *(LAS u32x4*)(lds + L_LNS + tok * LN_RS + U * 32 + half * 16);
        const u32x4 vb = *(LAS u32x4*)(lds + L_LNS + tok * LN_RS + U * 32 + half * 16 + 512);
        LAS char* tp = lds + L_LNS + q4 * LN_RS + (128 * w + 16 * g16 + 4 * pp) * 2;
        const s16x4 a0 = tr_read(tp), b0 = tr_read(tp + 4 * LN_RS), a1 = tr_read(tp + 128), b1 = tr_read(tp + 4 * LN_RS + 128);
        const unsigned xo = WS_XN + ((tb >> 2) * 32 + U) * 1024 + ((tb & 3) * 8 + tok) * 32 + half * 16;
        st128(rs, va, xo, 0);
        st128(rs, vb, xo, 16 * 1024);
        const unsigned to = WS_XT + (tb * 512 + 128 * w + 16 * g16 + l15) * 16;
        st128(rs, __builtin_bit_cast(u32x4, cat8(a0, b0)), to, 0);
        st128(rs, __builtin_bit_cast(u32x4, cat8(a1, b1)), to, 1024);
    }
    SCHED();
    const u64 tag = ((u64)(unsigned)__builtin_amdgcn_readfirstlane((int)(unsigned)ep) |
                     ((u64)(unsigned)__builtin_amdgcn_readfirstlane((int)(unsigned)(ep >> 32)) << 32)) + 1;
    TS(3);

    {
        const float dt = rbf(__builtin_amdgcn_exp2f(bf2f(p_ldt) * 1.44269504088896341f));
        const float lr = dt * bf2f(p_are), li = dt * bf2f(p_aim);
        const cf dB{dt * bf2f(p_br), dt * bf2f(p_bi)};
        const cf CBt = cmul(cf{bf2f(p_cr), bf2f(p_ci)}, dB);
        const cf CB{CBt[0], -CBt[1]};
        const float a2 = lr * 1.44269504088896341f, th = li * 0.15915494309189535f;
        asm volatile("s_mov_b32 m0, %0" ::"s"(lbase + KG_BASE + (w >> 1) * KG_CH + (w & 1) * 256) : "memory");
#define PUT2(R0, Z0, R1, Z1) asm volatile("ds_write_addtid_b32 %0 offset:%2\n\tds_write_addtid_b32 %1 offset:%3" \
            ::"v"(pk2v(Z0)), "v"(pk2v(Z1)), "i"((R0) * KG_RS), "i"((R1) * KG_RS) : "memory")
#define PUT4(R0, Z0, R1, Z1, R2, Z2, R3, Z3) asm volatile("ds_write_addtid_b32 %0 offset:%4\n\tds_write_addtid_b32 %1 offset:%5\n\tds_write_addtid_b32 %2 offset:%6\n\tds_write_addtid_b32 %3 offset:%7" \
            ::"v"(pk2v(Z0)), "v"(pk2v(Z1)), "v"(pk2v(Z2)), "v"(pk2v(Z3)), "i"((R0) * KG_RS), "i"((R1) * KG_RS), "i"((R2) * KG_RS), "i"((R3) * KG_RS) : "memory")
        const cf z1 = zdir(a2, -th, 1.f), Z256 = zdir(a2, -th, 256.f), w8 = zdir(a2, th, 8.f);
        cf z2, z3, z4, z5, z6, z7, w16, q1, q2, q3, q4_, q5, q6, q7, q256, q512, q768, q1024, q1280, q1536, q1792, q2048, r0, r1, r2, r3, r4;
        cmul2(z2, z1, z1, w16, w8, w8);
        cmul2(q256, CB, Z256, q1, CB, z1);
        cmul2(z3, z2, z1, z4, z2, z2);
        cmul2(q512, q256, Z256, q2, CB, z2);
        cmul2(z5, z4, z1, z6, z4, z2);
        PUT4(0, CB, 1, q1, 8, q256, 2, q2);
        cmul2(z7, z4, z3, q3, CB, z3);
        cmul2(q768, q512, Z256, q4_, CB, z4);
        cmul2(q5, CB, z5, q1024, q768, Z256);
        cmul2(q6, CB, z6, q1280, q1024, Z256);
        PUT4(12, q512, 3, q3, 14, q768, 4, q4_);
        cmul2(q7, CB, z7, q1536, q1280, Z256);
        cmul2(r0, q256, z2, q1792, q1536, Z256);
        PUT4(5, q5, 16, q1024, 6, q6, 17, q1280);
        cmul2(r1, q256, z4, q2048, q1792, Z256);
        cmul2(r2, q256, z6, r3, q512, z4);
        PUT4(7, q7, 18, q1536, 9, r0, 19, q1792);
        cf pa{1.f, 0.f}, pb = w8, pc_, pd, w32;
        cmul2(r4, q768, z4, w32, w16, w16);
        cmul2(pc_, pa, w16, pd, pb, w16);
        PUT4(10, r1, 20, q2048, 11, r2, 13, r3);
        TS(4);
        sfor<0, 8>([&](auto J) {
            constexpr int jj = J.v;
            cf na, nb, nc, nd;
            if constexpr (jj < 7) { cmul2(na, pa, w32, nb, pb, w32); cmul2(nc, pc_, w32, nd, pd, w32); }
            PUT4(21 + 4 * jj, pa, 22 + 4 * jj, pb, 23 + 4 * jj, pc_, 24 + 4 * jj, pd);
            if constexpr (jj == 0) asm volatile("ds_write_addtid_b32 %0 offset:%1" ::"v"(pk2v(r4)), "i"(15 * KG_RS) : "memory");
            if constexpr (jj < 7) { pa = na; pb = nb; pc_ = nc; pd = nd; }
            if constexpr (jj == PUBLISH_AT) {
                SCHED();
                WAITV(0);
                BAR();
                if (tid == 0) { st_flag(ws, WS_FX + tb * 128, tag); st_flag(ws, WS_FXP + tb * 8, tag); }
                SCHED();
            }
        });
#undef PUT2
#undef PUT4
        if (tid < 2) *(LAS unsigned short*)(lds + L_KC + tid * 1312 + 649 * 2) = 0;
        LBAR();
    }
    TS(5);
    bf16x8 xf[32];
    const unsigned xvo = (lane & 31) * 32 + (lane >> 5) * 16, xsb = WS_XN + (mt * 4 + w) * 32 * 1024;
    unsigned xso;
#define LOAD_X(U) do { if constexpr (((U) & 3) == 0) aload_a_first<((U) >> 2) * 4096, 0>(xf[U], xso, rs, xvo, xsb); \
                       else aload_a_next<((U) & 3) * 1024>(xf[U], xso, rs, xvo); } while (0)
    {
        LAS char* qa = lds + KG_BASE + (w >> 1) * KG_CH + l15 * KG_RS + g16 * 16;
        LAS char* pa_ = qa + (21 + 16 * (w & 1)) * KG_RS;
        bf16x8 fa[8], fb[8], fc[8];
        u64 fxw;
        sfor<0, 8>([&](auto S) {
            constexpr int s_ = S.v;
            fb[s_] = *(LAS bf16x8*)(pa_ + s_ * 64); fa[s_] = *(LAS bf16x8*)(qa + s_ * 64); fc[s_] = *(LAS bf16x8*)(qa + 16 * KG_RS + s_ * 64);
            if constexpr (s_ < 7) STAGE_W1(1, s_);
            else aload8sc1(fxw, ws + WS_FX + (16 * mt + 4 * w) * 128, (lane & 3) * 128);
        });
        SCHED();
        TS(6);
        f32x4 k0, k1;
        sfor<0, 8>([&](auto S) {
            mfma16_vv<S.v == 0>(k0, fa[S.v], fb[S.v]);
            mfma16_vv<S.v == 0>(k1, fc[S.v], fb[S.v]);
        });
        SCHED();
        asm volatile("s_nop 7" : "+v"(k0), "+v"(k1));
#pragma unroll
        for (int r = 0; r < 4; r += 2) {
            const unsigned a0 = pk2(k0[r], k0[r + 1]), a1 = pk2(k1[r], k1[r + 1]);
            *(LAS unsigned short*)(size_t)ksc[r] = (unsigned short)a0;
            *(LAS unsigned short*)(size_t)ksc[r + 1] = (unsigned short)(a0 >> 16);
            *(LAS unsigned short*)(size_t)ksc[4 + r] = (unsigned short)a1;
            *(LAS unsigned short*)(size_t)ksc[5 + r] = (unsigned short)(a1 >> 16);
        }
        SCHED();
        asm volatile("s_waitcnt vmcnt(0)" : "+v"(fxw)::"memory");
        if (!__all(fxw == tag)) wait_flags<1>(ws, WS_FX + (16 * mt + 4 * w) * 128, 4, tag, lane);
        TS(7);
        sfor<0, 8>([&](auto U) { LOAD_X(U.v); });
        SCHED();
    }
    SCHED();
    LBAR();
    TS(8);
    {
        unsigned val[2][9];
#pragma unroll
        for (int ch = 0; ch < 2; ++ch)
#pragma unroll
            for (int e = 0; e < 9; ++e) val[ch][e] = *(LAS unsigned short*)(size_t)(kga[e] + ch * 1312);
        SCHED();
        sfor<0, 7>([&](auto I) { STAGE_W1(2, I.v); });
        {
            const int r = 35 * (w >> 1) + (lane < 35 ? lane : 34), row = (r * 147) >> 10;
            warm4(pf, (w & 1) ? wu : wg, (192 + 10 * j + row) * (F_ * 2) + (xcd & 3) * 896 + (r - 7 * row) * 128);
        }
#pragma unroll
        for (int ch = 0; ch < 2; ++ch) {
            u32x4 E, O;
#pragma unroll
            for (int m = 0; m < 4; ++m) { E[m] = val[ch][2 * m] | (val[ch][2 * m + 1] << 16); O[m] = val[ch][2 * m + 1] | (val[ch][2 * m + 2] << 16); }
            *(LAS u32x4*)(lds + L_KIMG + ch * CV_KCH + tid * 16) = E;
            *(LAS u32x4*)(lds + L_KIMG + ch * CV_KCH + 4160 + tid * 16) = O;
        }
        if (tid < 16) *(LAS u32x4*)(lds + L_KIMG + (tid >> 3) * CV_KCH + ((tid >> 2) & 1) * 4160 + 4096 + (tid & 3) * 16) = u32x4{0, 0, 0, 0};
    }
    SCHED();
    TS(9);

    f32x16 acc[7];
    u32x4 uu0, uu1;
    bf16x8 wf[28][2];
    const unsigned wtvo = lane * 16, wtsb = WS_WT + (28 * w * 16 + 2 * dt2) * 1024;
    unsigned wtso;
#define LOAD_WT(I) do { if constexpr (((I) & 1) == 0) aload_a_first<((I) >> 1) * 16384, 0>(wf[(I) >> 1][0], wtso, rs, wtvo, wtsb); \
                        else aload_a_next<1024>(wf[(I) >> 1][1], wtso, rs, wtvo); } while (0)
    {
        const unsigned ab0 = lbase + (g16 & 1) * G1_UP + (8 * (g16 >> 1) + q4) * 224 + phi * 8, ab1 = ab0 + 2 * G1_SLOT, ab2 = ab0 + 4 * G1_SLOT;
        auto rdw = [&](auto STP, auto P) {
            constexpr int st = STP.v, p = P.v, sl = g1slot(st >> 2), off = (sl & 1) * G1_SLOT + (st & 3) * 16 * 224 + p * 32;
            const unsigned base = sl < 2 ? ab0 : sl < 4 ? ab1 : ab2;
            return cat8(atr<off>(base), atr<off + 4 * 224>(base));
        };
        bf16x8 cur[7], nxt[7];
        u32x4 fx[2];
        waitv<G1S.younger(1, 1, 0)>();
        LBAR();
        sfor<0, 7>([&](auto P) { cur[P.v] = rdw(IC<0>{}, P); });
        SCHED();
        TS(10);
        sfor<0, 32>([&](auto ST) {
            constexpr int st = ST.v, s = st >> 2, kk = st & 3;
            if constexpr (kk == 0 && s > 0) {
                G1_STAMP(2 * s - 2);
                if constexpr (s <= 6) waitv<G1S.younger(1, s + 1, st)>();
                BAR();
                G1_STAMP(2 * s - 1);
                SCHED();
            }
            if constexpr (st == 6) {
                asm volatile("s_waitcnt vmcnt(%2)" : "+v"(fx[0]), "+v"(fx[1]) : "i"(G1S.younger(3, 1, st)) : "memory");
                const u64 f0 = ((u64)fx[0][1] << 32) | fx[0][0], f1 = ((u64)fx[0][3] << 32) | fx[0][2], f2 = ((u64)fx[1][1] << 32) | fx[1][0], f3 = ((u64)fx[1][3] << 32) | fx[1][2];
                if (!__all(f0 == tag && f1 == tag && f2 == tag && f3 == tag)) wait_flags<4>(ws, WS_FX, 64, tag, lane);
                SCHED();
            }
            waitv<G1S.younger(0, st, st)>();
            sfor<0, 7>([&](auto P) {
                constexpr int p = P.v;
                if constexpr (p == 0) waitl<8>(); else if constexpr (p == 3) waitl<(st < 31 ? 10 : 4)>(); else if constexpr (p == 5) waitl<(st < 31 ? 10 : 0)>();
                mfma32_va<st == 0>(acc[p], cur[p], xf[st]);
                if constexpr (st < 31) nxt[p] = rdw(IC<st + 1>{}, P);
                sfor<0, G1S.n>([&](auto K) {
                    constexpr G1Op o = G1S.op[K.v];
                    if constexpr (o.step == st && o.gap == p) {
                        if constexpr (o.kind == 0) LOAD_X(o.a);
                        else if constexpr (o.kind == 1) STAGE_W1(o.a, o.b);
                        else if constexpr (o.kind == 2) LOAD_WT(o.a);
                        else if constexpr (o.kind == 3) aload_v<o.a * 16>(fx[o.a], rs, WS_FXP + lane * 32, 0);
                        else aload_v<0>(o.a == 0 ? uu0 : uu1, rs, WS_XT + ((tid >> 1) * 512 + c0 + (tid & 1)) * 16, o.a * 128 * 512 * 16);
                    }
                });
                SCHED();
            });
#pragma unroll
            for (int p = 0; p < 7; ++p) cur[p] = nxt[p];
        });
    }
    MFMA_DRAIN();
    TS(11);
    asm volatile("s_waitcnt vmcnt(%3)" : "+v"(uu0), "+v"(uu1), "+v"(pf) : "i"(G1S.younger(4, 1, 32)) : "memory");
    if (tid < 64) *(LAS u32x4*)(lds + L_U + (tid >> 5) * CV_UCH + (tid & 31) * 16) = u32x4{0, 0, 0, 0};
    *(LAS u32x4*)(lds + L_U + (tid & 1) * CV_UCH + 512 + (tid >> 1) * 16) = uu0;
    *(LAS u32x4*)(lds + L_U + (tid & 1) * CV_UCH + 512 + 2048 + (tid >> 1) * 16) = uu1;
    SCHED();
    TS(12);
    {
        const unsigned hvo = WS_H + (32 * (w & 1) + (lane & 31)) * 32 + (lane >> 5) * 16, hso = ((mt * 2 + (w >> 1)) * 112 + chunk * 7) * 2048;
        sfor<0, 7>([&](auto P) {
            constexpr int p = P.v;
            u32x4 o;
            f32x2 e[4], gu[4];
#pragma unroll
            for (int r = 0; r < 4; ++r) {
                const f32x2 g = {acc[p][2 * r], acc[p][2 * r + 1]}, u = {acc[p][8 + 2 * r], acc[p][9 + 2 * r]};
                const f32x2 t = g * -1.44269504088896341f;
                e[r] = f32x2{__builtin_amdgcn_exp2f(t[0]), __builtin_amdgcn_exp2f(t[1])};
                gu[r] = g * u;
            }
            SCHED();
            sfor<G1_WT + (2 * p * WT_EPI) / 14, G1_WT + ((2 * p + 1) * WT_EPI) / 14>([&](auto I) { LOAD_WT(I.v); });
#pragma unroll
            for (int r = 0; r < 4; ++r) {
                const f32x2 d = e[r] + 1.0f;
                e[r] = f32x2{__builtin_amdgcn_rcpf(d[0]), __builtin_amdgcn_rcpf(d[1])};
            }
#pragma unroll
            for (int r = 0; r < 4; ++r) o[r] = pk2v(gu[r] * e[r]);
            SCHED();
            sfor<G1_WT + ((2 * p + 1) * WT_EPI) / 14, G1_WT + ((2 * p + 2) * WT_EPI) / 14>([&](auto I) { LOAD_WT(I.v); });
            st128(rs, o, hvo, hso + p * 2048);
            SCHED();
        });
    }
    LBAR();
    TS(13);

    const int chl = w >> 1, rh = w & 1;
    constexpr int PD = 12;
    bf16x8 hf[PD][2];
    const unsigned hvoff = WS_H + (lane & 31) * 32 + (lane >> 5) * 16;
    const unsigned hsoff = ((mt2 * 2 + th2) * 112 + 28 * w) * 2048;
    auto ldh = [&](int s, int t) { return asbf(ld128(rs, hvoff + (s & 1) * 2048 + t * 1024, hsoff + (s >> 1) * 4096)); };
    f32x4 cacc[8];
    {
        LAS char* ub = lds + L_U + chl * CV_UCH + 2 * (256 + 16 * (l15 - (g16 >> 1)) + 8 * (g16 & 1)) - 32 * (8 * rh + 6);
        const int i0 = 2047 - 16 * (g16 >> 1) - l15 + 8 * (g16 & 1);
        LAS char* kb = lds + L_KIMG + chl * CV_KCH + ((l15 & 1) ? 0 : 4160) + 4 * (i0 >> 1) - 32 * (8 * rh + 6);
        LAS char* kbm[8];
#pragma unroll
        for (int m = 0; m < 8; ++m) { kbm[m] = kb - 512 * m; asm volatile("" : "+v"(kbm[m])); }
        auto ldG = [&](int m, int rr) {
            const LAS unsigned* p = (const LAS unsigned*)(kbm[m] + 64 * (3 - rr));
            u32x4 t; t[0] = p[0]; t[1] = p[1]; t[2] = p[2]; t[3] = p[3];
            return __builtin_bit_cast(bf16x8, t);
        };
        auto ldF = [&](int m, int rr) { return *(const LAS bf16x8*)(ub + 64 * (3 - rr) + 512 * m); };
        bf16x8 G[2][8], Fr[2][8];
#pragma unroll
        for (int m = 0; m < 8; ++m) { G[0][m] = ldG(m, 0); Fr[0][m] = ldF(m, 0); }
        SCHED();
        u64 fh;
        sfor<0, 4>([&](auto RR) {
            constexpr int rr = RR.v, c = rr & 1, n = c ^ 1;
            CV_STAMP(rr);
            if constexpr (rr == 1) {
                waitv<((56 - G1_WT - WT_EPI + 2) / 3 < 9 ? (56 - G1_WT - WT_EPI + 2) / 3 : 9)>();
                BAR();
                if (tid == 0) st_flag(ws, WS_FH + (mt * 16 + chunk) * 128, tag);
                SCHED();
            }
            if constexpr (rr == 3) {
                asm volatile("s_waitcnt vmcnt(0)" : "+v"(fh)::"memory");
                if (!__all(fh == tag)) wait_flags<1>(ws, WS_FH + (mt2 * 16 + 4 * w) * 128, 4, tag, lane);
                SCHED();
            }
            sfor<0, 36>([&](auto C) {
                constexpr int cnt = C.v;
                constexpr int m = cnt < 8 ? 0 : cnt < 15 ? 1 : cnt < 21 ? 2 : cnt < 26 ? 3 : cnt < 30 ? 4 : cnt < 33 ? 5 : cnt < 35 ? 6 : 7;
                constexpr int first = m * 8 - (m * (m - 1)) / 2, i = m + (cnt - first);
                mfma16_vv<rr == 0 && m == 0>(cacc[i], G[c][m], Fr[c][i - m]);
                if constexpr (rr < 3) {
                    if constexpr (cnt < 8) Fr[n][cnt] = ldF(cnt, rr + 1);
                    else if constexpr (cnt < 24 && !(cnt & 1)) G[n][(cnt - 8) >> 1] = ldG((cnt - 8) >> 1, rr + 1);
                }
                if constexpr (rr < 3 && cnt % 4 == 2) {
                    constexpr int nrest = 56 - G1_WT - WT_EPI, per = (nrest + 2) / 3, k = cnt / 4, i = G1_WT + WT_EPI + rr * per + k;
                    if constexpr (k < per && i < 56) LOAD_WT(i);
                }
                if constexpr (rr == 2 && cnt == 8) aload8sc1(fh, ws + WS_FH + (mt2 * 16 + 4 * w) * 128, (lane & 3) * 128);
                if constexpr (rr == 3 && cnt % 3 == 1 && cnt / 3 < 10) hf[cnt / 6][(cnt / 3) & 1] = ldh(cnt / 6, (cnt / 3) & 1);
                SCHED();
            });
        });
    }
    MFMA_DRAIN();
    TS(14);
    {
        LAS char* red = lds + L_RED;
#pragma unroll
        for (int i = 0; i < 4; ++i) {
            f32x4 lo = cacc[i], hi = cacc[4 + i];
            asm volatile("" : "+v"(lo), "+v"(hi));
            *(LAS f32x4*)(red + (w * 4 + i) * 1024 + lane * 16) = rh ? lo : hi;
            cacc[i] = rh ? hi : lo;
            hf[5 + i][0] = ldh(5 + i, 0); hf[5 + i][1] = ldh(5 + i, 1);
            SCHED();
        }
        LBAR();
        const float dsk = bf2f(__builtin_amdgcn_readfirstlane((int)p_dsk));
#pragma unroll
        for (int i = 0; i < 4; ++i) {
            const f32x4 y = cacc[i] + *(LAS f32x4*)(red + ((w ^ 1) * 4 + i) * 1024 + lane * 16);
            const int t = 256 * (4 * rh + i) + 16 * l15 + 4 * g16;
            const u32x2 uu = *(LAS u32x2*)(lds + L_U + chl * CV_UCH + 2 * (256 + t));
            cacc[i] = f32x4{y[0] + bflo(uu[0]) * dsk, y[1] + bfhi(uu[0]) * dsk, y[2] + bflo(uu[1]) * dsk, y[3] + bfhi(uu[1]) * dsk};
            if (i < 3) { hf[9 + i][0] = ldh(9 + i, 0); hf[9 + i][1] = ldh(9 + i, 1); }
            SCHED();
        }
#pragma unroll
        for (int i = 0; i < 4; ++i) {
            const int t = 256 * (4 * rh + i) + 16 * l15 + 4 * g16;
            st64(rs, u32x2{pk2(cacc[i][0], cacc[i][1]), pk2(cacc[i][2], cacc[i][3])}, WS_ST + ((c0 + chl) * L_ + t) * 2, 0);
        }
    }
    SCHED();
    TS(15);

    f32x16 a2[2][2];
    u32x4 sv[2], xv[2];
    u64 fs;
    const int d0 = 64 * dt2, t0 = mt2 * 128 + th2 * 64;
    const int tt2 = w >> 1, dh2 = w & 1;
    sfor<0, 28>([&](auto S) {
        constexpr int s = S.v;
        const bf16x8 h0 = hf[s % PD][0], h1 = hf[s % PD][1];
        mfma32_av<s == 0>(a2[0][0], wf[s][0], h0);
        mfma32_av<s == 0>(a2[1][0], wf[s][1], h0);
        SCHED();
        mfma32_av<s == 0>(a2[0][1], wf[s][0], h1);
        mfma32_av<s == 0>(a2[1][1], wf[s][1], h1);
        sfor<PD, 28>([&](auto T) {
            constexpr int t = T.v;
            if constexpr (((t - PD) * 21) / 16 == s) { hf[t % PD][0] = ldh(t, 0); hf[t % PD][1] = ldh(t, 1); }
        });
        if constexpr (s == 10) {
            G2_STAMP(0);
            waitv<G2_YOUNGER>();
            BAR();
            G2_STAMP(1);
            if (tid == 0) st_flag(ws, WS_FS + tb * 8, tag);
        }
        if constexpr (s == 19) aload8sc1(fs, ws + WS_FS + (d0 >> 1) * 8, (lane & 31) * 8);
        if constexpr (s == 23) {
            G2_STAMP(2);
            asm volatile("s_waitcnt vmcnt(0)" : "+v"(fs)::"memory");
            G2_STAMP(3);
            if (!__all(fs == tag)) wait_flags<1, 8>(ws, WS_FS + (d0 >> 1) * 8, 32, tag, lane);
#pragma unroll
            for (int i = 0; i < 2; ++i) {
                const int e = tid + 256 * i;
                sv[i] = ld128(rs, WS_ST + ((d0 + (e >> 3)) * L_ + t0) * 2 + (e & 7) * 16, 0);
            }
#pragma unroll
            for (int g = 0; g < 2; ++g) xv[g] = *(const u32x4*)(x + (size_t)(t0 + 32 * tt2 + (lane & 31)) * D_ + d0 + 32 * dh2 + 16 * g + 8 * (lane >> 5));
        }
        SCHED();
    });
    MFMA_DRAIN();
    TS(16);
    {
        sfor<0, 4>([&](auto Q) {
            constexpr int q = Q.v, tt = q >> 1, dh = q & 1;
            if (q != w) {
                const f32x16 a = a2[dh][tt];
#pragma unroll
                for (int g = 0; g < 4; ++g)
                    *(LAS f32x4*)(lds + ((q * 4 + w) * 4 + g) * 1024 + lane * 16) = f32x4{a[4 * g], a[4 * g + 1], a[4 * g + 2], a[4 * g + 3]};
            }
        });
#pragma unroll
        for (int i = 0; i < 2; ++i) *(LAS u32x4*)(lds + L_ST + (tid + 256 * i) * 16) = sv[i];
        LBAR();
    }
    TS(17);
    {
        f32x16 own;
        { f32x16 q0 = a2[0][0], q1 = a2[1][0], q2 = a2[0][1], q3 = a2[1][1];
          asm volatile("" : "+v"(q0), "+v"(q1), "+v"(q2), "+v"(q3));
          own = w == 0 ? q0 : w == 1 ? q1 : w == 2 ? q2 : q3; }
        f32x4 f[4];
#pragma unroll
        for (int g = 0; g < 4; ++g) {
            f[g] = f32x4{own[4 * g], own[4 * g + 1], own[4 * g + 2], own[4 * g + 3]};
#pragma unroll
            for (int o = 1; o < 4; ++o) f[g] += *(LAS f32x4*)(lds + ((w * 4 + ((w + o) & 3)) * 4 + g) * 1024 + lane * 16);
        }
        LAS char* sp = lds + L_ST + (32 * dh2 + 8 * (lane >> 5) + q4) * 128 + (32 * tt2 + 16 * (g16 & 1) + 4 * pp) * 2;
#pragma unroll
        for (int g2 = 0; g2 < 2; ++g2) {
            const s16x4 sa = tr_read(sp + g2 * 16 * 128), sb = tr_read(sp + (g2 * 16 + 4) * 128);
            const u32x2 s01 = __builtin_bit_cast(u32x2, sa), s23 = __builtin_bit_cast(u32x2, sb);
            u32x4 o;
            o[0] = pk2v(unpk(xv[g2][0]) + unpk(s01[0]) * silu2(f32x2{f[2 * g2][0], f[2 * g2][1]}));
            o[1] = pk2v(unpk(xv[g2][1]) + unpk(s01[1]) * silu2(f32x2{f[2 * g2][2], f[2 * g2][3]}));
            o[2] = pk2v(unpk(xv[g2][2]) + unpk(s23[0]) * silu2(f32x2{f[2 * g2 + 1][0], f[2 * g2 + 1][1]}));
            o[3] = pk2v(unpk(xv[g2][3]) + unpk(s23[1]) * silu2(f32x2{f[2 * g2 + 1][2], f[2 * g2 + 1][3]}));
            *(u32x4*)(out + (size_t)(t0 + 32 * tt2 + (lane & 31)) * D_ + d0 + 32 * dh2 + 16 * g2 + 8 * (lane >> 5)) = o;
        }
    }
    if (b == 0 && tid == 0) st_flag(ws, WS_EP, tag);
    TS(18);
    TSR(47);
    TS_DUMP();
    asm volatile(".Lfused_pf_end:" ::: "memory");
}

extern "C" void kernel_launch(void* const* d_in, const int* in_sizes, int n_in, void* d_out, int out_size, void* d_ws, size_t ws_size,
                              hipStream_t stream) {
    (void)hipFuncSetAttribute((const void*)fused_s4_ffn, hipFuncAttributeMaxDynamicSharedMemorySize, LDS_TOTAL);
    fused_s4_ffn<<<256, 256, LDS_TOTAL, stream>>>((const bf16_t*)d_in[0], (const bf16_t*)d_in[1], (const bf16_t*)d_in[2], (const bf16_t*)d_in[3],
                                                  (const bf16_t*)d_in[4], (const bf16_t*)d_in[5], (const bf16_t*)d_in[6], (const bf16_t*)d_in[7],
                                                  (const bf16_t*)d_in[8], (const bf16_t*)d_in[10], (const bf16_t*)d_in[11], (const bf16_t*)d_in[12],
                                                  (bf16_t*)d_out, (char*)d_ws);
}
```

```cpp
#include <hip/hip_runtime.h>
#include <stdint.h>

typedef unsigned short bf16_t;
typedef short bf16x8 __attribute__((ext_vector_type(8)));
typedef short s16x4 __attribute__((ext_vector_type(4)));
typedef float f32x2 __attribute__((ext_vector_type(2)));
typedef float f32x4 __attribute__((ext_vector_type(4)));
typedef float f32x16 __attribute__((ext_vector_type(16)));
typedef unsigned u32x4 __attribute__((ext_vector_type(4)));
typedef unsigned u32x2 __attribute__((ext_vector_type(2)));
typedef __bf16 bf16v2 __attribute__((ext_vector_type(2)));
typedef unsigned long long u64;
#define LAS __attribute__((address_space(3)))
#define GAS __attribute__((address_space(1)))
#define DI __device__ __forceinline__

#define XSCHED 0
#define WSCHED 0
#define PROBE_B 77
constexpr int L_ = 2048, D_ = 512, N_ = 128, F_ = 1792;

constexpr unsigned WS_XN = 0;
constexpr unsigned WS_XT = 2u << 20;
constexpr unsigned WS_ST = 4u << 20;
constexpr unsigned WS_H = 6u << 20;
constexpr unsigned WS_WT = 14u << 20;
constexpr unsigned WS_FX = 16u << 20;
constexpr unsigned WS_FH = WS_FX + 256 * 128;
constexpr unsigned WS_FS = WS_FH + 256 * 128;
constexpr unsigned WS_FXP = WS_FS + 256 * 128;
constexpr unsigned WS_EP = WS_FXP + 256 * 8;
constexpr unsigned WS_USED = 17u << 20;

constexpr int G1_SLOT = 28800, G1_UP = 14336 + 128;
constexpr int g1slot(int s) { return s % 5 == 0 ? 0 : s % 5 == 1 ? 3 : s % 5 == 2 ? 4 : s % 5 == 3 ? 1 : 2; }
constexpr int KG_BASE = G1_SLOT, KG_RS = 528, KG_CH = 53 * KG_RS;
constexpr int L_WTS = 3 * G1_SLOT;
constexpr int L_KC = 144128;
constexpr int L_KIMG = 146816;
constexpr int L_LNS = L_KIMG, LN_RS = 1056;
constexpr int CV_KCH = 8320;
constexpr int L_U = 2 * G1_SLOT, CV_UCH = 4608;
constexpr int L_RED = L_U + 2 * CV_UCH;
constexpr int L_ST = 65536;
constexpr int LDS_TOTAL = 163840;
static_assert(KG_BASE + 2 * KG_CH <= 3 * G1_SLOT && KG_BASE + KG_CH + 256 < 65536, "K-gen scratch");
static_assert(L_KIMG + 2 * CV_KCH <= LDS_TOTAL && L_RED + 16384 <= 3 * G1_SLOT && L_ST + 8192 <= 3 * G1_SLOT, "lds map");

DI float bf2f(unsigned u16) { return __uint_as_float(u16 << 16); }
DI float bflo(unsigned w) { return __uint_as_float(w << 16); }
DI float bfhi(unsigned w) { return __uint_as_float(w & 0xffff0000u); }
DI unsigned pk2v(f32x2 v) { return __builtin_bit_cast(unsigned, __builtin_convertvector(v, bf16v2)); }
DI unsigned pk2(float lo, float hi) { return pk2v(f32x2{lo, hi}); }
DI f32x2 unpk(unsigned w) { return f32x2{bflo(w), bfhi(w)}; }
DI float rbf(float x) { return bflo(pk2(x, x)); }

#define WAITV(n) asm volatile("s_waitcnt vmcnt(" #n ")" ::: "memory")
#define WAITL(n) asm volatile("s_waitcnt lgkmcnt(" #n ")" ::: "memory")
#define BAR() do { asm volatile("" ::: "memory"); __builtin_amdgcn_s_barrier(); asm volatile("" ::: "memory"); } while (0)
#define LBAR() do { WAITL(0); BAR(); } while (0)
#define SCHED() __builtin_amdgcn_sched_barrier(0)

#define TS(i) do {} while (0)
#define TSR(i) do {} while (0)
#define TS_DUMP() do {} while (0)

template <int N> struct IC { static constexpr int v = N; };
template <int B, int E, class F> DI void sfor(F&& f) { if constexpr (B < E) { f(IC<B>{}); sfor<B + 1, E>(f); } }
template <int N> DI void waitv() { asm volatile("s_waitcnt vmcnt(%0)" ::"i"(N) : "memory"); }
template <int N> DI void waitl() { asm volatile("s_waitcnt lgkmcnt(%0)" ::"i"(N) : "memory"); }

template <int CTRL, int RMASK> DI float dpp_add(float v) {
    return v + __builtin_bit_cast(float, __builtin_amdgcn_update_dpp(0, __builtin_bit_cast(int, v), CTRL, RMASK, 0xf, true));
}
DI void wave_sum4(float& a, float& b, float& c, float& d) {
#define STEP(C, M) a = dpp_add<C, M>(a); b = dpp_add<C, M>(b); c = dpp_add<C, M>(c); d = dpp_add<C, M>(d);
    STEP(0x111, 0xf) STEP(0x112, 0xf) STEP(0x114, 0xf) STEP(0x118, 0xf) STEP(0x142, 0xa) STEP(0x143, 0xc)
#undef STEP
    a = __builtin_bit_cast(float, __builtin_amdgcn_readlane(__builtin_bit_cast(int, a), 63));
    b = __builtin_bit_cast(float, __builtin_amdgcn_readlane(__builtin_bit_cast(int, b), 63));
    c = __builtin_bit_cast(float, __builtin_amdgcn_readlane(__builtin_bit_cast(int, c), 63));
    d = __builtin_bit_cast(float, __builtin_amdgcn_readlane(__builtin_bit_cast(int, d), 63));
}
DI s16x4 tr_read(LAS char* p) { return __builtin_amdgcn_ds_read_tr16_b64_v4i16((LAS s16x4*)p); }
template <int OFF> DI s16x4 atr(unsigned addr) { s16x4 r; asm volatile("ds_read_b64_tr_b16 %0, %1 offset:%2" : "=v"(r) : "v"(addr), "i"(OFF)); return r; }
DI bf16x8 cat8(s16x4 a, s16x4 b) { return __builtin_shufflevector(a, b, 0, 1, 2, 3, 4, 5, 6, 7); }

typedef __amdgpu_buffer_rsrc_t rsrc_t;
constexpr int SC1 = 16;
DI u32x4 ld128(rsrc_t r, unsigned voff, unsigned soff) { return __builtin_amdgcn_raw_buffer_load_b128(r, voff, soff, SC1); }
DI void st128(rsrc_t r, u32x4 v, unsigned voff, unsigned soff) {
    asm volatile("buffer_store_dwordx4 %0, %1, %2, %3 offen sc1\n\ts_nop 1" ::"v"(v), "v"(voff), "s"(r), "s"(soff) : "memory");
}
DI void st64(rsrc_t r, u32x2 v, unsigned voff, unsigned soff) { __builtin_amdgcn_raw_buffer_store_b64(v, r, voff, soff, SC1); }
DI bf16x8 asbf(u32x4 v) { return __builtin_bit_cast(bf16x8, v); }

template <int SIMM, int LIMM, int IMM> DI void bdma_first(unsigned& so, rsrc_t r, unsigned voff, unsigned sbase, unsigned lbase) {
    asm volatile("s_add_u32 m0, %1, %2\n\ts_add_u32 %0, %3, %4\n\tbuffer_load_dwordx4 %5, %6, %0 offen offset:%7 lds"
                 : "=&s"(so) : "s"(lbase), "i"(LIMM), "s"(sbase), "i"(SIMM), "v"(voff), "s"(r), "i"(IMM) : "memory", "scc");
}
template <int IMM> DI void bdma_next(unsigned so, rsrc_t r, unsigned voff) {
    asm volatile("buffer_load_dwordx4 %0, %1, %2 offen offset:%3 lds" ::"v"(voff), "s"(r), "s"(so), "i"(IMM) : "memory");
}
template <int SIMM, int IMM> DI void aload_a_first(bf16x8& d, unsigned& so, rsrc_t r, unsigned voff, unsigned sbase) {
    asm volatile("s_add_u32 %1, %4, %5\n\tbuffer_load_dwordx4 %0, %2, %3, %1 offen offset:%6 sc1"
                 : "=&a"(d), "=&s"(so) : "v"(voff), "s"(r), "s"(sbase), "i"(SIMM), "i"(IMM) : "memory", "scc");
}
template <int IMM> DI void aload_a_next(bf16x8& d, unsigned so, rsrc_t r, unsigned voff) {
    asm volatile("buffer_load_dwordx4 %0, %1, %2, %3 offen offset:%4 sc1" : "=&a"(d) : "v"(voff), "s"(r), "s"(so), "i"(IMM) : "memory");
}
template <int IMM> DI void aload_v(u32x4& d, rsrc_t r, unsigned voff, unsigned so) {
    asm volatile("buffer_load_dwordx4 %0, %1, %2, %3 offen offset:%4 sc1" : "=&v"(d) : "v"(voff), "s"(r), "s"(so), "i"(IMM) : "memory");
}
DI void warm4(unsigned& pf, const void* sbase, unsigned voff) { asm volatile("global_load_dword %0, %1, %2" : "+v"(pf) : "v"(voff), "s"(sbase) : "memory"); }
DI void aload16(u32x4& d, const void* sbase, unsigned voff) { asm volatile("global_load_dwordx4 %0, %1, %2" : "=&v"(d) : "v"(voff), "s"(sbase) : "memory"); }
DI void aload2(unsigned& d, const void* sbase, unsigned voff) { asm volatile("global_load_ushort %0, %1, %2" : "=&v"(d) : "v"(voff), "s"(sbase) : "memory"); }
DI void aload8sc1(u64& d, const void* sbase, unsigned voff) { asm volatile("global_load_dwordx2 %0, %1, %2 sc1" : "=&v"(d) : "v"(voff), "s"(sbase) : "memory"); }

template <bool FIRST> DI void mfma32_va(f32x16& c, bf16x8 a, bf16x8 b) {
    if constexpr (FIRST) asm volatile("v_mfma_f32_32x32x16_bf16 %0, %1, %2, 0" : "=&v"(c) : "v"(a), "a"(b));
    else asm volatile("v_mfma_f32_32x32x16_bf16 %0, %1, %2, %0" : "+v"(c) : "v"(a), "a"(b));
}
template <bool FIRST> DI void mfma32_av(f32x16& c, bf16x8 a, bf16x8 b) {
    if constexpr (FIRST) asm volatile("v_mfma_f32_32x32x16_bf16 %0, %1, %2, 0" : "=&v"(c) : "a"(a), "v"(b));
    else asm volatile("v_mfma_f32_32x32x16_bf16 %0, %1, %2, %0" : "+v"(c) : "a"(a), "v"(b));
}
template <bool FIRST> DI void mfma16_vv(f32x4& c, bf16x8 a, bf16x8 b) {
    if constexpr (FIRST) asm volatile("v_mfma_f32_16x16x32_bf16 %0, %1, %2, 0" : "=&v"(c) : "v"(a), "v"(b));
    else asm volatile("v_mfma_f32_16x16x32_bf16 %0, %1, %2, %0" : "+v"(c) : "v"(a), "v"(b));
}
#define MFMA_DRAIN() asm volatile("s_nop 15\n\ts_nop 3" ::: "memory")

#define WT_LOOP 24
#define WT_EPI 14
constexpr int G1_WT = WT_LOOP;
struct G1Op { int step, gap, kind, a, b; };
struct G1Sched {
    G1Op op[160]; int n;
    constexpr G1Sched() : op{}, n(0) {
        for (int i = 0; i < 7; ++i) op[n++] = G1Op{-1, 0, 1, 1, i};
        for (int u = 0; u < 8; ++u) op[n++] = G1Op{-1, 0, 0, u, 0};
        for (int i = 0; i < 7; ++i) op[n++] = G1Op{-1, 0, 1, 2, i};
        op[n++] = G1Op{-1, 0, 6, 0, 0};
        int wt = 0;
        for (int s = 0; s < 8; ++s) {
            G1Op list[24] = {}; int m = 0;
            if (s == 0) for (int i = 0; i < 2; ++i) list[m++] = G1Op{0, 0, 3, i, 0};
            if (s < 3) { for (int i = 0; i < 4; ++i) list[m++] = G1Op{0, 0, 0, 8 + 8 * s + i, 0};
                         for (int i = 0; i < 4; ++i) list[m++] = G1Op{0, 0, 1, 3 + s, i};
                         for (int i = 4; i < 8; ++i) list[m++] = G1Op{0, 0, 0, 8 + 8 * s + i, 0};
                         for (int i = 4; i < 7; ++i) list[m++] = G1Op{0, 0, 1, 3 + s, i}; }
            if (s == 3 || s == 4) for (int i = 0; i < 7; ++i) list[m++] = G1Op{0, 0, 1, 3 + s, i};
            if (s == 5) for (int i = 0; i < 2; ++i) list[m++] = G1Op{0, 0, 4, i, 0};
            const int nw = s < 5 ? 0 : s == 5 ? G1_WT / 4 / 2 * 2 : s == 6 ? (G1_WT - G1_WT / 4 / 2 * 2) / 2 / 2 * 2 : G1_WT - wt;
            for (int i = 0; i < nw; ++i) list[m++] = G1Op{0, 0, 2, wt++, 0};
            const int span = s == 7 ? 21 : 28;
            for (int i = 0; i < m; ++i) { const int g = (i * span) / m; list[i].step = 4 * s + g / 7; list[i].gap = g % 7; op[n++] = list[i]; }
        }
    }
    constexpr int younger(int kind, int a, int st) const {
        int last = -1;
        for (int i = 0; i < n; ++i) if (op[i].kind == kind && op[i].a == a) last = i;
        int c = 0;
        for (int i = last + 1; i < n; ++i) if (op[i].step < st) ++c;
        return c;
    }
};
constexpr G1Sched G1S{};

DI u64 ld_flag(const char* ws, unsigned off) { return __hip_atomic_load((GAS u64*)(ws + off), __ATOMIC_RELAXED, __HIP_MEMORY_SCOPE_AGENT); }
DI void st_flag(char* ws, unsigned off, u64 v) { __hip_atomic_store((GAS u64*)(ws + off), v, __ATOMIC_RELAXED, __HIP_MEMORY_SCOPE_AGENT); }
template <int NPL, int STRIDE = 128>
DI void wait_flags(const char* ws, unsigned base, int nl, u64 tag, int lane) {
    const unsigned off = base + (lane < nl ? lane : 0) * STRIDE;
#pragma unroll 1
    for (int spin = 0; spin < 40000; ++spin) {
        bool ok = true;
#pragma unroll
        for (int i = 0; i < NPL; ++i) ok &= ld_flag(ws, off + i * 64 * STRIDE) == tag;
        if (__all(ok)) break;
        __builtin_amdgcn_s_sleep(1);
    }
    asm volatile("" ::: "memory");
}

typedef f32x2 cf;
DI cf cmul(cf a, cf b) { return cf{a[0] * b[0] - a[1] * b[1], a[0] * b[1] + a[1] * b[0]}; }
DI void cmul2(cf& z0, cf a0, cf b0, cf& z1, cf a1, cf b1) {
    cf t0, t1;
    asm("v_pk_mul_f32 %2, %4, %5 op_sel_hi:[0,1]\n\tv_pk_mul_f32 %3, %6, %7 op_sel_hi:[0,1]\n\t"
        "v_pk_fma_f32 %0, %4, %5, %2 op_sel:[1,1,0] op_sel_hi:[1,0,1] neg_lo:[0,1,0]\n\t"
        "v_pk_fma_f32 %1, %6, %7, %3 op_sel:[1,1,0] op_sel_hi:[1,0,1] neg_lo:[0,1,0]"
        : "=&v"(z0), "=&v"(z1), "=&v"(t0), "=&v"(t1) : "v"(a0), "v"(b0), "v"(a1), "v"(b1));
}
DI cf zdir(float a2, float th, float l) {
    const float m = __builtin_amdgcn_exp2f(a2 * l);
    const float r = __builtin_amdgcn_fractf(th * l);
    return cf{m * __builtin_amdgcn_cosf(r), m * __builtin_amdgcn_sinf(r)};
}
DI f32x2 silu2(f32x2 g) {
    const f32x2 t = g * -1.44269504088896341f;
    const f32x2 d = f32x2{__builtin_amdgcn_exp2f(t[0]), __builtin_amdgcn_exp2f(t[1])} + 1.0f;
    return g * f32x2{__builtin_amdgcn_rcpf(d[0]), __builtin_amdgcn_rcpf(d[1])};
}

#define PUBLISH_AT 3
constexpr int g2_younger() { int c = 0; for (int t = 12; t < 28; ++t) if (((t - 12) * 21) / 16 <= 10) c += 2; return c; }
#define G2_YOUNGER g2_younger()
constexpr int g2_after_fs() { int c = 0; for (int t = 12; t < 28; ++t) { const int st = ((t - 12) * 21) / 16; if (st > 17 && st <= 21) c += 2; } return c; }
#define G2_AFTER_FS g2_after_fs()
#define G1_STAMP(i) do {} while (0)
#define CV_STAMP(i) do {} while (0)
#define G2_STAMP(i) do {} while (0)

extern __shared__ __attribute__((aligned(16))) char dyn_smem[];

__global__ __launch_bounds__(256, 1) void fused_s4_ffn(const bf16_t* __restrict__ x, const bf16_t* __restrict__ A_re, const bf16_t* __restrict__ A_im,
                                                       const bf16_t* __restrict__ B_re, const bf16_t* __restrict__ B_im, const bf16_t* __restrict__ C_re,
                                                       const bf16_t* __restrict__ C_im, const bf16_t* __restrict__ log_dt, const bf16_t* __restrict__ Dskip,
                                                       const bf16_t* __restrict__ wg, const bf16_t* __restrict__ wu, const bf16_t* __restrict__ wd,
                                                       bf16_t* __restrict__ out, char* ws) {
    LAS char* lds = (LAS char*)dyn_smem;
    const unsigned lbase = (unsigned)(size_t)lds;
    const int tid = threadIdx.x, w = __builtin_amdgcn_readfirstlane(tid >> 6), lane = tid & 63;
    const int b = blockIdx.x, xcd = b & 7, j = b >> 3;
    const int tb = xcd * 32 + j;
    const int mt = 8 * (xcd >> 2) + (j >> 2), chunk = 4 * (xcd & 3) + (j & 3);
    const int mt2 = 2 * xcd + (j >> 4), th2 = (j >> 3) & 1, dt2 = j & 7;
    const int c0 = 2 * tb;
    const unsigned long long probe_t0 = __builtin_amdgcn_s_memrealtime();
    unsigned long long probe_a = 0;
    if (threadIdx.x == 0) __hip_atomic_store((GAS u64*)((char*)ws + (20u << 20) + blockIdx.x * 8), probe_t0, __ATOMIC_RELAXED, __HIP_MEMORY_SCOPE_AGENT);
    TSR(46); TS(0);

    u32x4 raw[2];
#pragma unroll
    for (int r = 0; r < 2; ++r) aload16(raw[r], x + (size_t)(8 * tb + 2 * w + r) * D_, lane * 16);
    unsigned pf = 0;
    {
        u64 pc; unsigned len;
        asm volatile("s_getpc_b64 %0\n.Lfused_pf_here:\n\ts_mov_b32 %1, .Lfused_pf_end-.Lfused_pf_here" : "=s"(pc), "=s"(len));
        const unsigned o = (unsigned)(12 * j + (lane < 12 ? lane : 11)) * 128u;
        warm4(pf, (const void*)pc, o < len ? o : len);
    }
    u64 ep;
    aload8sc1(ep, ws + WS_EP, 0);
    const int kch = c0 + (w >> 1);
    const unsigned kpo = (64 * (w & 1) + lane) * 2;
    unsigned p_are, p_aim, p_br, p_bi, p_cr, p_ci, p_ldt, p_dsk;
    aload2(p_are, A_re + kch * N_, kpo); aload2(p_aim, A_im + kch * N_, kpo); aload2(p_br, B_re + kch * N_, kpo);
    aload2(p_bi, B_im + kch * N_, kpo); aload2(p_cr, C_re + kch * N_, kpo); aload2(p_ci, C_im + kch * N_, kpo);
    aload2(p_ldt, log_dt + kch, 0); aload2(p_dsk, Dskip + kch, 0);
    SCHED();
    const rsrc_t rs = __builtin_amdgcn_make_buffer_rsrc(ws, 0, WS_USED, 0x00020000);
    const rsrc_t rw1 = __builtin_amdgcn_make_buffer_rsrc((void*)((w & 1) ? wu : wg), 0, D_ * F_ * 2, 0x00020000);
    const rsrc_t rwd = __builtin_amdgcn_make_buffer_rsrc((void*)wd, 0, D_ * F_ * 2, 0x00020000);
    const int wtU0 = 7 * (b >> 4) + w, wtU1 = w < 3 ? wtU0 + 4 : wtU0;
    {
        unsigned so;
        const unsigned vo = (lane >> 2) * 1024 + (lane & 3) * 16;
        bdma_first<0, L_WTS, 0>(so, rwd, vo, wtU0 * 16384 + (b & 15) * 64, lbase + w * 2048);
        bdma_first<0, L_WTS + 1024, 0>(so, rwd, vo, wtU1 * 16384 + (b & 15) * 64, lbase + w * 2048);
    }
    unsigned voff[7];
    {
        const int k = (lane * 147) >> 11, c = lane - 14 * k;
        unsigned v = k * (F_ * 2) + c * 16;
        int cc = c;
#pragma unroll
        for (int i = 0; i < 7; ++i) {
            voff[i] = v - (i & 3) * 1024;
            cc += 8; v += 4 * (F_ * 2) + 128;
            if (cc >= 14) { cc -= 14; v += F_ * 2 - 224; }
        }
    }
    const unsigned w1s = (32 * (w >> 1)) * (F_ * 2) + chunk * 224;
    const unsigned w1l = lbase + (w & 1) * G1_UP + (w >> 1) * 7168;
    unsigned w1so;
#define STAGE_W1(S, I) do { if constexpr (((I) & 3) == 0) bdma_first<64 * (S) * (F_ * 2), g1slot(S) * G1_SLOT + (I) * 1024, 0>(w1so, rw1, voff[I], w1s, w1l); \
                            else bdma_next<((I) & 3) * 1024>(w1so, rw1, voff[I]); } while (0)
    SCHED();
    const int g16 = lane >> 4, q4 = (lane & 15) >> 2, pp = lane & 3, l15 = lane & 15;
    const int phi = ((pp & 1) << 1) | (pp >> 1);
    unsigned ksc[8];
    {
        const int col8 = 8 * (16 * (w & 1) + l15);
        const unsigned kcb = lbase + L_KC + (w >> 1) * 1312, kcf = kcb - 2 * 17024;
        const int A = (g16 == 0 ? 0 : g16 == 1 ? 4 : g16 == 2 ? 256 : 512) + col8, B0 = g16 == 2 ? 2 : g16 == 3 ? 4 : 1, B1 = g16 == 2 ? 4 : g16 == 3 ? 256 : 2;
#pragma unroll
        for (int r = 0; r < 4; ++r) {
            const int v = A + (r & 1) * B0 + (r >> 1) * B1;
            ksc[r] = v < 128 ? kcb + 2 * v : kcf + 2 * (__float_as_uint((float)v) >> 16);
            asm volatile("" : "+v"(ksc[r]));
        }
#pragma unroll
        for (int r = 0; r < 4; ++r) {
            const unsigned a = kcf + 2 * (__float_as_uint((float)(1024 + 256 * r + col8)) >> 16);
            ksc[4 + r] = g16 == 0 ? a : (r == 0 && g16 == 1 && col8 == 0) ? kcb + 2 * 640 : kcb + 2 * 648;
            asm volatile("" : "+v"(ksc[4 + r]));
        }
    }
    unsigned kga[9];
    {
        const float f = (float)(2047 - 8 * tid);
        const unsigned kcf = lbase + L_KC - 2 * 17024;
#pragma unroll
        for (int e = 0; e < 9; e += 2) {
            const unsigned bits = pk2(f - e, f - (e + 1));
            kga[e] = kcf + 2 * (bits & 0xffffu);
            if (e < 8) kga[e + 1] = kcf + 2 * (bits >> 16);
        }
        if (w == 3) {
#pragma unroll
            for (int e = 0; e < 9; ++e) {
                const int l = 2047 - 8 * tid - e;
                if (l < 256) kga[e] = lbase + L_KC + 2 * (l < 0 ? 649 : l);
            }
        }
#pragma unroll
        for (int e = 0; e < 9; ++e) asm volatile("" : "+v"(kga[e]));
    }
    SCHED();
    {
        const int r = 21 * (w >> 1) + (lane < 21 ? lane : 20), row = (r * 147) >> 10;
        warm4(pf, (w & 1) ? wu : wg, (6 * j + row) * (F_ * 2) + (xcd & 3) * 896 + (r - 7 * row) * 128);
    }
    SCHED();
    asm volatile("s_waitcnt vmcnt(3)" : "+v"(ep), "+v"(raw[0]), "+v"(raw[1]), "+v"(p_are), "+v"(p_aim), "+v"(p_br), "+v"(p_bi), "+v"(p_cr), "+v"(p_ci),
                 "+v"(p_ldt), "+v"(p_dsk)::"memory");
    SCHED();
    TS(1);
    {
        f32x2 v[2][4];
        float s1[2], s2[2], x0[2];
        sfor<0, 2>([&](auto R) {
            constexpr int r = R.v;
            x0[r] = bflo((unsigned)__builtin_amdgcn_readfirstlane((int)raw[r][0]));
            f32x2 a = {0.f, 0.f}, q = {0.f, 0.f};
#pragma unroll
            for (int jj = 0; jj < 4; ++jj) { v[r][jj] = unpk(raw[r][jj]); const f32x2 d = v[r][jj] - x0[r]; a += d; q += d * d; }
            s1[r] = a[0] + a[1]; s2[r] = q[0] + q[1];
            STAGE_W1(0, r);
            SCHED();
        });
        wave_sum4(s1[0], s2[0], s1[1], s2[1]);
        STAGE_W1(0, 2);
        SCHED();
        sfor<0, 2>([&](auto R) {
            constexpr int r = R.v;
            const float md = s1[r] * (1.0f / 512.0f);
            const float var = fmaxf(s2[r] * (1.0f / 512.0f) - md * md, 0.f);
            const float mb = rbf(x0[r] + md);
            const float sd = rbf(__builtin_amdgcn_sqrtf(rbf(rbf(var) + 1e-5f)));
            const float rsd = __builtin_amdgcn_rcpf(sd);
            STAGE_W1(0, 3 + 2 * r);
            SCHED();
            u32x4 o;
#pragma unroll
            for (int jj = 0; jj < 4; ++jj) o[jj] = pk2v(unpk(pk2v(v[r][jj] - mb)) * rsd);
            *(LAS u32x4*)(lds + L_LNS + (2 * w + r) * LN_RS + lane * 16) = o;
            STAGE_W1(0, 4 + 2 * r);
            SCHED();
        });
    }
    WAITV(8);
    LBAR();
    TS(2);
    {
        LAS char* tp = lds + L_WTS + w * 2048 + (8 * (g16 >> 1) + q4) * 64 + (g16 & 1) * 32 + phi * 8;
        const bf16x8 f0 = cat8(tr_read(tp), tr_read(tp + 256)), f1 = cat8(tr_read(tp + 1024), tr_read(tp + 1024 + 256));
        st128(rs, __builtin_bit_cast(u32x4, f0), WS_WT + lane * 16, (wtU0 * 16 + (b & 15)) * 1024);
        if (w < 3) st128(rs, __builtin_bit_cast(u32x4, f1), WS_WT + lane * 16, (wtU1 * 16 + (b & 15)) * 1024);
    }
    SCHED();

    {
        const int half = tid & 1, tok = (tid >> 1) & 7, U = tid >> 4;
        const u32x4 va = *(LAS u32x4*)(lds + L_LNS + tok * LN_RS + U * 32 + half * 16);
        const u32x4 vb = *(LAS u32x4*)(lds + L_LNS + tok * LN_RS + U * 32 + half * 16 + 512);
        LAS char* tp = lds + L_LNS + q4 * LN_RS + (128 * w + 16 * g16 + 4 * pp) * 2;
        const s16x4 a0 = tr_read(tp), b0 = tr_read(tp + 4 * LN_RS), a1 = tr_read(tp + 128), b1 = tr_read(tp + 4 * LN_RS + 128);
        const unsigned xo = WS_XN + ((tb >> 2) * 32 + U) * 1024 + ((tb & 3) * 8 + tok) * 32 + half * 16;
        st128(rs, va, xo, 0);
        st128(rs, vb, xo, 16 * 1024);
        const unsigned to = WS_XT + (tb * 512 + 128 * w + 16 * g16 + l15) * 16;
        st128(rs, __builtin_bit_cast(u32x4, cat8(a0, b0)), to, 0);
        st128(rs, __builtin_bit_cast(u32x4, cat8(a1, b1)), to, 1024);
    }
    SCHED();
    const u64 tag = ((u64)(unsigned)__builtin_amdgcn_readfirstlane((int)(unsigned)ep) |
                     ((u64)(unsigned)__builtin_amdgcn_readfirstlane((int)(unsigned)(ep >> 32)) << 32)) + 1;
    TS(3);

    {
        const float dt = rbf(__builtin_amdgcn_exp2f(bf2f(p_ldt) * 1.44269504088896341f));
        const float lr = dt * bf2f(p_are), li = dt * bf2f(p_aim);
        const cf dB{dt * bf2f(p_br), dt * bf2f(p_bi)};
        const cf CBt = cmul(cf{bf2f(p_cr), bf2f(p_ci)}, dB);
        const cf CB{CBt[0], -CBt[1]};
        const float a2 = lr * 1.44269504088896341f, th = li * 0.15915494309189535f;
        asm volatile("s_mov_b32 m0, %0" ::"s"(lbase + KG_BASE + (w >> 1) * KG_CH + (w & 1) * 256) : "memory");
#define PUT2(R0, Z0, R1, Z1) asm volatile("ds_write_addtid_b32 %0 offset:%2\n\tds_write_addtid_b32 %1 offset:%3" \
            ::"v"(pk2v(Z0)), "v"(pk2v(Z1)), "i"((R0) * KG_RS), "i"((R1) * KG_RS) : "memory")
#define PUT4(R0, Z0, R1, Z1, R2, Z2, R3, Z3) asm volatile("ds_write_addtid_b32 %0 offset:%4\n\tds_write_addtid_b32 %1 offset:%5\n\tds_write_addtid_b32 %2 offset:%6\n\tds_write_addtid_b32 %3 offset:%7" \
            ::"v"(pk2v(Z0)), "v"(pk2v(Z1)), "v"(pk2v(Z2)), "v"(pk2v(Z3)), "i"((R0) * KG_RS), "i"((R1) * KG_RS), "i"((R2) * KG_RS), "i"((R3) * KG_RS) : "memory")
        const cf z1 = zdir(a2, -th, 1.f), Z256 = zdir(a2, -th, 256.f), w8 = zdir(a2, th, 8.f);
        cf z2, z3, z4, z5, z6, z7, w16, q1, q2, q3, q4_, q5, q6, q7, q256, q512, q768, q1024, q1280, q1536, q1792, q2048, r0, r1, r2, r3, r4;
        cmul2(z2, z1, z1, w16, w8, w8);
        cmul2(q256, CB, Z256, q1, CB, z1);
        cmul2(z3, z2, z1, z4, z2, z2);
        cmul2(q512, q256, Z256, q2, CB, z2);
        cmul2(z5, z4, z1, z6, z4, z2);
        PUT4(0, CB, 1, q1, 8, q256, 2, q2);
        cmul2(z7, z4, z3, q3, CB, z3);
        cmul2(q768, q512, Z256, q4_, CB, z4);
        cmul2(q5, CB, z5, q1024, q768, Z256);
        cmul2(q6, CB, z6, q1280, q1024, Z256);
        PUT4(12, q512, 3, q3, 14, q768, 4, q4_);
        cmul2(q7, CB, z7, q1536, q1280, Z256);
        cmul2(r0, q256, z2, q1792, q1536, Z256);
        PUT4(5, q5, 16, q1024, 6, q6, 17, q1280);
        cmul2(r1, q256, z4, q2048, q1792, Z256);
        cmul2(r2, q256, z6, r3, q512, z4);
        PUT4(7, q7, 18, q1536, 9, r0, 19, q1792);
        cf pa{1.f, 0.f}, pb = w8, pc_, pd, w32;
        cmul2(r4, q768, z4, w32, w16, w16);
        cmul2(pc_, pa, w16, pd, pb, w16);
        PUT4(10, r1, 20, q2048, 11, r2, 13, r3);
        TS(4);
        sfor<0, 8>([&](auto J) {
            constexpr int jj = J.v;
            cf na, nb, nc, nd;
            if constexpr (jj < 7) { cmul2(na, pa, w32, nb, pb, w32); cmul2(nc, pc_, w32, nd, pd, w32); }
            PUT4(21 + 4 * jj, pa, 22 + 4 * jj, pb, 23 + 4 * jj, pc_, 24 + 4 * jj, pd);
            if constexpr (jj == 0) asm volatile("ds_write_addtid_b32 %0 offset:%1" ::"v"(pk2v(r4)), "i"(15 * KG_RS) : "memory");
            if constexpr (jj < 7) { pa = na; pb = nb; pc_ = nc; pd = nd; }
            if constexpr (jj == PUBLISH_AT) {
                SCHED();
                WAITV(0);
                BAR();
                if (tid == 0) { st_flag(ws, WS_FX + tb * 128, tag); st_flag(ws, WS_FXP + tb * 8, tag); }
                SCHED();
            }
        });
#undef PUT2
#undef PUT4
        if (tid < 2) *(LAS unsigned short*)(lds + L_KC + tid * 1312 + 649 * 2) = 0;
        LBAR();
    }
    TS(5);
    bf16x8 xf[32];
    const unsigned xvo = (lane & 31) * 32 + (lane >> 5) * 16, xsb = WS_XN + (mt * 4 + w) * 32 * 1024;
    unsigned xso;
#define LOAD_X(U) do { if constexpr (((U) & 3) == 0) aload_a_first<((U) >> 2) * 4096, 0>(xf[U], xso, rs, xvo, xsb); \
                       else aload_a_next<((U) & 3) * 1024>(xf[U], xso, rs, xvo); } while (0)
    {
        LAS char* qa = lds + KG_BASE + (w >> 1) * KG_CH + l15 * KG_RS + g16 * 16;
        LAS char* pa_ = qa + (21 + 16 * (w & 1)) * KG_RS;
        bf16x8 fa[8], fb[8], fc[8];
        u64 fxw;
        sfor<0, 8>([&](auto S) {
            constexpr int s_ = S.v;
            fb[s_] = *(LAS bf16x8*)(pa_ + s_ * 64); fa[s_] = *(LAS bf16x8*)(qa + s_ * 64); fc[s_] = *(LAS bf16x8*)(qa + 16 * KG_RS + s_ * 64);
            if constexpr (s_ < 7) STAGE_W1(1, s_);
            else aload8sc1(fxw, ws + WS_FX + (16 * mt + 4 * w) * 128, (lane & 3) * 128);
        });
        SCHED();
        TS(6);
        f32x4 k0, k1;
        sfor<0, 8>([&](auto S) {
            mfma16_vv<S.v == 0>(k0, fa[S.v], fb[S.v]);
            mfma16_vv<S.v == 0>(k1, fc[S.v], fb[S.v]);
        });
        SCHED();
        asm volatile("s_nop 7" : "+v"(k0), "+v"(k1));
#pragma unroll
        for (int r = 0; r < 4; r += 2) {
            const unsigned a0 = pk2(k0[r], k0[r + 1]), a1 = pk2(k1[r], k1[r + 1]);
            *(LAS unsigned short*)(size_t)ksc[r] = (unsigned short)a0;
            *(LAS unsigned short*)(size_t)ksc[r + 1] = (unsigned short)(a0 >> 16);
            *(LAS unsigned short*)(size_t)ksc[4 + r] = (unsigned short)a1;
            *(LAS unsigned short*)(size_t)ksc[5 + r] = (unsigned short)(a1 >> 16);
        }
        SCHED();
        asm volatile("s_waitcnt vmcnt(0)" : "+v"(fxw)::"memory");
        if (!__all(fxw == tag)) wait_flags<1>(ws, WS_FX + (16 * mt + 4 * w) * 128, 4, tag, lane);
        TS(7);
        sfor<0, 8>([&](auto U) { LOAD_X(U.v); });
        SCHED();
    }
    SCHED();
    LBAR();
    TS(8);
    {
        unsigned val[2][9];
#pragma unroll
        for (int ch = 0; ch < 2; ++ch)
#pragma unroll
            for (int e = 0; e < 9; ++e) val[ch][e] = *(LAS unsigned short*)(size_t)(kga[e] + ch * 1312);
        SCHED();
        sfor<0, 7>([&](auto I) { STAGE_W1(2, I.v); });
        {
            const int r = 35 * (w >> 1) + (lane < 35 ? lane : 34), row = (r * 147) >> 10;
            warm4(pf, (w & 1) ? wu : wg, (192 + 10 * j + row) * (F_ * 2) + (xcd & 3) * 896 + (r - 7 * row) * 128);
        }
#pragma unroll
        for (int ch = 0; ch < 2; ++ch) {
            u32x4 E, O;
#pragma unroll
            for (int m = 0; m < 4; ++m) { E[m] = val[ch][2 * m] | (val[ch][2 * m + 1] << 16); O[m] = val[ch][2 * m + 1] | (val[ch][2 * m + 2] << 16); }
            *(LAS u32x4*)(lds + L_KIMG + ch * CV_KCH + tid * 16) = E;
            *(LAS u32x4*)(lds + L_KIMG + ch * CV_KCH + 4160 + tid * 16) = O;
        }
        if (tid < 16) *(LAS u32x4*)(lds + L_KIMG + (tid >> 3) * CV_KCH + ((tid >> 2) & 1) * 4160 + 4096 + (tid & 3) * 16) = u32x4{0, 0, 0, 0};
    }
    SCHED();
    TS(9);

    f32x16 acc[7];
    u32x4 uu0, uu1;
    bf16x8 wf[28][2];
    const unsigned wtvo0 = lane * 16 + (w & 1) * 1024, wtvo1 = wtvo0 ^ 1024, wtsb = WS_WT + (28 * w * 16 + 2 * dt2) * 1024;
    unsigned wtso;
#define LOAD_WT(I) do { if constexpr (((I) & 1) == 0) aload_a_first<((I) >> 1) * 16384, 0>(wf[(I) >> 1][0], wtso, rs, wtvo0, wtsb); \
                        else aload_a_next<0>(wf[(I) >> 1][1], wtso, rs, wtvo1); } while (0)
    {
        const unsigned ab0 = lbase + (g16 & 1) * G1_UP + (8 * (g16 >> 1) + q4) * 224 + phi * 8, ab1 = ab0 + 2 * G1_SLOT, ab2 = ab0 + 4 * G1_SLOT;
        auto rdw = [&](auto STP, auto P) {
            constexpr int st = STP.v, p = P.v, sl = g1slot(st >> 2), off = (sl & 1) * G1_SLOT + (st & 3) * 16 * 224 + p * 32;
            const unsigned base = sl < 2 ? ab0 : sl < 4 ? ab1 : ab2;
            return cat8(atr<off>(base), atr<off + 4 * 224>(base));
        };
        bf16x8 cur[7], nxt[7];
        u32x4 fx[2];
        waitv<G1S.younger(1, 1, 0)>();
        LBAR();
        sfor<0, 7>([&](auto P) { cur[P.v] = rdw(IC<0>{}, P); });
        SCHED();
        TS(10);
        sfor<0, 32>([&](auto ST) {
            constexpr int st = ST.v, s = st >> 2, kk = st & 3;
            if constexpr (kk == 0 && s > 0) {
                G1_STAMP(2 * s - 2);
                if constexpr (s <= 6) waitv<G1S.younger(1, s + 1, st)>();
                BAR();
                G1_STAMP(2 * s - 1);
                SCHED();
            }
            if constexpr (st == 6) {
                asm volatile("s_waitcnt vmcnt(%2)" : "+v"(fx[0]), "+v"(fx[1]) : "i"(G1S.younger(3, 1, st)) : "memory");
                const u64 f0 = ((u64)fx[0][1] << 32) | fx[0][0], f1 = ((u64)fx[0][3] << 32) | fx[0][2], f2 = ((u64)fx[1][1] << 32) | fx[1][0], f3 = ((u64)fx[1][3] << 32) | fx[1][2];
                if (!__all(f0 == tag && f1 == tag && f2 == tag && f3 == tag)) wait_flags<4>(ws, WS_FX, 64, tag, lane);
                SCHED();
            }
            waitv<G1S.younger(0, st, st)>();
            sfor<0, 7>([&](auto P) {
                constexpr int p = P.v;
                if constexpr (p == 0) waitl<8>(); else if constexpr (p == 3) waitl<(st < 31 ? 10 : 4)>(); else if constexpr (p == 5) waitl<(st < 31 ? 10 : 0)>();
                mfma32_va<st == 0>(acc[p], cur[p], xf[st]);
                if constexpr (st < 31) nxt[p] = rdw(IC<st + 1>{}, P);
                sfor<0, G1S.n>([&](auto K) {
                    constexpr G1Op o = G1S.op[K.v];
                    if constexpr (o.step == st && o.gap == p) {
                        if constexpr (o.kind == 0) LOAD_X(o.a);
                        else if constexpr (o.kind == 1) STAGE_W1(o.a, o.b);
                        else if constexpr (o.kind == 2) LOAD_WT(o.a);
                        else if constexpr (o.kind == 3) aload_v<o.a * 16>(fx[o.a], rs, WS_FXP + lane * 32, 0);
                        else aload_v<0>(o.a == 0 ? uu0 : uu1, rs, WS_XT + ((tid >> 1) * 512 + c0 + (tid & 1)) * 16, o.a * 128 * 512 * 16);
                    }
                });
                SCHED();
            });
#pragma unroll
            for (int p = 0; p < 7; ++p) cur[p] = nxt[p];
        });
    }
    MFMA_DRAIN();
    TS(11);
    asm volatile("s_waitcnt vmcnt(%3)" : "+v"(uu0), "+v"(uu1), "+v"(pf) : "i"(G1S.younger(4, 1, 32)) : "memory");
    if (tid < 64) *(LAS u32x4*)(lds + L_U + (tid >> 5) * CV_UCH + (tid & 31) * 16) = u32x4{0, 0, 0, 0};
    *(LAS u32x4*)(lds + L_U + (tid & 1) * CV_UCH + 512 + (tid >> 1) * 16) = uu0;
    *(LAS u32x4*)(lds + L_U + (tid & 1) * CV_UCH + 512 + 2048 + (tid >> 1) * 16) = uu1;
    SCHED();
    TS(12);
    const int chl = w >> 1, rh = w & 1;
    LAS char* ub = lds + L_U + chl * CV_UCH + 2 * (256 + 16 * (l15 - (g16 >> 1)) + 8 * (g16 & 1)) - 32 * (8 * rh + 6);
    LAS char* kbm[8];
    {
        const int i0 = 2047 - 16 * (g16 >> 1) - l15 + 8 * (g16 & 1);
        LAS char* kb = lds + L_KIMG + chl * CV_KCH + ((l15 & 1) ? 0 : 4160) + 4 * (i0 >> 1) - 32 * (8 * rh + 6);
#pragma unroll
        for (int m = 0; m < 8; ++m) { kbm[m] = kb - 512 * m; asm volatile("" : "+v"(kbm[m])); }
    }
    auto ldG = [&](int m, int rr) {
        const LAS unsigned* p = (const LAS unsigned*)(kbm[m] + 64 * (3 - rr));
        u32x4 t; t[0] = p[0]; t[1] = p[1]; t[2] = p[2]; t[3] = p[3];
        return __builtin_bit_cast(bf16x8, t);
    };
    auto ldF = [&](int m, int rr) { return *(const LAS bf16x8*)(ub + 64 * (3 - rr) + 512 * m); };
    bf16x8 G[2][8], Fr[2][8];
    {
        const unsigned hvo = WS_H + (32 * (w & 1) + (lane & 31)) * 32 + (lane >> 5) * 16, hso = ((mt * 2 + (w >> 1)) * 112 + chunk * 7) * 2048;
        sfor<0, 7>([&](auto P) {
            constexpr int p = P.v;
            u32x4 o;
            f32x2 e[4], gu[4];
#pragma unroll
            for (int r = 0; r < 4; ++r) {
                const f32x2 g = {acc[p][2 * r], acc[p][2 * r + 1]}, u = {acc[p][8 + 2 * r], acc[p][9 + 2 * r]};
                const f32x2 t = g * -1.44269504088896341f;
                e[r] = f32x2{__builtin_amdgcn_exp2f(t[0]), __builtin_amdgcn_exp2f(t[1])};
                gu[r] = g * u;
            }
            SCHED();
            sfor<G1_WT + (2 * p * WT_EPI) / 14, G1_WT + ((2 * p + 1) * WT_EPI) / 14>([&](auto I) { LOAD_WT(I.v); });
#pragma unroll
            for (int r = 0; r < 4; ++r) {
                const f32x2 d = e[r] + 1.0f;
                e[r] = f32x2{__builtin_amdgcn_rcpf(d[0]), __builtin_amdgcn_rcpf(d[1])};
            }
#pragma unroll
            for (int r = 0; r < 4; ++r) o[r] = pk2v(gu[r] * e[r]);
            SCHED();
            sfor<G1_WT + ((2 * p + 1) * WT_EPI) / 14, G1_WT + ((2 * p + 2) * WT_EPI) / 14>([&](auto I) { LOAD_WT(I.v); });
            st128(rs, o, hvo, hso + p * 2048);
            SCHED();
            if constexpr (p == 2) { LBAR(); SCHED(); }
            if constexpr (p >= 3) { G[0][2 * p - 6] = ldG(2 * p - 6, 0); G[0][2 * p - 5] = ldG(2 * p - 5, 0); Fr[0][2 * p - 6] = ldF(2 * p - 6, 0); Fr[0][2 * p - 5] = ldF(2 * p - 5, 0); SCHED(); }
        });
    }
    TS(13);

    constexpr int PD = 12;
    bf16x8 hf[PD][2];
    const unsigned hvoff0 = WS_H + (lane & 31) * 32 + (lane >> 5) * 16 + (w >> 1) * 1024, hvoff1 = hvoff0 ^ 1024;
    const unsigned hsoff = ((mt2 * 2 + th2) * 112 + 28 * w) * 2048;
    auto ldh = [&](int s, int t) { return asbf(ld128(rs, (t ? hvoff1 : hvoff0) + (s & 1) * 2048, hsoff + (s >> 1) * 4096)); };
    f32x4 cacc[8];
    {
        u64 fh;
        sfor<0, 4>([&](auto RR) {
            constexpr int rr = RR.v, c = rr & 1, n = c ^ 1;
            CV_STAMP(rr);
            if constexpr (rr == 1) {
                waitv<((56 - G1_WT - WT_EPI + 2) / 3 < 9 ? (56 - G1_WT - WT_EPI + 2) / 3 : 9)>();
                BAR();
                if (tid == 0) st_flag(ws, WS_FH + (mt * 16 + chunk) * 128, tag);
                SCHED();
            }
            if constexpr (rr == 3) {
                asm volatile("s_waitcnt vmcnt(0)" : "+v"(fh)::"memory");
                if (!__all(fh == tag)) wait_flags<1>(ws, WS_FH + (mt2 * 16 + 4 * w) * 128, 4, tag, lane);
                SCHED();
            }
            sfor<0, 36>([&](auto C) {
                constexpr int cnt = C.v;
                constexpr int m = cnt < 8 ? 0 : cnt < 15 ? 1 : cnt < 21 ? 2 : cnt < 26 ? 3 : cnt < 30 ? 4 : cnt < 33 ? 5 : cnt < 35 ? 6 : 7;
                constexpr int first = m * 8 - (m * (m - 1)) / 2, i = m + (cnt - first);
                mfma16_vv<rr == 0 && m == 0>(cacc[i], G[c][m], Fr[c][i - m]);
                if constexpr (rr < 3) {
                    if constexpr (cnt < 8) Fr[n][cnt] = ldF(cnt, rr + 1);
                    else if constexpr (cnt < 24 && !(cnt & 1)) G[n][(cnt - 8) >> 1] = ldG((cnt - 8) >> 1, rr + 1);
                }
                if constexpr (rr < 3 && cnt % 4 == 2) {
                    constexpr int nrest = 56 - G1_WT - WT_EPI, per = (nrest + 2) / 3, k = cnt / 4, i = G1_WT + WT_EPI + rr * per + k;
                    if constexpr (k < per && i < 56) LOAD_WT(i);
                }
                if constexpr (rr == 2 && cnt == 8) aload8sc1(fh, ws + WS_FH + (mt2 * 16 + 4 * w) * 128, (lane & 3) * 128);
                if constexpr (rr == 3 && cnt % 3 == 1 && cnt / 3 < 10) hf[cnt / 6][(cnt / 3) & 1] = ldh(cnt / 6, (cnt / 3) & 1);
                SCHED();
            });
        });
    }
    MFMA_DRAIN();
    TS(14);
    {
        LAS char* red = lds + L_RED;
#pragma unroll
        for (int i = 0; i < 4; ++i) {
            f32x4 lo = cacc[i], hi = cacc[4 + i];
            asm volatile("" : "+v"(lo), "+v"(hi));
            *(LAS f32x4*)(red + (w * 4 + i) * 1024 + lane * 16) = rh ? lo : hi;
            cacc[i] = rh ? hi : lo;
            hf[5 + i][0] = ldh(5 + i, 0); hf[5 + i][1] = ldh(5 + i, 1);
            SCHED();
        }
        LBAR();
        const float dsk = bf2f(__builtin_amdgcn_readfirstlane((int)p_dsk));
#pragma unroll
        for (int i = 0; i < 4; ++i) {
            const f32x4 y = cacc[i] + *(LAS f32x4*)(red + ((w ^ 1) * 4 + i) * 1024 + lane * 16);
            const int t = 256 * (4 * rh + i) + 16 * l15 + 4 * g16;
            const u32x2 uu = *(LAS u32x2*)(lds + L_U + chl * CV_UCH + 2 * (256 + t));
            cacc[i] = f32x4{y[0] + bflo(uu[0]) * dsk, y[1] + bfhi(uu[0]) * dsk, y[2] + bflo(uu[1]) * dsk, y[3] + bfhi(uu[1]) * dsk};
            if (i < 3) { hf[9 + i][0] = ldh(9 + i, 0); hf[9 + i][1] = ldh(9 + i, 1); }
            SCHED();
        }
#pragma unroll
        for (int i = 0; i < 4; ++i) {
            const int t = 256 * (4 * rh + i) + 16 * l15 + 4 * g16;
            st64(rs, u32x2{pk2(cacc[i][0], cacc[i][1]), pk2(cacc[i][2], cacc[i][3])}, WS_ST + ((c0 + chl) * L_ + t) * 2, 0);
        }
    }
    SCHED();
    TS(15);

    f32x16 a2[2][2];
    u32x4 sv[2], xv[2];
    u64 fs;
    const int d0 = 64 * dt2, t0 = mt2 * 128 + th2 * 64;
    const int tt2 = w >> 1, dh2 = w & 1;
    sfor<0, 24>([&](auto S) {
        constexpr int s = S.v;
        const bf16x8 h0 = hf[s % PD][0], h1 = hf[s % PD][1];
        mfma32_av<s == 0>(a2[0][0], wf[s][0], h0);
        mfma32_av<s == 0>(a2[1][0], wf[s][1], h0);
        SCHED();
        mfma32_av<s == 0>(a2[0][1], wf[s][0], h1);
        mfma32_av<s == 0>(a2[1][1], wf[s][1], h1);
        sfor<PD, 28>([&](auto T) {
            constexpr int t = T.v;
            if constexpr (((t - PD) * 21) / 16 == s) { hf[t % PD][0] = ldh(t, 0); hf[t % PD][1] = ldh(t, 1); }
        });
        if constexpr (s == 10) {
            G2_STAMP(0);
            waitv<G2_YOUNGER>();
            BAR();
            G2_STAMP(1);
            if (tid == 0) st_flag(ws, WS_FS + tb * 8, tag);
        }
        if constexpr (s == 17) aload8sc1(fs, ws + WS_FS + (d0 >> 1) * 8, (lane & 31) * 8);
        if constexpr (s == 21) {
            G2_STAMP(2);
            asm volatile("s_waitcnt vmcnt(%1)" : "+v"(fs) : "i"(G2_AFTER_FS) : "memory");
            G2_STAMP(3);
            if (!__all(fs == tag)) wait_flags<1, 8>(ws, WS_FS + (d0 >> 1) * 8, 32, tag, lane);
#pragma unroll
            for (int i = 0; i < 2; ++i) {
                const int e = tid + 256 * i;
                sv[i] = ld128(rs, WS_ST + ((d0 + (e >> 3)) * L_ + t0) * 2 + (e & 7) * 16, 0);
            }
#pragma unroll
            for (int g = 0; g < 2; ++g) xv[g] = *(const u32x4*)(x + (size_t)(t0 + 32 * tt2 + (lane & 31)) * D_ + d0 + 32 * dh2 + 16 * g + 8 * (lane >> 5));
        }
        SCHED();
    });
    {
        auto put = [&](auto DH, auto TT) {
            const int q = 2 * (TT.v ^ (w >> 1)) + (DH.v ^ (w & 1));
            const f32x16 a = a2[DH.v][TT.v];
            LAS char* p = lds + (q * 4 + w) * 4096 + lane * 16;
#pragma unroll
            for (int g = 0; g < 4; ++g) *(LAS f32x4*)(p + g * 1024) = f32x4{a[4 * g], a[4 * g + 1], a[4 * g + 2], a[4 * g + 3]};
        };
        sfor<24, 28>([&](auto S) { mfma32_av<false>(a2[1][1], wf[S.v][1], hf[S.v % PD][1]); });
        sfor<24, 28>([&](auto S) { mfma32_av<false>(a2[1][0], wf[S.v][1], hf[S.v % PD][0]); });
        SCHED();
        put(IC<1>{}, IC<1>{});
        SCHED();
        sfor<24, 28>([&](auto S) { mfma32_av<false>(a2[0][1], wf[S.v][0], hf[S.v % PD][1]); });
        SCHED();
        put(IC<1>{}, IC<0>{});
        SCHED();
        sfor<24, 28>([&](auto S) { mfma32_av<false>(a2[0][0], wf[S.v][0], hf[S.v % PD][0]); });
        SCHED();
        put(IC<0>{}, IC<1>{});
#pragma unroll
        for (int i = 0; i < 2; ++i) *(LAS u32x4*)(lds + L_ST + (tid + 256 * i) * 16) = sv[i];
        MFMA_DRAIN();
        TS(16);
        LBAR();
    }
    TS(17);
    {
        const f32x16 own = a2[0][0];
        f32x4 f[4];
#pragma unroll
        for (int g = 0; g < 4; ++g) {
            f[g] = f32x4{own[4 * g], own[4 * g + 1], own[4 * g + 2], own[4 * g + 3]};
#pragma unroll
            for (int o = 1; o < 4; ++o) f[g] += *(LAS f32x4*)(lds + ((w * 4 + ((w + o) & 3)) * 4 + g) * 1024 + lane * 16);
        }
        LAS char* sp = lds + L_ST + (32 * dh2 + 8 * (lane >> 5) + q4) * 128 + (32 * tt2 + 16 * (g16 & 1) + 4 * pp) * 2;
#pragma unroll
        for (int g2 = 0; g2 < 2; ++g2) {
            const s16x4 sa = tr_read(sp + g2 * 16 * 128), sb = tr_read(sp + (g2 * 16 + 4) * 128);
            const u32x2 s01 = __builtin_bit_cast(u32x2, sa), s23 = __builtin_bit_cast(u32x2, sb);
            u32x4 o;
            o[0] = pk2v(unpk(xv[g2][0]) + unpk(s01[0]) * silu2(f32x2{f[2 * g2][0], f[2 * g2][1]}));
            o[1] = pk2v(unpk(xv[g2][1]) + unpk(s01[1]) * silu2(f32x2{f[2 * g2][2], f[2 * g2][3]}));
            o[2] = pk2v(unpk(xv[g2][2]) + unpk(s23[0]) * silu2(f32x2{f[2 * g2 + 1][0], f[2 * g2 + 1][1]}));
            o[3] = pk2v(unpk(xv[g2][3]) + unpk(s23[1]) * silu2(f32x2{f[2 * g2 + 1][2], f[2 * g2 + 1][3]}));
            *(u32x4*)(out + (size_t)(t0 + 32 * tt2 + (lane & 31)) * D_ + d0 + 32 * dh2 + 16 * g2 + 8 * (lane >> 5)) = o;
        }
    }
    if (b == PROBE_B && w == 0) {
        asm volatile("s_waitcnt vmcnt(0)" ::: "memory");
        u64 mn = ~0ull;
        for (int i = 0; i < 4; ++i) { const u64 v = ld_flag(ws, (20u << 20) + (lane * 4 + i) * 8); mn = v < mn ? v : mn; }
        for (int o = 32; o >= 1; o >>= 1) { const u64 a = __shfl_xor(mn, o); mn = a < mn ? a : mn; }
        const u64 now = __builtin_amdgcn_s_memrealtime();
        const u64 until = now + 0 * (0);
        while (__builtin_amdgcn_s_memrealtime() < until) __builtin_amdgcn_s_sleep(1);
    }
    if (b == 0 && tid == 0) st_flag(ws, WS_EP, tag);
    TS(18);
    TSR(47);
    TS_DUMP();
    asm volatile(".Lfused_pf_end:" ::: "memory");
}

extern "C" void kernel_launch(void* const* d_in, const int* in_sizes, int n_in, void* d_out, int out_size, void* d_ws, size_t ws_size,
                              hipStream_t stream) {
    (void)hipFuncSetAttribute((const void*)fused_s4_ffn, hipFuncAttributeMaxDynamicSharedMemorySize, LDS_TOTAL);
    fused_s4_ffn<<<256, 256, LDS_TOTAL, stream>>>((const bf16_t*)d_in[0], (const bf16_t*)d_in[1], (const bf16_t*)d_in[2], (const bf16_t*)d_in[3],
                                                  (const bf16_t*)d_in[4], (const bf16_t*)d_in[5], (const bf16_t*)d_in[6], (const bf16_t*)d_in[7],
                                                  (const bf16_t*)d_in[8], (const bf16_t*)d_in[10], (const bf16_t*)d_in[11], (const bf16_t*)d_in[12],
                                                  (bf16_t*)d_out, (char*)d_ws);
}
```

```cpp
#include <hip/hip_runtime.h>
#include <stdint.h>

typedef unsigned short bf16_t;
typedef short bf16x8 __attribute__((ext_vector_type(8)));
typedef short s16x4 __attribute__((ext_vector_type(4)));
typedef float f32x2 __attribute__((ext_vector_type(2)));
typedef float f32x4 __attribute__((ext_vector_type(4)));
typedef float f32x16 __attribute__((ext_vector_type(16)));
typedef unsigned u32x4 __attribute__((ext_vector_type(4)));
typedef unsigned u32x2 __attribute__((ext_vector_type(2)));
typedef __bf16 bf16v2 __attribute__((ext_vector_type(2)));
typedef unsigned long long u64;
#define LAS __attribute__((address_space(3)))
#define GAS __attribute__((address_space(1)))
#define DI __device__ __forceinline__

#define XSCHED 0
#define WSCHED 0
#define WARM_AT 0
constexpr int L_ = 2048, D_ = 512, N_ = 128, F_ = 1792;

constexpr unsigned WS_XN = 0;
constexpr unsigned WS_XT = 2u << 20;
constexpr unsigned WS_ST = 4u << 20;
constexpr unsigned WS_H = 6u << 20;
constexpr unsigned WS_WT = 14u << 20;
constexpr unsigned WS_FX = 16u << 20;
constexpr unsigned WS_FH = WS_FX + 256 * 128;
constexpr unsigned WS_FS = WS_FH + 256 * 128;
constexpr unsigned WS_FXP = WS_FS + 256 * 128;
constexpr unsigned WS_EP = WS_FXP + 256 * 8;
constexpr unsigned WS_USED = 17u << 20;

constexpr int G1_SLOT = 28800, G1_UP = 14336 + 128;
constexpr int g1slot(int s) { return s % 5 == 0 ? 0 : s % 5 == 1 ? 3 : s % 5 == 2 ? 4 : s % 5 == 3 ? 1 : 2; }
constexpr int KG_BASE = G1_SLOT, KG_RS = 528, KG_CH = 53 * KG_RS;
constexpr int L_WTS = 3 * G1_SLOT;
constexpr int L_KC = 144128;
constexpr int L_KIMG = 146816;
constexpr int L_LNS = L_KIMG, LN_RS = 1056;
constexpr int CV_KCH = 8320;
constexpr int L_U = 2 * G1_SLOT, CV_UCH = 4608;
constexpr int L_RED = L_U + 2 * CV_UCH;
constexpr int L_ST = 65536;
constexpr int LDS_TOTAL = 163840;
static_assert(KG_BASE + 2 * KG_CH <= 3 * G1_SLOT && KG_BASE + KG_CH + 256 < 65536, "K-gen scratch");
static_assert(L_KIMG + 2 * CV_KCH <= LDS_TOTAL && L_RED + 16384 <= 3 * G1_SLOT && L_ST + 8192 <= 3 * G1_SLOT, "lds map");

DI float bf2f(unsigned u16) { return __uint_as_float(u16 << 16); }
DI float bflo(unsigned w) { return __uint_as_float(w << 16); }
DI float bfhi(unsigned w) { return __uint_as_float(w & 0xffff0000u); }
DI unsigned pk2v(f32x2 v) { return __builtin_bit_cast(unsigned, __builtin_convertvector(v, bf16v2)); }
DI unsigned pk2(float lo, float hi) { return pk2v(f32x2{lo, hi}); }
DI f32x2 unpk(unsigned w) { return f32x2{bflo(w), bfhi(w)}; }
DI float rbf(float x) { return bflo(pk2(x, x)); }

#define WAITV(n) asm volatile("s_waitcnt vmcnt(" #n ")" ::: "memory")
#define WAITL(n) asm volatile("s_waitcnt lgkmcnt(" #n ")" ::: "memory")
#define BAR() do { asm volatile("" ::: "memory"); __builtin_amdgcn_s_barrier(); asm volatile("" ::: "memory"); } while (0)
#define LBAR() do { WAITL(0); BAR(); } while (0)
#define SCHED() __builtin_amdgcn_sched_barrier(0)

#define TS(i) do {} while (0)
#define TSR(i) do {} while (0)
#define TS_DUMP() do {} while (0)

template <int N> struct IC { static constexpr int v = N; };
template <int B, int E, class F> DI void sfor(F&& f) { if constexpr (B < E) { f(IC<B>{}); sfor<B + 1, E>(f); } }
template <int N> DI void waitv() { asm volatile("s_waitcnt vmcnt(%0)" ::"i"(N) : "memory"); }
template <int N> DI void waitl() { asm volatile("s_waitcnt lgkmcnt(%0)" ::"i"(N) : "memory"); }

template <int CTRL, int RMASK> DI float dpp_add(float v) {
    return v + __builtin_bit_cast(float, __builtin_amdgcn_update_dpp(0, __builtin_bit_cast(int, v), CTRL, RMASK, 0xf, true));
}
DI void wave_sum4(float& a, float& b, float& c, float& d) {
#define STEP(C, M) a = dpp_add<C, M>(a); b = dpp_add<C, M>(b); c = dpp_add<C, M>(c); d = dpp_add<C, M>(d);
    STEP(0x111, 0xf) STEP(0x112, 0xf) STEP(0x114, 0xf) STEP(0x118, 0xf) STEP(0x142, 0xa) STEP(0x143, 0xc)
#undef STEP
    a = __builtin_bit_cast(float, __builtin_amdgcn_readlane(__builtin_bit_cast(int, a), 63));
    b = __builtin_bit_cast(float, __builtin_amdgcn_readlane(__builtin_bit_cast(int, b), 63));
    c = __builtin_bit_cast(float, __builtin_amdgcn_readlane(__builtin_bit_cast(int, c), 63));
    d = __builtin_bit_cast(float, __builtin_amdgcn_readlane(__builtin_bit_cast(int, d), 63));
}
DI s16x4 tr_read(LAS char* p) { return __builtin_amdgcn_ds_read_tr16_b64_v4i16((LAS s16x4*)p); }
template <int OFF> DI s16x4 atr(unsigned addr) { s16x4 r; asm volatile("ds_read_b64_tr_b16 %0, %1 offset:%2" : "=v"(r) : "v"(addr), "i"(OFF)); return r; }
DI bf16x8 cat8(s16x4 a, s16x4 b) { return __builtin_shufflevector(a, b, 0, 1, 2, 3, 4, 5, 6, 7); }

typedef __amdgpu_buffer_rsrc_t rsrc_t;
constexpr int SC1 = 16;
DI u32x4 ld128(rsrc_t r, unsigned voff, unsigned soff) { return __builtin_amdgcn_raw_buffer_load_b128(r, voff, soff, SC1); }
DI void st128(rsrc_t r, u32x4 v, unsigned voff, unsigned soff) {
    asm volatile("buffer_store_dwordx4 %0, %1, %2, %3 offen sc1\n\ts_nop 1" ::"v"(v), "v"(voff), "s"(r), "s"(soff) : "memory");
}
DI void st64(rsrc_t r, u32x2 v, unsigned voff, unsigned soff) { __builtin_amdgcn_raw_buffer_store_b64(v, r, voff, soff, SC1); }
DI bf16x8 asbf(u32x4 v) { return __builtin_bit_cast(bf16x8, v); }

template <int SIMM, int LIMM, int IMM> DI void bdma_first(unsigned& so, rsrc_t r, unsigned voff, unsigned sbase, unsigned lbase) {
    asm volatile("s_add_u32 m0, %1, %2\n\ts_add_u32 %0, %3, %4\n\tbuffer_load_dwordx4 %5, %6, %0 offen offset:%7 lds"
                 : "=&s"(so) : "s"(lbase), "i"(LIMM), "s"(sbase), "i"(SIMM), "v"(voff), "s"(r), "i"(IMM) : "memory", "scc");
}
template <int IMM> DI void bdma_next(unsigned so, rsrc_t r, unsigned voff) {
    asm volatile("buffer_load_dwordx4 %0, %1, %2 offen offset:%3 lds" ::"v"(voff), "s"(r), "s"(so), "i"(IMM) : "memory");
}
template <int SIMM, int IMM> DI void aload_a_first(bf16x8& d, unsigned& so, rsrc_t r, unsigned voff, unsigned sbase) {
    asm volatile("s_add_u32 %1, %4, %5\n\tbuffer_load_dwordx4 %0, %2, %3, %1 offen offset:%6 sc1"
                 : "=&a"(d), "=&s"(so) : "v"(voff), "s"(r), "s"(sbase), "i"(SIMM), "i"(IMM) : "memory", "scc");
}
template <int IMM> DI void aload_a_next(bf16x8& d, unsigned so, rsrc_t r, unsigned voff) {
    asm volatile("buffer_load_dwordx4 %0, %1, %2, %3 offen offset:%4 sc1" : "=&a"(d) : "v"(voff), "s"(r), "s"(so), "i"(IMM) : "memory");
}
template <int IMM> DI void aload_v(u32x4& d, rsrc_t r, unsigned voff, unsigned so) {
    asm volatile("buffer_load_dwordx4 %0, %1, %2, %3 offen offset:%4 sc1" : "=&v"(d) : "v"(voff), "s"(r), "s"(so), "i"(IMM) : "memory");
}
DI void warm4(unsigned& pf, const void* sbase, unsigned voff) { asm volatile("global_load_dword %0, %1, %2" : "+v"(pf) : "v"(voff), "s"(sbase) : "memory"); }
DI void aload16(u32x4& d, const void* sbase, unsigned voff) { asm volatile("global_load_dwordx4 %0, %1, %2" : "=&v"(d) : "v"(voff), "s"(sbase) : "memory"); }
DI void aload2(unsigned& d, const void* sbase, unsigned voff) { asm volatile("global_load_ushort %0, %1, %2" : "=&v"(d) : "v"(voff), "s"(sbase) : "memory"); }
DI void aload8sc1(u64& d, const void* sbase, unsigned voff) { asm volatile("global_load_dwordx2 %0, %1, %2 sc1" : "=&v"(d) : "v"(voff), "s"(sbase) : "memory"); }

template <bool FIRST> DI void mfma32_va(f32x16& c, bf16x8 a, bf16x8 b) {
    if constexpr (FIRST) asm volatile("v_mfma_f32_32x32x16_bf16 %0, %1, %2, 0" : "=&v"(c) : "v"(a), "a"(b));
    else asm volatile("v_mfma_f32_32x32x16_bf16 %0, %1, %2, %0" : "+v"(c) : "v"(a), "a"(b));
}
template <bool FIRST> DI void mfma32_av(f32x16& c, bf16x8 a, bf16x8 b) {
    if constexpr (FIRST) asm volatile("v_mfma_f32_32x32x16_bf16 %0, %1, %2, 0" : "=&v"(c) : "a"(a), "v"(b));
    else asm volatile("v_mfma_f32_32x32x16_bf16 %0, %1, %2, %0" : "+v"(c) : "a"(a), "v"(b));
}
template <bool FIRST> DI void mfma16_vv(f32x4& c, bf16x8 a, bf16x8 b) {
    if constexpr (FIRST) asm volatile("v_mfma_f32_16x16x32_bf16 %0, %1, %2, 0" : "=&v"(c) : "v"(a), "v"(b));
    else asm volatile("v_mfma_f32_16x16x32_bf16 %0, %1, %2, %0" : "+v"(c) : "v"(a), "v"(b));
}
#define MFMA_DRAIN() asm volatile("s_nop 15\n\ts_nop 3" ::: "memory")

#define WT_LOOP 24
#define WT_EPI 14
constexpr int G1_WT = WT_LOOP;
struct G1Op { int step, gap, kind, a, b; };
struct G1Sched {
    G1Op op[160]; int n;
    constexpr G1Sched() : op{}, n(0) {
        for (int i = 0; i < 7; ++i) op[n++] = G1Op{-1, 0, 1, 1, i};
        for (int u = 0; u < 8; ++u) op[n++] = G1Op{-1, 0, 0, u, 0};
        for (int i = 0; i < 7; ++i) op[n++] = G1Op{-1, 0, 1, 2, i};
        if (WARM_AT == 0) op[n++] = G1Op{-1, 0, 6, 0, 0};
        int wt = 0;
        for (int s = 0; s < 8; ++s) {
            G1Op list[24] = {}; int m = 0;
            if (s == 0) for (int i = 0; i < 2; ++i) list[m++] = G1Op{0, 0, 3, i, 0};
            if (s < 3) { for (int i = 0; i < 4; ++i) list[m++] = G1Op{0, 0, 0, 8 + 8 * s + i, 0};
                         for (int i = 0; i < 4; ++i) list[m++] = G1Op{0, 0, 1, 3 + s, i};
                         for (int i = 4; i < 8; ++i) list[m++] = G1Op{0, 0, 0, 8 + 8 * s + i, 0};
                         for (int i = 4; i < 7; ++i) list[m++] = G1Op{0, 0, 1, 3 + s, i}; }
            if (s == 3 || s == 4) for (int i = 0; i < 7; ++i) list[m++] = G1Op{0, 0, 1, 3 + s, i};
            if (s == 5) for (int i = 0; i < 2; ++i) list[m++] = G1Op{0, 0, 4, i, 0};
            const int nw = s < 5 ? 0 : s == 5 ? G1_WT / 4 / 2 * 2 : s == 6 ? (G1_WT - G1_WT / 4 / 2 * 2) / 2 / 2 * 2 : G1_WT - wt;
            for (int i = 0; i < nw; ++i) list[m++] = G1Op{0, 0, 2, wt++, 0};
            const int span = s == 7 ? 21 : 28;
            for (int i = 0; i < m; ++i) { const int g = (i * span) / m; list[i].step = 4 * s + g / 7; list[i].gap = g % 7; op[n++] = list[i]; }
        }
    }
    constexpr int younger(int kind, int a, int st) const {
        int last = -1;
        for (int i = 0; i < n; ++i) if (op[i].kind == kind && op[i].a == a) last = i;
        int c = 0;
        for (int i = last + 1; i < n; ++i) if (op[i].step < st) ++c;
        return c;
    }
};
constexpr G1Sched G1S{};

DI u64 ld_flag(const char* ws, unsigned off) { return __hip_atomic_load((GAS u64*)(ws + off), __ATOMIC_RELAXED, __HIP_MEMORY_SCOPE_AGENT); }
DI void st_flag(char* ws, unsigned off, u64 v) { __hip_atomic_store((GAS u64*)(ws + off), v, __ATOMIC_RELAXED, __HIP_MEMORY_SCOPE_AGENT); }
template <int NPL, int STRIDE = 128>
DI void wait_flags(const char* ws, unsigned base, int nl, u64 tag, int lane) {
    const unsigned off = base + (lane < nl ? lane : 0) * STRIDE;
#pragma unroll 1
    for (int spin = 0; spin < 40000; ++spin) {
        bool ok = true;
#pragma unroll
        for (int i = 0; i < NPL; ++i) ok &= ld_flag(ws, off + i * 64 * STRIDE) == tag;
        if (__all(ok)) break;
        __builtin_amdgcn_s_sleep(1);
    }
    asm volatile("" ::: "memory");
}

typedef f32x2 cf;
DI cf cmul(cf a, cf b) { return cf{a[0] * b[0] - a[1] * b[1], a[0] * b[1] + a[1] * b[0]}; }
DI void cmul2(cf& z0, cf a0, cf b0, cf& z1, cf a1, cf b1) {
    cf t0, t1;
    asm("v_pk_mul_f32 %2, %4, %5 op_sel_hi:[0,1]\n\tv_pk_mul_f32 %3, %6, %7 op_sel_hi:[0,1]\n\t"
        "v_pk_fma_f32 %0, %4, %5, %2 op_sel:[1,1,0] op_sel_hi:[1,0,1] neg_lo:[0,1,0]\n\t"
        "v_pk_fma_f32 %1, %6, %7, %3 op_sel:[1,1,0] op_sel_hi:[1,0,1] neg_lo:[0,1,0]"
        : "=&v"(z0), "=&v"(z1), "=&v"(t0), "=&v"(t1) : "v"(a0), "v"(b0), "v"(a1), "v"(b1));
}
DI cf zdir(float a2, float th, float l) {
    const float m = __builtin_amdgcn_exp2f(a2 * l);
    const float r = __builtin_amdgcn_fractf(th * l);
    return cf{m * __builtin_amdgcn_cosf(r), m * __builtin_amdgcn_sinf(r)};
}
DI f32x2 silu2(f32x2 g) {
    const f32x2 t = g * -1.44269504088896341f;
    const f32x2 d = f32x2{__builtin_amdgcn_exp2f(t[0]), __builtin_amdgcn_exp2f(t[1])} + 1.0f;
    return g * f32x2{__builtin_amdgcn_rcpf(d[0]), __builtin_amdgcn_rcpf(d[1])};
}

#define PUBLISH_AT 3
constexpr int g2_younger() { int c = 0; for (int t = 12; t < 28; ++t) if (((t - 12) * 21) / 16 <= 10) c += 2; return c; }
#define G2_YOUNGER g2_younger()
constexpr int g2_after_fs() { int c = 0; for (int t = 12; t < 28; ++t) { const int st = ((t - 12) * 21) / 16; if (st > 17 && st <= 21) c += 2; } return c; }
#define G2_AFTER_FS g2_after_fs()
#define G1_STAMP(i) do {} while (0)
#define CV_STAMP(i) do {} while (0)
#define G2_STAMP(i) do {} while (0)

extern __shared__ __attribute__((aligned(16))) char dyn_smem[];

__global__ __launch_bounds__(256, 1) void fused_s4_ffn(const bf16_t* __restrict__ x, const bf16_t* __restrict__ A_re, const bf16_t* __restrict__ A_im,
                                                       const bf16_t* __restrict__ B_re, const bf16_t* __restrict__ B_im, const bf16_t* __restrict__ C_re,
                                                       const bf16_t* __restrict__ C_im, const bf16_t* __restrict__ log_dt, const bf16_t* __restrict__ Dskip,
                                                       const bf16_t* __restrict__ wg, const bf16_t* __restrict__ wu, const bf16_t* __restrict__ wd,
                                                       bf16_t* __restrict__ out, char* ws) {
    LAS char* lds = (LAS char*)dyn_smem;
    const unsigned lbase = (unsigned)(size_t)lds;
    const int tid = threadIdx.x, w = __builtin_amdgcn_readfirstlane(tid >> 6), lane = tid & 63;
    const int b = blockIdx.x, xcd = b & 7, j = b >> 3;
    const int tb = xcd * 32 + j;
    const int mt = 8 * (xcd >> 2) + (j >> 2), chunk = 4 * (xcd & 3) + (j & 3);
    const int mt2 = 2 * xcd + (j >> 4), th2 = (j >> 3) & 1, dt2 = j & 7;
    const int c0 = 2 * tb;
    TSR(46); TS(0);

    u32x4 raw[2];
#pragma unroll
    for (int r = 0; r < 2; ++r) aload16(raw[r], x + (size_t)(8 * tb + 2 * w + r) * D_, lane * 16);
    unsigned pf = 0;
    {
        u64 pc; unsigned len;
        asm volatile("s_getpc_b64 %0\n.Lfused_pf_here:\n\ts_mov_b32 %1, .Lfused_pf_end-.Lfused_pf_here" : "=s"(pc), "=s"(len));
        const unsigned o = (unsigned)(12 * j + (lane < 12 ? lane : 11)) * 128u;
        warm4(pf, (const void*)pc, o < len ? o : len);
    }
    u64 ep;
    aload8sc1(ep, ws + WS_EP, 0);
    const int kch = c0 + (w >> 1);
    const unsigned kpo = (64 * (w & 1) + lane) * 2;
    unsigned p_are, p_aim, p_br, p_bi, p_cr, p_ci, p_ldt, p_dsk;
    aload2(p_are, A_re + kch * N_, kpo); aload2(p_aim, A_im + kch * N_, kpo); aload2(p_br, B_re + kch * N_, kpo);
    aload2(p_bi, B_im + kch * N_, kpo); aload2(p_cr, C_re + kch * N_, kpo); aload2(p_ci, C_im + kch * N_, kpo);
    aload2(p_ldt, log_dt + kch, 0); aload2(p_dsk, Dskip + kch, 0);
    SCHED();
    const rsrc_t rs = __builtin_amdgcn_make_buffer_rsrc(ws, 0, WS_USED, 0x00020000);
    const rsrc_t rw1 = __builtin_amdgcn_make_buffer_rsrc((void*)((w & 1) ? wu : wg), 0, D_ * F_ * 2, 0x00020000);
    const rsrc_t rwd = __builtin_amdgcn_make_buffer_rsrc((void*)wd, 0, D_ * F_ * 2, 0x00020000);
    const int wtU0 = 7 * (b >> 4) + w, wtU1 = w < 3 ? wtU0 + 4 : wtU0;
    {
        unsigned so;
        const unsigned vo = (lane >> 2) * 1024 + (lane & 3) * 16;
        bdma_first<0, L_WTS, 0>(so, rwd, vo, wtU0 * 16384 + (b & 15) * 64, lbase + w * 2048);
        bdma_first<0, L_WTS + 1024, 0>(so, rwd, vo, wtU1 * 16384 + (b & 15) * 64, lbase + w * 2048);
    }
    unsigned voff[7];
    {
        const int k = (lane * 147) >> 11, c = lane - 14 * k;
        unsigned v = k * (F_ * 2) + c * 16;
        int cc = c;
#pragma unroll
        for (int i = 0; i < 7; ++i) {
            voff[i] = v - (i & 3) * 1024;
            cc += 8; v += 4 * (F_ * 2) + 128;
            if (cc >= 14) { cc -= 14; v += F_ * 2 - 224; }
        }
    }
    const unsigned w1s = (32 * (w >> 1)) * (F_ * 2) + chunk * 224;
    const unsigned w1l = lbase + (w & 1) * G1_UP + (w >> 1) * 7168;
    unsigned w1so;
#define STAGE_W1(S, I) do { if constexpr (((I) & 3) == 0) bdma_first<64 * (S) * (F_ * 2), g1slot(S) * G1_SLOT + (I) * 1024, 0>(w1so, rw1, voff[I], w1s, w1l); \
                            else bdma_next<((I) & 3) * 1024>(w1so, rw1, voff[I]); } while (0)
    SCHED();
    const int g16 = lane >> 4, q4 = (lane & 15) >> 2, pp = lane & 3, l15 = lane & 15;
    const int phi = ((pp & 1) << 1) | (pp >> 1);
    unsigned ksc[8];
    {
        const int col8 = 8 * (16 * (w & 1) + l15);
        const unsigned kcb = lbase + L_KC + (w >> 1) * 1312, kcf = kcb - 2 * 17024;
        const int A = (g16 == 0 ? 0 : g16 == 1 ? 4 : g16 == 2 ? 256 : 512) + col8, B0 = g16 == 2 ? 2 : g16 == 3 ? 4 : 1, B1 = g16 == 2 ? 4 : g16 == 3 ? 256 : 2;
#pragma unroll
        for (int r = 0; r < 4; ++r) {
            const int v = A + (r & 1) * B0 + (r >> 1) * B1;
            ksc[r] = v < 128 ? kcb + 2 * v : kcf + 2 * (__float_as_uint((float)v) >> 16);
            asm volatile("" : "+v"(ksc[r]));
        }
#pragma unroll
        for (int r = 0; r < 4; ++r) {
            const unsigned a = kcf + 2 * (__float_as_uint((float)(1024 + 256 * r + col8)) >> 16);
            ksc[4 + r] = g16 == 0 ? a : (r == 0 && g16 == 1 && col8 == 0) ? kcb + 2 * 640 : kcb + 2 * 648;
            asm volatile("" : "+v"(ksc[4 + r]));
        }
    }
    unsigned kga[9];
    {
        const float f = (float)(2047 - 8 * tid);
        const unsigned kcf = lbase + L_KC - 2 * 17024;
#pragma unroll
        for (int e = 0; e < 9; e += 2) {
            const unsigned bits = pk2(f - e, f - (e + 1));
            kga[e] = kcf + 2 * (bits & 0xffffu);
            if (e < 8) kga[e + 1] = kcf + 2 * (bits >> 16);
        }
        if (w == 3) {
#pragma unroll
            for (int e = 0; e < 9; ++e) {
                const int l = 2047 - 8 * tid - e;
                if (l < 256) kga[e] = lbase + L_KC + 2 * (l < 0 ? 649 : l);
            }
        }
#pragma unroll
        for (int e = 0; e < 9; ++e) asm volatile("" : "+v"(kga[e]));
    }
    SCHED();
    {
        const int r = 21 * (w >> 1) + (lane < 21 ? lane : 20), row = (r * 147) >> 10;
        warm4(pf, (w & 1) ? wu : wg, (6 * j + row) * (F_ * 2) + (xcd & 3) * 896 + (r - 7 * row) * 128);
    }
    if constexpr (WARM_AT == 1) {
        const int r = 35 * (w >> 1) + (lane < 35 ? lane : 34), row = (r * 147) >> 10;
        warm4(pf, (w & 1) ? wu : wg, (192 + 10 * j + row) * (F_ * 2) + (xcd & 3) * 896 + (r - 7 * row) * 128);
    }
    SCHED();
    asm volatile("s_waitcnt vmcnt(%11)" : "+v"(ep), "+v"(raw[0]), "+v"(raw[1]), "+v"(p_are), "+v"(p_aim), "+v"(p_br), "+v"(p_bi), "+v"(p_cr), "+v"(p_ci),
                 "+v"(p_ldt), "+v"(p_dsk) : "i"(WARM_AT == 1 ? 4 : 3) : "memory");
    SCHED();
    TS(1);
    if constexpr (WARM_AT == 2) {
        const int r = 35 * (w >> 1) + (lane < 35 ? lane : 34), row = (r * 147) >> 10;
        warm4(pf, (w & 1) ? wu : wg, (192 + 10 * j + row) * (F_ * 2) + (xcd & 3) * 896 + (r - 7 * row) * 128);
    }
    SCHED();
    {
        f32x2 v[2][4];
        float s1[2], s2[2], x0[2];
        sfor<0, 2>([&](auto R) {
            constexpr int r = R.v;
            x0[r] = bflo((unsigned)__builtin_amdgcn_readfirstlane((int)raw[r][0]));
            f32x2 a = {0.f, 0.f}, q = {0.f, 0.f};
#pragma unroll
            for (int jj = 0; jj < 4; ++jj) { v[r][jj] = unpk(raw[r][jj]); const f32x2 d = v[r][jj] - x0[r]; a += d; q += d * d; }
            s1[r] = a[0] + a[1]; s2[r] = q[0] + q[1];
            STAGE_W1(0, r);
            SCHED();
        });
        wave_sum4(s1[0], s2[0], s1[1], s2[1]);
        STAGE_W1(0, 2);
        SCHED();
        sfor<0, 2>([&](auto R) {
            constexpr int r = R.v;
            const float md = s1[r] * (1.0f / 512.0f);
            const float var = fmaxf(s2[r] * (1.0f / 512.0f) - md * md, 0.f);
            const float mb = rbf(x0[r] + md);
            const float sd = rbf(__builtin_amdgcn_sqrtf(rbf(rbf(var) + 1e-5f)));
            const float rsd = __builtin_amdgcn_rcpf(sd);
            STAGE_W1(0, 3 + 2 * r);
            SCHED();
            u32x4 o;
#pragma unroll
            for (int jj = 0; jj < 4; ++jj) o[jj] = pk2v(unpk(pk2v(v[r][jj] - mb)) * rsd);
            *(LAS u32x4*)(lds + L_LNS + (2 * w + r) * LN_RS + lane * 16) = o;
            STAGE_W1(0, 4 + 2 * r);
            SCHED();
        });
    }
    waitv<(WARM_AT == 0 ? 8 : 9)>();
    LBAR();
    TS(2);
    {
        LAS char* tp = lds + L_WTS + w * 2048 + (8 * (g16 >> 1) + q4) * 64 + (g16 & 1) * 32 + phi * 8;
        const bf16x8 f0 = cat8(tr_read(tp), tr_read(tp + 256)), f1 = cat8(tr_read(tp + 1024), tr_read(tp + 1024 + 256));
        st128(rs, __builtin_bit_cast(u32x4, f0), WS_WT + lane * 16, (wtU0 * 16 + (b & 15)) * 1024);
        if (w < 3) st128(rs, __builtin_bit_cast(u32x4, f1), WS_WT + lane * 16, (wtU1 * 16 + (b & 15)) * 1024);
    }
    SCHED();

    {
        const int half = tid & 1, tok = (tid >> 1) & 7, U = tid >> 4;
        const u32x4 va = *(LAS u32x4*)(lds + L_LNS + tok * LN_RS + U * 32 + half * 16);
        const u32x4 vb = *(LAS u32x4*)(lds + L_LNS + tok * LN_RS + U * 32 + half * 16 + 512);
        LAS char* tp = lds + L_LNS + q4 * LN_RS + (128 * w + 16 * g16 + 4 * pp) * 2;
        const s16x4 a0 = tr_read(tp), b0 = tr_read(tp + 4 * LN_RS), a1 = tr_read(tp + 128), b1 = tr_read(tp + 4 * LN_RS + 128);
        const unsigned xo = WS_XN + ((tb >> 2) * 32 + U) * 1024 + ((tb & 3) * 8 + tok) * 32 + half * 16;
        st128(rs, va, xo, 0);
        st128(rs, vb, xo, 16 * 1024);
        const unsigned to = WS_XT + (tb * 512 + 128 * w + 16 * g16 + l15) * 16;
        st128(rs, __builtin_bit_cast(u32x4, cat8(a0, b0)), to, 0);
        st128(rs, __builtin_bit_cast(u32x4, cat8(a1, b1)), to, 1024);
    }
    SCHED();
    const u64 tag = ((u64)(unsigned)__builtin_amdgcn_readfirstlane((int)(unsigned)ep) |
                     ((u64)(unsigned)__builtin_amdgcn_readfirstlane((int)(unsigned)(ep >> 32)) << 32)) + 1;
    TS(3);

    {
        const float dt = rbf(__builtin_amdgcn_exp2f(bf2f(p_ldt) * 1.44269504088896341f));
        const float lr = dt * bf2f(p_are), li = dt * bf2f(p_aim);
        const cf dB{dt * bf2f(p_br), dt * bf2f(p_bi)};
        const cf CBt = cmul(cf{bf2f(p_cr), bf2f(p_ci)}, dB);
        const cf CB{CBt[0], -CBt[1]};
        const float a2 = lr * 1.44269504088896341f, th = li * 0.15915494309189535f;
        asm volatile("s_mov_b32 m0, %0" ::"s"(lbase + KG_BASE + (w >> 1) * KG_CH + (w & 1) * 256) : "memory");
#define PUT2(R0, Z0, R1, Z1) asm volatile("ds_write_addtid_b32 %0 offset:%2\n\tds_write_addtid_b32 %1 offset:%3" \
            ::"v"(pk2v(Z0)), "v"(pk2v(Z1)), "i"((R0) * KG_RS), "i"((R1) * KG_RS) : "memory")
#define PUT4(R0, Z0, R1, Z1, R2, Z2, R3, Z3) asm volatile("ds_write_addtid_b32 %0 offset:%4\n\tds_write_addtid_b32 %1 offset:%5\n\tds_write_addtid_b32 %2 offset:%6\n\tds_write_addtid_b32 %3 offset:%7" \
            ::"v"(pk2v(Z0)), "v"(pk2v(Z1)), "v"(pk2v(Z2)), "v"(pk2v(Z3)), "i"((R0) * KG_RS), "i"((R1) * KG_RS), "i"((R2) * KG_RS), "i"((R3) * KG_RS) : "memory")
        const cf z1 = zdir(a2, -th, 1.f), Z256 = zdir(a2, -th, 256.f), w8 = zdir(a2, th, 8.f);
        cf z2, z3, z4, z5, z6, z7, w16, q1, q2, q3, q4_, q5, q6, q7, q256, q512, q768, q1024, q1280, q1536, q1792, q2048, r0, r1, r2, r3, r4;
        cmul2(z2, z1, z1, w16, w8, w8);
        cmul2(q256, CB, Z256, q1, CB, z1);
        cmul2(z3, z2, z1, z4, z2, z2);
        cmul2(q512, q256, Z256, q2, CB, z2);
        cmul2(z5, z4, z1, z6, z4, z2);
        PUT4(0, CB, 1, q1, 8, q256, 2, q2);
        cmul2(z7, z4, z3, q3, CB, z3);
        cmul2(q768, q512, Z256, q4_, CB, z4);
        cmul2(q5, CB, z5, q1024, q768, Z256);
        cmul2(q6, CB, z6, q1280, q1024, Z256);
        PUT4(12, q512, 3, q3, 14, q768, 4, q4_);
        cmul2(q7, CB, z7, q1536, q1280, Z256);
        cmul2(r0, q256, z2, q1792, q1536, Z256);
        PUT4(5, q5, 16, q1024, 6, q6, 17, q1280);
        cmul2(r1, q256, z4, q2048, q1792, Z256);
        cmul2(r2, q256, z6, r3, q512, z4);
        PUT4(7, q7, 18, q1536, 9, r0, 19, q1792);
        cf pa{1.f, 0.f}, pb = w8, pc_, pd, w32;
        cmul2(r4, q768, z4, w32, w16, w16);
        cmul2(pc_, pa, w16, pd, pb, w16);
        PUT4(10, r1, 20, q2048, 11, r2, 13, r3);
        TS(4);
        sfor<0, 8>([&](auto J) {
            constexpr int jj = J.v;
            cf na, nb, nc, nd;
            if constexpr (jj < 7) { cmul2(na, pa, w32, nb, pb, w32); cmul2(nc, pc_, w32, nd, pd, w32); }
            PUT4(21 + 4 * jj, pa, 22 + 4 * jj, pb, 23 + 4 * jj, pc_, 24 + 4 * jj, pd);
            if constexpr (jj == 0) asm volatile("ds_write_addtid_b32 %0 offset:%1" ::"v"(pk2v(r4)), "i"(15 * KG_RS) : "memory");
            if constexpr (jj < 7) { pa = na; pb = nb; pc_ = nc; pd = nd; }
            if constexpr (jj == PUBLISH_AT) {
                SCHED();
                WAITV(0);
                BAR();
                if (tid == 0) { st_flag(ws, WS_FX + tb * 128, tag); st_flag(ws, WS_FXP + tb * 8, tag); }
                SCHED();
            }
        });
#undef PUT2
#undef PUT4
        if (tid < 2) *(LAS unsigned short*)(lds + L_KC + tid * 1312 + 649 * 2) = 0;
        LBAR();
    }
    TS(5);
    bf16x8 xf[32];
    const unsigned xvo = (lane & 31) * 32 + (lane >> 5) * 16, xsb = WS_XN + (mt * 4 + w) * 32 * 1024;
    unsigned xso;
#define LOAD_X(U) do { if constexpr (((U) & 3) == 0) aload_a_first<((U) >> 2) * 4096, 0>(xf[U], xso, rs, xvo, xsb); \
                       else aload_a_next<((U) & 3) * 1024>(xf[U], xso, rs, xvo); } while (0)
    {
        LAS char* qa = lds + KG_BASE + (w >> 1) * KG_CH + l15 * KG_RS + g16 * 16;
        LAS char* pa_ = qa + (21 + 16 * (w & 1)) * KG_RS;
        bf16x8 fa[8], fb[8], fc[8];
        u64 fxw;
        sfor<0, 8>([&](auto S) {
            constexpr int s_ = S.v;
            fb[s_] = *(LAS bf16x8*)(pa_ + s_ * 64); fa[s_] = *(LAS bf16x8*)(qa + s_ * 64); fc[s_] = *(LAS bf16x8*)(qa + 16 * KG_RS + s_ * 64);
            if constexpr (s_ < 7) STAGE_W1(1, s_);
            else aload8sc1(fxw, ws + WS_FX + (16 * mt + 4 * w) * 128, (lane & 3) * 128);
        });
        SCHED();
        TS(6);
        f32x4 k0, k1;
        sfor<0, 8>([&](auto S) {
            mfma16_vv<S.v == 0>(k0, fa[S.v], fb[S.v]);
            mfma16_vv<S.v == 0>(k1, fc[S.v], fb[S.v]);
        });
        SCHED();
        asm volatile("s_nop 7" : "+v"(k0), "+v"(k1));
#pragma unroll
        for (int r = 0; r < 4; r += 2) {
            const unsigned a0 = pk2(k0[r], k0[r + 1]), a1 = pk2(k1[r], k1[r + 1]);
            *(LAS unsigned short*)(size_t)ksc[r] = (unsigned short)a0;
            *(LAS unsigned short*)(size_t)ksc[r + 1] = (unsigned short)(a0 >> 16);
            *(LAS unsigned short*)(size_t)ksc[4 + r] = (unsigned short)a1;
            *(LAS unsigned short*)(size_t)ksc[5 + r] = (unsigned short)(a1 >> 16);
        }
        SCHED();
        asm volatile("s_waitcnt vmcnt(0)" : "+v"(fxw)::"memory");
        if (!__all(fxw == tag)) wait_flags<1>(ws, WS_FX + (16 * mt + 4 * w) * 128, 4, tag, lane);
        TS(7);
        sfor<0, 8>([&](auto U) { LOAD_X(U.v); });
        SCHED();
    }
    SCHED();
    LBAR();
    TS(8);
    {
        unsigned val[2][9];
#pragma unroll
        for (int ch = 0; ch < 2; ++ch)
#pragma unroll
            for (int e = 0; e < 9; ++e) val[ch][e] = *(LAS unsigned short*)(size_t)(kga[e] + ch * 1312);
        SCHED();
        sfor<0, 7>([&](auto I) { STAGE_W1(2, I.v); });
        if constexpr (WARM_AT == 0) {
            const int r = 35 * (w >> 1) + (lane < 35 ? lane : 34), row = (r * 147) >> 10;
            warm4(pf, (w & 1) ? wu : wg, (192 + 10 * j + row) * (F_ * 2) + (xcd & 3) * 896 + (r - 7 * row) * 128);
        }
#pragma unroll
        for (int ch = 0; ch < 2; ++ch) {
            u32x4 E, O;
#pragma unroll
            for (int m = 0; m < 4; ++m) { E[m] = val[ch][2 * m] | (val[ch][2 * m + 1] << 16); O[m] = val[ch][2 * m + 1] | (val[ch][2 * m + 2] << 16); }
            *(LAS u32x4*)(lds + L_KIMG + ch * CV_KCH + tid * 16) = E;
            *(LAS u32x4*)(lds + L_KIMG + ch * CV_KCH + 4160 + tid * 16) = O;
        }
        if (tid < 16) *(LAS u32x4*)(lds + L_KIMG + (tid >> 3) * CV_KCH + ((tid >> 2) & 1) * 4160 + 4096 + (tid & 3) * 16) = u32x4{0, 0, 0, 0};
    }
    SCHED();
    TS(9);

    f32x16 acc[7];
    u32x4 uu0, uu1;
    bf16x8 wf[28][2];
    const unsigned wtvo0 = lane * 16 + (w & 1) * 1024, wtvo1 = wtvo0 ^ 1024, wtsb = WS_WT + (28 * w * 16 + 2 * dt2) * 1024;
    unsigned wtso;
#define LOAD_WT(I) do { if constexpr (((I) & 1) == 0) aload_a_first<((I) >> 1) * 16384, 0>(wf[(I) >> 1][0], wtso, rs, wtvo0, wtsb); \
                        else aload_a_next<0>(wf[(I) >> 1][1], wtso, rs, wtvo1); } while (0)
    {
        const unsigned ab0 = lbase + (g16 & 1) * G1_UP + (8 * (g16 >> 1) + q4) * 224 + phi * 8, ab1 = ab0 + 2 * G1_SLOT, ab2 = ab0 + 4 * G1_SLOT;
        auto rdw = [&](auto STP, auto P) {
            constexpr int st = STP.v, p = P.v, sl = g1slot(st >> 2), off = (sl & 1) * G1_SLOT + (st & 3) * 16 * 224 + p * 32;
            const unsigned base = sl < 2 ? ab0 : sl < 4 ? ab1 : ab2;
            return cat8(atr<off>(base), atr<off + 4 * 224>(base));
        };
        bf16x8 cur[7], nxt[7];
        u32x4 fx[2];
        waitv<G1S.younger(1, 1, 0)>();
        LBAR();
        sfor<0, 7>([&](auto P) { cur[P.v] = rdw(IC<0>{}, P); });
        SCHED();
        TS(10);
        sfor<0, 32>([&](auto ST) {
            constexpr int st = ST.v, s = st >> 2, kk = st & 3;
            if constexpr (kk == 0 && s > 0) {
                G1_STAMP(2 * s - 2);
                if constexpr (s <= 6) waitv<G1S.younger(1, s + 1, st)>();
                BAR();
                G1_STAMP(2 * s - 1);
                SCHED();
            }
            if constexpr (st == 6) {
                asm volatile("s_waitcnt vmcnt(%2)" : "+v"(fx[0]), "+v"(fx[1]) : "i"(G1S.younger(3, 1, st)) : "memory");
                const u64 f0 = ((u64)fx[0][1] << 32) | fx[0][0], f1 = ((u64)fx[0][3] << 32) | fx[0][2], f2 = ((u64)fx[1][1] << 32) | fx[1][0], f3 = ((u64)fx[1][3] << 32) | fx[1][2];
                if (!__all(f0 == tag && f1 == tag && f2 == tag && f3 == tag)) wait_flags<4>(ws, WS_FX, 64, tag, lane);
                SCHED();
            }
            waitv<G1S.younger(0, st, st)>();
            sfor<0, 7>([&](auto P) {
                constexpr int p = P.v;
                if constexpr (p == 0) waitl<8>(); else if constexpr (p == 3) waitl<(st < 31 ? 10 : 4)>(); else if constexpr (p == 5) waitl<(st < 31 ? 10 : 0)>();
                mfma32_va<st == 0>(acc[p], cur[p], xf[st]);
                if constexpr (st < 31) nxt[p] = rdw(IC<st + 1>{}, P);
                sfor<0, G1S.n>([&](auto K) {
                    constexpr G1Op o = G1S.op[K.v];
                    if constexpr (o.step == st && o.gap == p) {
                        if constexpr (o.kind == 0) LOAD_X(o.a);
                        else if constexpr (o.kind == 1) STAGE_W1(o.a, o.b);
                        else if constexpr (o.kind == 2) LOAD_WT(o.a);
                        else if constexpr (o.kind == 3) aload_v<o.a * 16>(fx[o.a], rs, WS_FXP + lane * 32, 0);
                        else aload_v<0>(o.a == 0 ? uu0 : uu1, rs, WS_XT + ((tid >> 1) * 512 + c0 + (tid & 1)) * 16, o.a * 128 * 512 * 16);
                    }
                });
                SCHED();
            });
#pragma unroll
            for (int p = 0; p < 7; ++p) cur[p] = nxt[p];
        });
    }
    MFMA_DRAIN();
    TS(11);
    asm volatile("s_waitcnt vmcnt(%3)" : "+v"(uu0), "+v"(uu1), "+v"(pf) : "i"(G1S.younger(4, 1, 32)) : "memory");
    if (tid < 64) *(LAS u32x4*)(lds + L_U + (tid >> 5) * CV_UCH + (tid & 31) * 16) = u32x4{0, 0, 0, 0};
    *(LAS u32x4*)(lds + L_U + (tid & 1) * CV_UCH + 512 + (tid >> 1) * 16) = uu0;
    *(LAS u32x4*)(lds + L_U + (tid & 1) * CV_UCH + 512 + 2048 + (tid >> 1) * 16) = uu1;
    SCHED();
    TS(12);
    const int chl = w >> 1, rh = w & 1;
    LAS char* ub = lds + L_U + chl * CV_UCH + 2 * (256 + 16 * (l15 - (g16 >> 1)) + 8 * (g16 & 1)) - 32 * (8 * rh + 6);
    LAS char* kbm[8];
    {
        const int i0 = 2047 - 16 * (g16 >> 1) - l15 + 8 * (g16 & 1);
        LAS char* kb = lds + L_KIMG + chl * CV_KCH + ((l15 & 1) ? 0 : 4160) + 4 * (i0 >> 1) - 32 * (8 * rh + 6);
#pragma unroll
        for (int m = 0; m < 8; ++m) { kbm[m] = kb - 512 * m; asm volatile("" : "+v"(kbm[m])); }
    }
    auto ldG = [&](int m, int rr) {
        const LAS unsigned* p = (const LAS unsigned*)(kbm[m] + 64 * (3 - rr));
        u32x4 t; t[0] = p[0]; t[1] = p[1]; t[2] = p[2]; t[3] = p[3];
        return __builtin_bit_cast(bf16x8, t);
    };
    auto ldF = [&](int m, int rr) { return *(const LAS bf16x8*)(ub + 64 * (3 - rr) + 512 * m); };
    bf16x8 G[2][8], Fr[2][8];
    {
        const unsigned hvo = WS_H + (32 * (w & 1) + (lane & 31)) * 32 + (lane >> 5) * 16, hso = ((mt * 2 + (w >> 1)) * 112 + chunk * 7) * 2048;
        sfor<0, 7>([&](auto P) {
            constexpr int p = P.v;
            u32x4 o;
            f32x2 e[4], gu[4];
#pragma unroll
            for (int r = 0; r < 4; ++r) {
                const f32x2 g = {acc[p][2 * r], acc[p][2 * r + 1]}, u = {acc[p][8 + 2 * r], acc[p][9 + 2 * r]};
                const f32x2 t = g * -1.44269504088896341f;
                e[r] = f32x2{__builtin_amdgcn_exp2f(t[0]), __builtin_amdgcn_exp2f(t[1])};
                gu[r] = g * u;
            }
            SCHED();
            sfor<G1_WT + (2 * p * WT_EPI) / 14, G1_WT + ((2 * p + 1) * WT_EPI) / 14>([&](auto I) { LOAD_WT(I.v); });
#pragma unroll
            for (int r = 0; r < 4; ++r) {
                const f32x2 d = e[r] + 1.0f;
                e[r] = f32x2{__builtin_amdgcn_rcpf(d[0]), __builtin_amdgcn_rcpf(d[1])};
            }
#pragma unroll
            for (int r = 0; r < 4; ++r) o[r] = pk2v(gu[r] * e[r]);
            SCHED();
            sfor<G1_WT + ((2 * p + 1) * WT_EPI) / 14, G1_WT + ((2 * p + 2) * WT_EPI) / 14>([&](auto I) { LOAD_WT(I.v); });
            st128(rs, o, hvo, hso + p * 2048);
            SCHED();
            if constexpr (p == 2) { LBAR(); SCHED(); }
            if constexpr (p >= 3) { G[0][2 * p - 6] = ldG(2 * p - 6, 0); G[0][2 * p - 5] = ldG(2 * p - 5, 0); Fr[0][2 * p - 6] = ldF(2 * p - 6, 0); Fr[0][2 * p - 5] = ldF(2 * p - 5, 0); SCHED(); }
        });
    }
    TS(13);

    constexpr int PD = 12;
    bf16x8 hf[PD][2];
    const unsigned hvoff0 = WS_H + (lane & 31) * 32 + (lane >> 5) * 16 + (w >> 1) * 1024, hvoff1 = hvoff0 ^ 1024;
    const unsigned hsoff = ((mt2 * 2 + th2) * 112 + 28 * w) * 2048;
    auto ldh = [&](int s, int t) { return asbf(ld128(rs, (t ? hvoff1 : hvoff0) + (s & 1) * 2048, hsoff + (s >> 1) * 4096)); };
    f32x4 cacc[8];
    {
        u64 fh;
        sfor<0, 4>([&](auto RR) {
            constexpr int rr = RR.v, c = rr & 1, n = c ^ 1;
            CV_STAMP(rr);
            if constexpr (rr == 1) {
                waitv<((56 - G1_WT - WT_EPI + 2) / 3 < 9 ? (56 - G1_WT - WT_EPI + 2) / 3 : 9)>();
                BAR();
                if (tid == 0) st_flag(ws, WS_FH + (mt * 16 + chunk) * 128, tag);
                SCHED();
            }
            if constexpr (rr == 3) {
                asm volatile("s_waitcnt vmcnt(0)" : "+v"(fh)::"memory");
                if (!__all(fh == tag)) wait_flags<1>(ws, WS_FH + (mt2 * 16 + 4 * w) * 128, 4, tag, lane);
                SCHED();
            }
            sfor<0, 36>([&](auto C) {
                constexpr int cnt = C.v;
                constexpr int m = cnt < 8 ? 0 : cnt < 15 ? 1 : cnt < 21 ? 2 : cnt < 26 ? 3 : cnt < 30 ? 4 : cnt < 33 ? 5 : cnt < 35 ? 6 : 7;
                constexpr int first = m * 8 - (m * (m - 1)) / 2, i = m + (cnt - first);
                mfma16_vv<rr == 0 && m == 0>(cacc[i], G[c][m], Fr[c][i - m]);
                if constexpr (rr < 3) {
                    if constexpr (cnt < 8) Fr[n][cnt] = ldF(cnt, rr + 1);
                    else if constexpr (cnt < 24 && !(cnt & 1)) G[n][(cnt - 8) >> 1] = ldG((cnt - 8) >> 1, rr + 1);
                }
                if constexpr (rr < 3 && cnt % 4 == 2) {
                    constexpr int nrest = 56 - G1_WT - WT_EPI, per = (nrest + 2) / 3, k = cnt / 4, i = G1_WT + WT_EPI + rr * per + k;
                    if constexpr (k < per && i < 56) LOAD_WT(i);
                }
                if constexpr (rr == 2 && cnt == 8) aload8sc1(fh, ws + WS_FH + (mt2 * 16 + 4 * w) * 128, (lane & 3) * 128);
                if constexpr (rr == 3 && cnt % 3 == 1 && cnt / 3 < 10) hf[cnt / 6][(cnt / 3) & 1] = ldh(cnt / 6, (cnt / 3) & 1);
                SCHED();
            });
        });
    }
    MFMA_DRAIN();
    TS(14);
    {
        LAS char* red = lds + L_RED;
#pragma unroll
        for (int i = 0; i < 4; ++i) {
            f32x4 lo = cacc[i], hi = cacc[4 + i];
            asm volatile("" : "+v"(lo), "+v"(hi));
            *(LAS f32x4*)(red + (w * 4 + i) * 1024 + lane * 16) = rh ? lo : hi;
            cacc[i] = rh ? hi : lo;
            hf[5 + i][0] = ldh(5 + i, 0); hf[5 + i][1] = ldh(5 + i, 1);
            SCHED();
        }
        LBAR();
        const float dsk = bf2f(__builtin_amdgcn_readfirstlane((int)p_dsk));
#pragma unroll
        for (int i = 0; i < 4; ++i) {
            const f32x4 y = cacc[i] + *(LAS f32x4*)(red + ((w ^ 1) * 4 + i) * 1024 + lane * 16);
            const int t = 256 * (4 * rh + i) + 16 * l15 + 4 * g16;
            const u32x2 uu = *(LAS u32x2*)(lds + L_U + chl * CV_UCH + 2 * (256 + t));
            cacc[i] = f32x4{y[0] + bflo(uu[0]) * dsk, y[1] + bfhi(uu[0]) * dsk, y[2] + bflo(uu[1]) * dsk, y[3] + bfhi(uu[1]) * dsk};
            if (i < 3) { hf[9 + i][0] = ldh(9 + i, 0); hf[9 + i][1] = ldh(9 + i, 1); }
            SCHED();
        }
#pragma unroll
        for (int i = 0; i < 4; ++i) {
            const int t = 256 * (4 * rh + i) + 16 * l15 + 4 * g16;
            st64(rs, u32x2{pk2(cacc[i][0], cacc[i][1]), pk2(cacc[i][2], cacc[i][3])}, WS_ST + ((c0 + chl) * L_ + t) * 2, 0);
        }
    }
    SCHED();
    TS(15);

    f32x16 a2[2][2];
    u32x4 sv[2], xv[2];
    u64 fs;
    const int d0 = 64 * dt2, t0 = mt2 * 128 + th2 * 64;
    const int tt2 = w >> 1, dh2 = w & 1;
    sfor<0, 24>([&](auto S) {
        constexpr int s = S.v;
        const bf16x8 h0 = hf[s % PD][0], h1 = hf[s % PD][1];
        mfma32_av<s == 0>(a2[0][0], wf[s][0], h0);
        mfma32_av<s == 0>(a2[1][0], wf[s][1], h0);
        SCHED();
        mfma32_av<s == 0>(a2[0][1], wf[s][0], h1);
        mfma32_av<s == 0>(a2[1][1], wf[s][1], h1);
        sfor<PD, 28>([&](auto T) {
            constexpr int t = T.v;
            if constexpr (((t - PD) * 21) / 16 == s) { hf[t % PD][0] = ldh(t, 0); hf[t % PD][1] = ldh(t, 1); }
        });
        if constexpr (s == 10) {
            G2_STAMP(0);
            waitv<G2_YOUNGER>();
            BAR();
            G2_STAMP(1);
            if (tid == 0) st_flag(ws, WS_FS + tb * 8, tag);
        }
        if constexpr (s == 17) aload8sc1(fs, ws + WS_FS + (d0 >> 1) * 8, (lane & 31) * 8);
        if constexpr (s == 21) {
            G2_STAMP(2);
            asm volatile("s_waitcnt vmcnt(%1)" : "+v"(fs) : "i"(G2_AFTER_FS) : "memory");
            G2_STAMP(3);
            if (!__all(fs == tag)) wait_flags<1, 8>(ws, WS_FS + (d0 >> 1) * 8, 32, tag, lane);
#pragma unroll
            for (int i = 0; i < 2; ++i) {
                const int e = tid + 256 * i;
                sv[i] = ld128(rs, WS_ST + ((d0 + (e >> 3)) * L_ + t0) * 2 + (e & 7) * 16, 0);
            }
#pragma unroll
            for (int g = 0; g < 2; ++g) xv[g] = *(const u32x4*)(x + (size_t)(t0 + 32 * tt2 + (lane & 31)) * D_ + d0 + 32 * dh2 + 16 * g + 8 * (lane >> 5));
        }
        SCHED();
    });
    {
        auto put = [&](auto DH, auto TT) {
            const int q = 2 * (TT.v ^ (w >> 1)) + (DH.v ^ (w & 1));
            const f32x16 a = a2[DH.v][TT.v];
            LAS char* p = lds + (q * 4 + w) * 4096 + lane * 16;
#pragma unroll
            for (int g = 0; g < 4; ++g) *(LAS f32x4*)(p + g * 1024) = f32x4{a[4 * g], a[4 * g + 1], a[4 * g + 2], a[4 * g + 3]};
        };
        sfor<24, 28>([&](auto S) { mfma32_av<false>(a2[1][1], wf[S.v][1], hf[S.v % PD][1]); });
        sfor<24, 28>([&](auto S) { mfma32_av<false>(a2[1][0], wf[S.v][1], hf[S.v % PD][0]); });
        SCHED();
        put(IC<1>{}, IC<1>{});
        SCHED();
        sfor<24, 28>([&](auto S) { mfma32_av<false>(a2[0][1], wf[S.v][0], hf[S.v % PD][1]); });
        SCHED();
        put(IC<1>{}, IC<0>{});
        SCHED();
        sfor<24, 28>([&](auto S) { mfma32_av<false>(a2[0][0], wf[S.v][0], hf[S.v % PD][0]); });
        SCHED();
        put(IC<0>{}, IC<1>{});
#pragma unroll
        for (int i = 0; i < 2; ++i) *(LAS u32x4*)(lds + L_ST + (tid + 256 * i) * 16) = sv[i];
        MFMA_DRAIN();
        TS(16);
        LBAR();
    }
    TS(17);
    {
        const f32x16 own = a2[0][0];
        f32x4 f[4];
#pragma unroll
        for (int g = 0; g < 4; ++g) {
            f[g] = f32x4{own[4 * g], own[4 * g + 1], own[4 * g + 2], own[4 * g + 3]};
#pragma unroll
            for (int o = 1; o < 4; ++o) f[g] += *(LAS f32x4*)(lds + ((w * 4 + ((w + o) & 3)) * 4 + g) * 1024 + lane * 16);
        }
        LAS char* sp = lds + L_ST + (32 * dh2 + 8 * (lane >> 5) + q4) * 128 + (32 * tt2 + 16 * (g16 & 1) + 4 * pp) * 2;
#pragma unroll
        for (int g2 = 0; g2 < 2; ++g2) {
            const s16x4 sa = tr_read(sp + g2 * 16 * 128), sb = tr_read(sp + (g2 * 16 + 4) * 128);
            const u32x2 s01 = __builtin_bit_cast(u32x2, sa), s23 = __builtin_bit_cast(u32x2, sb);
            u32x4 o;
            o[0] = pk2v(unpk(xv[g2][0]) + unpk(s01[0]) * silu2(f32x2{f[2 * g2][0], f[2 * g2][1]}));
            o[1] = pk2v(unpk(xv[g2][1]) + unpk(s01[1]) * silu2(f32x2{f[2 * g2][2], f[2 * g2][3]}));
            o[2] = pk2v(unpk(xv[g2][2]) + unpk(s23[0]) * silu2(f32x2{f[2 * g2 + 1][0], f[2 * g2 + 1][1]}));
            o[3] = pk2v(unpk(xv[g2][3]) + unpk(s23[1]) * silu2(f32x2{f[2 * g2 + 1][2], f[2 * g2 + 1][3]}));
            *(u32x4*)(out + (size_t)(t0 + 32 * tt2 + (lane & 31)) * D_ + d0 + 32 * dh2 + 16 * g2 + 8 * (lane >> 5)) = o;
        }
    }
    if (b == 0 && tid == 0) st_flag(ws, WS_EP, tag);
    TS(18);
    TSR(47);
    TS_DUMP();
    asm volatile(".Lfused_pf_end:" ::: "memory");
}

extern "C" void kernel_launch(void* const* d_in, const int* in_sizes, int n_in, void* d_out, int out_size, void* d_ws, size_t ws_size,
                              hipStream_t stream) {
    (void)hipFuncSetAttribute((const void*)fused_s4_ffn, hipFuncAttributeMaxDynamicSharedMemorySize, LDS_TOTAL);
    fused_s4_ffn<<<256, 256, LDS_TOTAL, stream>>>((const bf16_t*)d_in[0], (const bf16_t*)d_in[1], (const bf16_t*)d_in[2], (const bf16_t*)d_in[3],
                                                  (const bf16_t*)d_in[4], (const bf16_t*)d_in[5], (const bf16_t*)d_in[6], (const bf16_t*)d_in[7],
                                                  (const bf16_t*)d_in[8], (const bf16_t*)d_in[10], (const bf16_t*)d_in[11], (const bf16_t*)d_in[12],
                                                  (bf16_t*)d_out, (char*)d_ws);
}
```

```cpp
#include <hip/hip_runtime.h>
#include <stdint.h>

typedef unsigned short bf16_t;
typedef short bf16x8 __attribute__((ext_vector_type(8)));
typedef short s16x4 __attribute__((ext_vector_type(4)));
typedef float f32x2 __attribute__((ext_vector_type(2)));
typedef float f32x4 __attribute__((ext_vector_type(4)));
typedef float f32x16 __attribute__((ext_vector_type(16)));
typedef unsigned u32x4 __attribute__((ext_vector_type(4)));
typedef unsigned u32x2 __attribute__((ext_vector_type(2)));
typedef __bf16 bf16v2 __attribute__((ext_vector_type(2)));
typedef unsigned long long u64;
#define LAS __attribute__((address_space(3)))
#define GAS __attribute__((address_space(1)))
#define DI __device__ __forceinline__

#define XSCHED 0
#define WSCHED 0
constexpr int L_ = 2048, D_ = 512, N_ = 128, F_ = 1792;

constexpr unsigned WS_XN = 0;
constexpr unsigned WS_XT = 2u << 20;
constexpr unsigned WS_ST = 4u << 20;
constexpr unsigned WS_H = 6u << 20;
constexpr unsigned WS_WT = 14u << 20;
constexpr unsigned WS_FX = 16u << 20;
constexpr unsigned WS_FH = WS_FX + 256 * 128;
constexpr unsigned WS_FS = WS_FH + 256 * 128;
constexpr unsigned WS_FXP = WS_FS + 256 * 128;
constexpr unsigned WS_EP = WS_FXP + 256 * 8;
constexpr unsigned WS_USED = 17u << 20;

constexpr int G1_SLOT = 28800, G1_UP = 14336 + 128;
constexpr int g1slot(int s) { return s % 5 == 0 ? 0 : s % 5 == 1 ? 3 : s % 5 == 2 ? 4 : s % 5 == 3 ? 1 : 2; }
constexpr int KG_BASE = G1_SLOT, KG_RS = 528, KG_CH = 53 * KG_RS;
constexpr int L_WTS = 3 * G1_SLOT;
constexpr int L_KC = 144128;
constexpr int L_KIMG = 146816;
constexpr int L_LNS = L_KIMG, LN_RS = 1056;
constexpr int CV_KCH = 8320;
constexpr int L_U = 2 * G1_SLOT, CV_UCH = 4608;
constexpr int L_RED = L_U + 2 * CV_UCH;
constexpr int L_ST = 65536;
constexpr int LDS_TOTAL = 163840;
static_assert(KG_BASE + 2 * KG_CH <= 3 * G1_SLOT && KG_BASE + KG_CH + 256 < 65536, "K-gen scratch");
static_assert(L_KIMG + 2 * CV_KCH <= LDS_TOTAL && L_RED + 16384 <= 3 * G1_SLOT && L_ST + 8192 <= 3 * G1_SLOT, "lds map");

DI float bf2f(unsigned u16) { return __uint_as_float(u16 << 16); }
DI float bflo(unsigned w) { return __uint_as_float(w << 16); }
DI float bfhi(unsigned w) { return __uint_as_float(w & 0xffff0000u); }
DI unsigned pk2v(f32x2 v) { return __builtin_bit_cast(unsigned, __builtin_convertvector(v, bf16v2)); }
DI unsigned pk2(float lo, float hi) { return pk2v(f32x2{lo, hi}); }
DI f32x2 unpk(unsigned w) { return f32x2{bflo(w), bfhi(w)}; }
DI float rbf(float x) { return bflo(pk2(x, x)); }

#define WAITV(n) asm volatile("s_waitcnt vmcnt(" #n ")" ::: "memory")
#define WAITL(n) asm volatile("s_waitcnt lgkmcnt(" #n ")" ::: "memory")
#define BAR() do { asm volatile("" ::: "memory"); __builtin_amdgcn_s_barrier(); asm volatile("" ::: "memory"); } while (0)
#define LBAR() do { WAITL(0); BAR(); } while (0)
#define SCHED() __builtin_amdgcn_sched_barrier(0)

#define TS(i) do {} while (0)
#define TSR(i) do {} while (0)
#define TS_DUMP() do {} while (0)

template <int N> struct IC { static constexpr int v = N; };
template <int B, int E, class F> DI void sfor(F&& f) { if constexpr (B < E) { f(IC<B>{}); sfor<B + 1, E>(f); } }
template <int N> DI void waitv() { asm volatile("s_waitcnt vmcnt(%0)" ::"i"(N) : "memory"); }
template <int N> DI void waitl() { asm volatile("s_waitcnt lgkmcnt(%0)" ::"i"(N) : "memory"); }

template <int CTRL, int RMASK> DI float dpp_add(float v) {
    return v + __builtin_bit_cast(float, __builtin_amdgcn_update_dpp(0, __builtin_bit_cast(int, v), CTRL, RMASK, 0xf, true));
}
DI void wave_sum4(float& a, float& b, float& c, float& d) {
#define STEP(C, M) a = dpp_add<C, M>(a); b = dpp_add<C, M>(b); c = dpp_add<C, M>(c); d = dpp_add<C, M>(d);
    STEP(0x111, 0xf) STEP(0x112, 0xf) STEP(0x114, 0xf) STEP(0x118, 0xf) STEP(0x142, 0xa) STEP(0x143, 0xc)
#undef STEP
    a = __builtin_bit_cast(float, __builtin_amdgcn_readlane(__builtin_bit_cast(int, a), 63));
    b = __builtin_bit_cast(float, __builtin_amdgcn_readlane(__builtin_bit_cast(int, b), 63));
    c = __builtin_bit_cast(float, __builtin_amdgcn_readlane(__builtin_bit_cast(int, c), 63));
    d = __builtin_bit_cast(float, __builtin_amdgcn_readlane(__builtin_bit_cast(int, d), 63));
}
DI s16x4 tr_read(LAS char* p) { return __builtin_amdgcn_ds_read_tr16_b64_v4i16((LAS s16x4*)p); }
template <int OFF> DI s16x4 atr(unsigned addr) { s16x4 r; asm volatile("ds_read_b64_tr_b16 %0, %1 offset:%2" : "=v"(r) : "v"(addr), "i"(OFF)); return r; }
DI bf16x8 cat8(s16x4 a, s16x4 b) { return __builtin_shufflevector(a, b, 0, 1, 2, 3, 4, 5, 6, 7); }

typedef __amdgpu_buffer_rsrc_t rsrc_t;
constexpr int SC1 = 16;
DI u32x4 ld128(rsrc_t r, unsigned voff, unsigned soff) { return __builtin_amdgcn_raw_buffer_load_b128(r, voff, soff, SC1); }
DI void st128(rsrc_t r, u32x4 v, unsigned voff, unsigned soff) {
    asm volatile("buffer_store_dwordx4 %0, %1, %2, %3 offen sc1\n\ts_nop 1" ::"v"(v), "v"(voff), "s"(r), "s"(soff) : "memory");
}
DI void st64(rsrc_t r, u32x2 v, unsigned voff, unsigned soff) { __builtin_amdgcn_raw_buffer_store_b64(v, r, voff, soff, SC1); }
DI bf16x8 asbf(u32x4 v) { return __builtin_bit_cast(bf16x8, v); }

template <int SIMM, int LIMM, int IMM> DI void bdma_first(unsigned& so, rsrc_t r, unsigned voff, unsigned sbase, unsigned lbase) {
    asm volatile("s_add_u32 m0, %1, %2\n\ts_add_u32 %0, %3, %4\n\tbuffer_load_dwordx4 %5, %6, %0 offen offset:%7 lds"
                 : "=&s"(so) : "s"(lbase), "i"(LIMM), "s"(sbase), "i"(SIMM), "v"(voff), "s"(r), "i"(IMM) : "memory", "scc");
}
template <int IMM> DI void bdma_next(unsigned so, rsrc_t r, unsigned voff) {
    asm volatile("buffer_load_dwordx4 %0, %1, %2 offen offset:%3 lds" ::"v"(voff), "s"(r), "s"(so), "i"(IMM) : "memory");
}
template <int SIMM, int IMM> DI void aload_a_first(bf16x8& d, unsigned& so, rsrc_t r, unsigned voff, unsigned sbase) {
    asm volatile("s_add_u32 %1, %4, %5\n\tbuffer_load_dwordx4 %0, %2, %3, %1 offen offset:%6 sc1"
                 : "=&a"(d), "=&s"(so) : "v"(voff), "s"(r), "s"(sbase), "i"(SIMM), "i"(IMM) : "memory", "scc");
}
template <int IMM> DI void aload_a_next(bf16x8& d, unsigned so, rsrc_t r, unsigned voff) {
    asm volatile("buffer_load_dwordx4 %0, %1, %2, %3 offen offset:%4 sc1" : "=&a"(d) : "v"(voff), "s"(r), "s"(so), "i"(IMM) : "memory");
}
template <int IMM> DI void aload_v(u32x4& d, rsrc_t r, unsigned voff, unsigned so) {
    asm volatile("buffer_load_dwordx4 %0, %1, %2, %3 offen offset:%4 sc1" : "=&v"(d) : "v"(voff), "s"(r), "s"(so), "i"(IMM) : "memory");
}
DI void warm4(unsigned& pf, const void* sbase, unsigned voff) { asm volatile("global_load_dword %0, %1, %2" : "+v"(pf) : "v"(voff), "s"(sbase) : "memory"); }
DI void aload16(u32x4& d, const void* sbase, unsigned voff) { asm volatile("global_load_dwordx4 %0, %1, %2" : "=&v"(d) : "v"(voff), "s"(sbase) : "memory"); }
DI void aload2(unsigned& d, const void* sbase, unsigned voff) { asm volatile("global_load_ushort %0, %1, %2" : "=&v"(d) : "v"(voff), "s"(sbase) : "memory"); }
DI void aload8sc1(u64& d, const void* sbase, unsigned voff) { asm volatile("global_load_dwordx2 %0, %1, %2 sc1" : "=&v"(d) : "v"(voff), "s"(sbase) : "memory"); }

template <bool FIRST> DI void mfma32_va(f32x16& c, bf16x8 a, bf16x8 b) {
    if constexpr (FIRST) asm volatile("v_mfma_f32_32x32x16_bf16 %0, %1, %2, 0" : "=&v"(c) : "v"(a), "a"(b));
    else asm volatile("v_mfma_f32_32x32x16_bf16 %0, %1, %2, %0" : "+v"(c) : "v"(a), "a"(b));
}
template <bool FIRST> DI void mfma32_av(f32x16& c, bf16x8 a, bf16x8 b) {
    if constexpr (FIRST) asm volatile("v_mfma_f32_32x32x16_bf16 %0, %1, %2, 0" : "=&v"(c) : "a"(a), "v"(b));
    else asm volatile("v_mfma_f32_32x32x16_bf16 %0, %1, %2, %0" : "+v"(c) : "a"(a), "v"(b));
}
template <bool FIRST> DI void mfma16_vv(f32x4& c, bf16x8 a, bf16x8 b) {
    if constexpr (FIRST) asm volatile("v_mfma_f32_16x16x32_bf16 %0, %1, %2, 0" : "=&v"(c) : "v"(a), "v"(b));
    else asm volatile("v_mfma_f32_16x16x32_bf16 %0, %1, %2, %0" : "+v"(c) : "v"(a), "v"(b));
}
#define MFMA_DRAIN() asm volatile("s_nop 15\n\ts_nop 3" ::: "memory")

#define WT_LOOP 24
#define WT_EPI 14
constexpr int G1_WT = WT_LOOP;
struct G1Op { int step, gap, kind, a, b; };
struct G1Sched {
    G1Op op[160]; int n;
    constexpr G1Sched() : op{}, n(0) {
        for (int i = 0; i < 7; ++i) op[n++] = G1Op{-1, 0, 1, 1, i};
        for (int u = 0; u < 8; ++u) op[n++] = G1Op{-1, 0, 0, u, 0};
        for (int i = 0; i < 7; ++i) op[n++] = G1Op{-1, 0, 1, 2, i};
        op[n++] = G1Op{-1, 0, 6, 0, 0};
        int wt = 0;
        for (int s = 0; s < 8; ++s) {
            G1Op list[24] = {}; int m = 0;
            if (s == 0) for (int i = 0; i < 2; ++i) list[m++] = G1Op{0, 0, 3, i, 0};
            if (s < 3) { for (int i = 0; i < 4; ++i) list[m++] = G1Op{0, 0, 0, 8 + 8 * s + i, 0};
                         for (int i = 0; i < 4; ++i) list[m++] = G1Op{0, 0, 1, 3 + s, i};
                         for (int i = 4; i < 8; ++i) list[m++] = G1Op{0, 0, 0, 8 + 8 * s + i, 0};
                         for (int i = 4; i < 7; ++i) list[m++] = G1Op{0, 0, 1, 3 + s, i}; }
            if (s == 3 || s == 4) for (int i = 0; i < 7; ++i) list[m++] = G1Op{0, 0, 1, 3 + s, i};
            if (s == 5) for (int i = 0; i < 2; ++i) list[m++] = G1Op{0, 0, 4, i, 0};
            const int nw = s < 5 ? 0 : s == 5 ? G1_WT / 4 / 2 * 2 : s == 6 ? (G1_WT - G1_WT / 4 / 2 * 2) / 2 / 2 * 2 : G1_WT - wt;
            for (int i = 0; i < nw; ++i) list[m++] = G1Op{0, 0, 2, wt++, 0};
            const int span = s == 7 ? 21 : 28;
            for (int i = 0; i < m; ++i) { const int g = (i * span) / m; list[i].step = 4 * s + g / 7; list[i].gap = g % 7; op[n++] = list[i]; }
        }
    }
    constexpr int younger(int kind, int a, int st) const {
        int last = -1;
        for (int i = 0; i < n; ++i) if (op[i].kind == kind && op[i].a == a) last = i;
        int c = 0;
        for (int i = last + 1; i < n; ++i) if (op[i].step < st) ++c;
        return c;
    }
};
constexpr G1Sched G1S{};

DI u64 ld_flag(const char* ws, unsigned off) { return __hip_atomic_load((GAS u64*)(ws + off), __ATOMIC_RELAXED, __HIP_MEMORY_SCOPE_AGENT); }
DI void st_flag(char* ws, unsigned off, u64 v) { __hip_atomic_store((GAS u64*)(ws + off), v, __ATOMIC_RELAXED, __HIP_MEMORY_SCOPE_AGENT); }
template <int NPL, int STRIDE = 128>
DI void wait_flags(const char* ws, unsigned base, int nl, u64 tag, int lane) {
    const unsigned off = base + (lane < nl ? lane : 0) * STRIDE;
#pragma unroll 1
    for (int spin = 0; spin < 40000; ++spin) {
        bool ok = true;
#pragma unroll
        for (int i = 0; i < NPL; ++i) ok &= ld_flag(ws, off + i * 64 * STRIDE) == tag;
        if (__all(ok)) break;
        __builtin_amdgcn_s_sleep(1);
    }
    asm volatile("" ::: "memory");
}

typedef f32x2 cf;
DI cf cmul(cf a, cf b) { return cf{a[0] * b[0] - a[1] * b[1], a[0] * b[1] + a[1] * b[0]}; }
DI void cmul2(cf& z0, cf a0, cf b0, cf& z1, cf a1, cf b1) {
    cf t0, t1;
    asm("v_pk_mul_f32 %2, %4, %5 op_sel_hi:[0,1]\n\tv_pk_mul_f32 %3, %6, %7 op_sel_hi:[0,1]\n\t"
        "v_pk_fma_f32 %0, %4, %5, %2 op_sel:[1,1,0] op_sel_hi:[1,0,1] neg_lo:[0,1,0]\n\t"
        "v_pk_fma_f32 %1, %6, %7, %3 op_sel:[1,1,0] op_sel_hi:[1,0,1] neg_lo:[0,1,0]"
        : "=&v"(z0), "=&v"(z1), "=&v"(t0), "=&v"(t1) : "v"(a0), "v"(b0), "v"(a1), "v"(b1));
}
DI cf zdir(float a2, float th, float l) {
    const float m = __builtin_amdgcn_exp2f(a2 * l);
    const float r = __builtin_amdgcn_fractf(th * l);
    return cf{m * __builtin_amdgcn_cosf(r), m * __builtin_amdgcn_sinf(r)};
}
DI f32x2 silu2(f32x2 g) {
    const f32x2 t = g * -1.44269504088896341f;
    const f32x2 d = f32x2{__builtin_amdgcn_exp2f(t[0]), __builtin_amdgcn_exp2f(t[1])} + 1.0f;
    return g * f32x2{__builtin_amdgcn_rcpf(d[0]), __builtin_amdgcn_rcpf(d[1])};
}

#define PUBLISH_AT 3
constexpr int g2_younger() { int c = 0; for (int t = 12; t < 28; ++t) if (((t - 12) * 21) / 16 <= 10) c += 2; return c; }
#define G2_YOUNGER g2_younger()
constexpr int g2_after_fs() { int c = 0; for (int t = 12; t < 28; ++t) { const int st = ((t - 12) * 21) / 16; if (st > 17 && st <= 21) c += 2; } return c; }
#define G2_AFTER_FS g2_after_fs()
#define G1_STAMP(i) do {} while (0)
#define CV_STAMP(i) do {} while (0)
#define G2_STAMP(i) do {} while (0)

extern __shared__ __attribute__((aligned(16))) char dyn_smem[];

__global__ __launch_bounds__(256, 1) void fused_s4_ffn(const bf16_t* __restrict__ x, const bf16_t* __restrict__ A_re, const bf16_t* __restrict__ A_im,
                                                       const bf16_t* __restrict__ B_re, const bf16_t* __restrict__ B_im, const bf16_t* __restrict__ C_re,
                                                       const bf16_t* __restrict__ C_im, const bf16_t* __restrict__ log_dt, const bf16_t* __restrict__ Dskip,
                                                       const bf16_t* __restrict__ wg, const bf16_t* __restrict__ wu, const bf16_t* __restrict__ wd,
                                                       bf16_t* __restrict__ out, char* ws) {
    LAS char* lds = (LAS char*)dyn_smem;
    const unsigned lbase = (unsigned)(size_t)lds;
    const int tid = threadIdx.x, w = __builtin_amdgcn_readfirstlane(tid >> 6), lane = tid & 63;
    const int b = blockIdx.x, xcd = b & 7, j = b >> 3;
    const int tb = xcd * 32 + j;
    const int mt = 8 * (xcd >> 2) + (j >> 2), chunk = 4 * (xcd & 3) + (j & 3);
    const int mt2 = 2 * xcd + (j >> 4), th2 = (j >> 3) & 1, dt2 = j & 7;
    const int c0 = 2 * tb;
    const unsigned long long probe_t0 = __builtin_amdgcn_s_memrealtime();
    unsigned long long probe_a = 0;
    if (threadIdx.x == 0) __hip_atomic_store((GAS u64*)((char*)ws + (20u << 20) + blockIdx.x * 8), probe_t0, __ATOMIC_RELAXED, __HIP_MEMORY_SCOPE_AGENT);
    TSR(46); TS(0);

    u32x4 raw[2];
#pragma unroll
    for (int r = 0; r < 2; ++r) aload16(raw[r], x + (size_t)(8 * tb + 2 * w + r) * D_, lane * 16);
    unsigned pf = 0;
    {
        u64 pc; unsigned len;
        asm volatile("s_getpc_b64 %0\n.Lfused_pf_here:\n\ts_mov_b32 %1, .Lfused_pf_end-.Lfused_pf_here" : "=s"(pc), "=s"(len));
        const unsigned o = (unsigned)(12 * j + (lane < 12 ? lane : 11)) * 128u;
        warm4(pf, (const void*)pc, o < len ? o : len);
    }
    u64 ep;
    aload8sc1(ep, ws + WS_EP, 0);
    const int kch = c0 + (w >> 1);
    const unsigned kpo = (64 * (w & 1) + lane) * 2;
    unsigned p_are, p_aim, p_br, p_bi, p_cr, p_ci, p_ldt, p_dsk;
    aload2(p_are, A_re + kch * N_, kpo); aload2(p_aim, A_im + kch * N_, kpo); aload2(p_br, B_re + kch * N_, kpo);
    aload2(p_bi, B_im + kch * N_, kpo); aload2(p_cr, C_re + kch * N_, kpo); aload2(p_ci, C_im + kch * N_, kpo);
    aload2(p_ldt, log_dt + kch, 0); aload2(p_dsk, Dskip + kch, 0);
    SCHED();
    const rsrc_t rs = __builtin_amdgcn_make_buffer_rsrc(ws, 0, WS_USED, 0x00020000);
    const rsrc_t rw1 = __builtin_amdgcn_make_buffer_rsrc((void*)((w & 1) ? wu : wg), 0, D_ * F_ * 2, 0x00020000);
    const rsrc_t rwd = __builtin_amdgcn_make_buffer_rsrc((void*)wd, 0, D_ * F_ * 2, 0x00020000);
    const int wtU0 = 7 * (b >> 4) + w, wtU1 = w < 3 ? wtU0 + 4 : wtU0;
    {
        unsigned so;
        const unsigned vo = (lane >> 2) * 1024 + (lane & 3) * 16;
        bdma_first<0, L_WTS, 0>(so, rwd, vo, wtU0 * 16384 + (b & 15) * 64, lbase + w * 2048);
        bdma_first<0, L_WTS + 1024, 0>(so, rwd, vo, wtU1 * 16384 + (b & 15) * 64, lbase + w * 2048);
    }
    unsigned voff[7];
    {
        const int k = (lane * 147) >> 11, c = lane - 14 * k;
        unsigned v = k * (F_ * 2) + c * 16;
        int cc = c;
#pragma unroll
        for (int i = 0; i < 7; ++i) {
            voff[i] = v - (i & 3) * 1024;
            cc += 8; v += 4 * (F_ * 2) + 128;
            if (cc >= 14) { cc -= 14; v += F_ * 2 - 224; }
        }
    }
    const unsigned w1s = (32 * (w >> 1)) * (F_ * 2) + chunk * 224;
    const unsigned w1l = lbase + (w & 1) * G1_UP + (w >> 1) * 7168;
    unsigned w1so;
#define STAGE_W1(S, I) do { if constexpr (((I) & 3) == 0) bdma_first<64 * (S) * (F_ * 2), g1slot(S) * G1_SLOT + (I) * 1024, 0>(w1so, rw1, voff[I], w1s, w1l); \
                            else bdma_next<((I) & 3) * 1024>(w1so, rw1, voff[I]); } while (0)
    SCHED();
    const int g16 = lane >> 4, q4 = (lane & 15) >> 2, pp = lane & 3, l15 = lane & 15;
    const int phi = ((pp & 1) << 1) | (pp >> 1);
    unsigned ksc[8];
    {
        const int col8 = 8 * (16 * (w & 1) + l15);
        const unsigned kcb = lbase + L_KC + (w >> 1) * 1312, kcf = kcb - 2 * 17024;
        const int A = (g16 == 0 ? 0 : g16 == 1 ? 4 : g16 == 2 ? 256 : 512) + col8, B0 = g16 == 2 ? 2 : g16 == 3 ? 4 : 1, B1 = g16 == 2 ? 4 : g16 == 3 ? 256 : 2;
#pragma unroll
        for (int r = 0; r < 4; ++r) {
            const int v = A + (r & 1) * B0 + (r >> 1) * B1;
            ksc[r] = v < 128 ? kcb + 2 * v : kcf + 2 * (__float_as_uint((float)v) >> 16);
            asm volatile("" : "+v"(ksc[r]));
        }
#pragma unroll
        for (int r = 0; r < 4; ++r) {
            const unsigned a = kcf + 2 * (__float_as_uint((float)(1024 + 256 * r + col8)) >> 16);
            ksc[4 + r] = g16 == 0 ? a : (r == 0 && g16 == 1 && col8 == 0) ? kcb + 2 * 640 : kcb + 2 * 648;
            asm volatile("" : "+v"(ksc[4 + r]));
        }
    }
    unsigned kga[9];
    {
        const float f = (float)(2047 - 8 * tid);
        const unsigned kcf = lbase + L_KC - 2 * 17024;
#pragma unroll
        for (int e = 0; e < 9; e += 2) {
            const unsigned bits = pk2(f - e, f - (e + 1));
            kga[e] = kcf + 2 * (bits & 0xffffu);
            if (e < 8) kga[e + 1] = kcf + 2 * (bits >> 16);
        }
        if (w == 3) {
#pragma unroll
            for (int e = 0; e < 9; ++e) {
                const int l = 2047 - 8 * tid - e;
                if (l < 256) kga[e] = lbase + L_KC + 2 * (l < 0 ? 649 : l);
            }
        }
#pragma unroll
        for (int e = 0; e < 9; ++e) asm volatile("" : "+v"(kga[e]));
    }
    SCHED();
    {
        const int r = 21 * (w >> 1) + (lane < 21 ? lane : 20), row = (r * 147) >> 10;
        warm4(pf, (w & 1) ? wu : wg, (6 * j + row) * (F_ * 2) + (xcd & 3) * 896 + (r - 7 * row) * 128);
    }
    SCHED();
    asm volatile("s_waitcnt vmcnt(3)" : "+v"(ep), "+v"(raw[0]), "+v"(raw[1]), "+v"(p_are), "+v"(p_aim), "+v"(p_br), "+v"(p_bi), "+v"(p_cr), "+v"(p_ci),
                 "+v"(p_ldt), "+v"(p_dsk)::"memory");
    SCHED();
    TS(1);
    {
        f32x2 v[2][4];
        float s1[2], s2[2], x0[2];
        sfor<0, 2>([&](auto R) {
            constexpr int r = R.v;
            x0[r] = bflo((unsigned)__builtin_amdgcn_readfirstlane((int)raw[r][0]));
            f32x2 a = {0.f, 0.f}, q = {0.f, 0.f};
#pragma unroll
            for (int jj = 0; jj < 4; ++jj) { v[r][jj] = unpk(raw[r][jj]); const f32x2 d = v[r][jj] - x0[r]; a += d; q += d * d; }
            s1[r] = a[0] + a[1]; s2[r] = q[0] + q[1];
            STAGE_W1(0, r);
            SCHED();
        });
        wave_sum4(s1[0], s2[0], s1[1], s2[1]);
        STAGE_W1(0, 2);
        SCHED();
        sfor<0, 2>([&](auto R) {
            constexpr int r = R.v;
            const float md = s1[r] * (1.0f / 512.0f);
            const float var = fmaxf(s2[r] * (1.0f / 512.0f) - md * md, 0.f);
            const float mb = rbf(x0[r] + md);
            const float sd = rbf(__builtin_amdgcn_sqrtf(rbf(rbf(var) + 1e-5f)));
            const float rsd = __builtin_amdgcn_rcpf(sd);
            STAGE_W1(0, 3 + 2 * r);
            SCHED();
            u32x4 o;
#pragma unroll
            for (int jj = 0; jj < 4; ++jj) o[jj] = pk2v(unpk(pk2v(v[r][jj] - mb)) * rsd);
            *(LAS u32x4*)(lds + L_LNS + (2 * w + r) * LN_RS + lane * 16) = o;
            STAGE_W1(0, 4 + 2 * r);
            SCHED();
        });
    }
    WAITV(8);
    LBAR();
    TS(2);
    {
        LAS char* tp = lds + L_WTS + w * 2048 + (8 * (g16 >> 1) + q4) * 64 + (g16 & 1) * 32 + phi * 8;
        const bf16x8 f0 = cat8(tr_read(tp), tr_read(tp + 256)), f1 = cat8(tr_read(tp + 1024), tr_read(tp + 1024 + 256));
        st128(rs, __builtin_bit_cast(u32x4, f0), WS_WT + lane * 16, (wtU0 * 16 + (b & 15)) * 1024);
        if (w < 3) st128(rs, __builtin_bit_cast(u32x4, f1), WS_WT + lane * 16, (wtU1 * 16 + (b & 15)) * 1024);
    }
    SCHED();

    {
        const int half = tid & 1, tok = (tid >> 1) & 7, U = tid >> 4;
        const u32x4 va = *(LAS u32x4*)(lds + L_LNS + tok * LN_RS + U * 32 + half * 16);
        const u32x4 vb = *(LAS u32x4*)(lds + L_LNS + tok * LN_RS + U * 32 + half * 16 + 512);
        LAS char* tp = lds + L_LNS + q4 * LN_RS + (128 * w + 16 * g16 + 4 * pp) * 2;
        const s16x4 a0 = tr_read(tp), b0 = tr_read(tp + 4 * LN_RS), a1 = tr_read(tp + 128), b1 = tr_read(tp + 4 * LN_RS + 128);
        const unsigned xo = WS_XN + ((tb >> 2) * 32 + U) * 1024 + ((tb & 3) * 8 + tok) * 32 + half * 16;
        st128(rs, va, xo, 0);
        st128(rs, vb, xo, 16 * 1024);
        const unsigned to = WS_XT + (tb * 512 + 128 * w + 16 * g16 + l15) * 16;
        st128(rs, __builtin_bit_cast(u32x4, cat8(a0, b0)), to, 0);
        st128(rs, __builtin_bit_cast(u32x4, cat8(a1, b1)), to, 1024);
    }
    SCHED();
    const u64 tag = ((u64)(unsigned)__builtin_amdgcn_readfirstlane((int)(unsigned)ep) |
                     ((u64)(unsigned)__builtin_amdgcn_readfirstlane((int)(unsigned)(ep >> 32)) << 32)) + 1;
    TS(3);

    {
        const float dt = rbf(__builtin_amdgcn_exp2f(bf2f(p_ldt) * 1.44269504088896341f));
        const float lr = dt * bf2f(p_are), li = dt * bf2f(p_aim);
        const cf dB{dt * bf2f(p_br), dt * bf2f(p_bi)};
        const cf CBt = cmul(cf{bf2f(p_cr), bf2f(p_ci)}, dB);
        const cf CB{CBt[0], -CBt[1]};
        const float a2 = lr * 1.44269504088896341f, th = li * 0.15915494309189535f;
        asm volatile("s_mov_b32 m0, %0" ::"s"(lbase + KG_BASE + (w >> 1) * KG_CH + (w & 1) * 256) : "memory");
#define PUT2(R0, Z0, R1, Z1) asm volatile("ds_write_addtid_b32 %0 offset:%2\n\tds_write_addtid_b32 %1 offset:%3" \
            ::"v"(pk2v(Z0)), "v"(pk2v(Z1)), "i"((R0) * KG_RS), "i"((R1) * KG_RS) : "memory")
#define PUT4(R0, Z0, R1, Z1, R2, Z2, R3, Z3) asm volatile("ds_write_addtid_b32 %0 offset:%4\n\tds_write_addtid_b32 %1 offset:%5\n\tds_write_addtid_b32 %2 offset:%6\n\tds_write_addtid_b32 %3 offset:%7" \
            ::"v"(pk2v(Z0)), "v"(pk2v(Z1)), "v"(pk2v(Z2)), "v"(pk2v(Z3)), "i"((R0) * KG_RS), "i"((R1) * KG_RS), "i"((R2) * KG_RS), "i"((R3) * KG_RS) : "memory")
        const cf z1 = zdir(a2, -th, 1.f), Z256 = zdir(a2, -th, 256.f), w8 = zdir(a2, th, 8.f);
        cf z2, z3, z4, z5, z6, z7, w16, q1, q2, q3, q4_, q5, q6, q7, q256, q512, q768, q1024, q1280, q1536, q1792, q2048, r0, r1, r2, r3, r4;
        cmul2(z2, z1, z1, w16, w8, w8);
        cmul2(q256, CB, Z256, q1, CB, z1);
        cmul2(z3, z2, z1, z4, z2, z2);
        cmul2(q512, q256, Z256, q2, CB, z2);
        cmul2(z5, z4, z1, z6, z4, z2);
        PUT4(0, CB, 1, q1, 8, q256, 2, q2);
        cmul2(z7, z4, z3, q3, CB, z3);
        cmul2(q768, q512, Z256, q4_, CB, z4);
        cmul2(q5, CB, z5, q1024, q768, Z256);
        cmul2(q6, CB, z6, q1280, q1024, Z256);
        PUT4(12, q512, 3, q3, 14, q768, 4, q4_);
        cmul2(q7, CB, z7, q1536, q1280, Z256);
        cmul2(r0, q256, z2, q1792, q1536, Z256);
        PUT4(5, q5, 16, q1024, 6, q6, 17, q1280);
        cmul2(r1, q256, z4, q2048, q1792, Z256);
        cmul2(r2, q256, z6, r3, q512, z4);
        PUT4(7, q7, 18, q1536, 9, r0, 19, q1792);
        cf pa{1.f, 0.f}, pb = w8, pc_, pd, w32;
        cmul2(r4, q768, z4, w32, w16, w16);
        cmul2(pc_, pa, w16, pd, pb, w16);
        PUT4(10, r1, 20, q2048, 11, r2, 13, r3);
        TS(4);
        sfor<0, 8>([&](auto J) {
            constexpr int jj = J.v;
            cf na, nb, nc, nd;
            if constexpr (jj < 7) { cmul2(na, pa, w32, nb, pb, w32); cmul2(nc, pc_, w32, nd, pd, w32); }
            PUT4(21 + 4 * jj, pa, 22 + 4 * jj, pb, 23 + 4 * jj, pc_, 24 + 4 * jj, pd);
            if constexpr (jj == 0) asm volatile("ds_write_addtid_b32 %0 offset:%1" ::"v"(pk2v(r4)), "i"(15 * KG_RS) : "memory");
            if constexpr (jj < 7) { pa = na; pb = nb; pc_ = nc; pd = nd; }
            if constexpr (jj == PUBLISH_AT) {
                SCHED();
                WAITV(0);
                BAR();
                if (tid == 0) { st_flag(ws, WS_FX + tb * 128, tag); st_flag(ws, WS_FXP + tb * 8, tag); }
                SCHED();
            }
        });
#undef PUT2
#undef PUT4
        if (tid < 2) *(LAS unsigned short*)(lds + L_KC + tid * 1312 + 649 * 2) = 0;
        LBAR();
    }
    TS(5);
    bf16x8 xf[32];
    const unsigned xvo = (lane & 31) * 32 + (lane >> 5) * 16, xsb = WS_XN + (mt * 4 + w) * 32 * 1024;
    unsigned xso;
#define LOAD_X(U) do { if constexpr (((U) & 3) == 0) aload_a_first<((U) >> 2) * 4096, 0>(xf[U], xso, rs, xvo, xsb); \
                       else aload_a_next<((U) & 3) * 1024>(xf[U], xso, rs, xvo); } while (0)
    {
        LAS char* qa = lds + KG_BASE + (w >> 1) * KG_CH + l15 * KG_RS + g16 * 16;
        LAS char* pa_ = qa + (21 + 16 * (w & 1)) * KG_RS;
        bf16x8 fa[8], fb[8], fc[8];
        u64 fxw;
        sfor<0, 8>([&](auto S) {
            constexpr int s_ = S.v;
            fb[s_] = *(LAS bf16x8*)(pa_ + s_ * 64); fa[s_] = *(LAS bf16x8*)(qa + s_ * 64); fc[s_] = *(LAS bf16x8*)(qa + 16 * KG_RS + s_ * 64);
            if constexpr (s_ < 7) STAGE_W1(1, s_);
            else aload8sc1(fxw, ws + WS_FX + (16 * mt + 4 * w) * 128, (lane & 3) * 128);
        });
        SCHED();
        TS(6);
        f32x4 k0, k1;
        sfor<0, 8>([&](auto S) {
            mfma16_vv<S.v == 0>(k0, fa[S.v], fb[S.v]);
            mfma16_vv<S.v == 0>(k1, fc[S.v], fb[S.v]);
        });
        SCHED();
        asm volatile("s_nop 7" : "+v"(k0), "+v"(k1));
#pragma unroll
        for (int r = 0; r < 4; r += 2) {
            const unsigned a0 = pk2(k0[r], k0[r + 1]), a1 = pk2(k1[r], k1[r + 1]);
            *(LAS unsigned short*)(size_t)ksc[r] = (unsigned short)a0;
            *(LAS unsigned short*)(size_t)ksc[r + 1] = (unsigned short)(a0 >> 16);
            *(LAS unsigned short*)(size_t)ksc[4 + r] = (unsigned short)a1;
            *(LAS unsigned short*)(size_t)ksc[5 + r] = (unsigned short)(a1 >> 16);
        }
        SCHED();
        asm volatile("s_waitcnt vmcnt(0)" : "+v"(fxw)::"memory");
        if (!__all(fxw == tag)) wait_flags<1>(ws, WS_FX + (16 * mt + 4 * w) * 128, 4, tag, lane);
        TS(7);
        sfor<0, 8>([&](auto U) { LOAD_X(U.v); });
        SCHED();
    }
    SCHED();
    LBAR();
    TS(8);
    {
        unsigned val[2][9];
#pragma unroll
        for (int ch = 0; ch < 2; ++ch)
#pragma unroll
            for (int e = 0; e < 9; ++e) val[ch][e] = *(LAS unsigned short*)(size_t)(kga[e] + ch * 1312);
        SCHED();
        sfor<0, 7>([&](auto I) { STAGE_W1(2, I.v); });
        {
            const int r = 35 * (w >> 1) + (lane < 35 ? lane : 34), row = (r * 147) >> 10;
            warm4(pf, (w & 1) ? wu : wg, (192 + 10 * j + row) * (F_ * 2) + (xcd & 3) * 896 + (r - 7 * row) * 128);
        }
#pragma unroll
        for (int ch = 0; ch < 2; ++ch) {
            u32x4 E, O;
#pragma unroll
            for (int m = 0; m < 4; ++m) { E[m] = val[ch][2 * m] | (val[ch][2 * m + 1] << 16); O[m] = val[ch][2 * m + 1] | (val[ch][2 * m + 2] << 16); }
            *(LAS u32x4*)(lds + L_KIMG + ch * CV_KCH + tid * 16) = E;
            *(LAS u32x4*)(lds + L_KIMG + ch * CV_KCH + 4160 + tid * 16) = O;
        }
        if (tid < 16) *(LAS u32x4*)(lds + L_KIMG + (tid >> 3) * CV_KCH + ((tid >> 2) & 1) * 4160 + 4096 + (tid & 3) * 16) = u32x4{0, 0, 0, 0};
    }
    SCHED();
    TS(9);

    f32x16 acc[7];
    u32x4 uu0, uu1;
    bf16x8 wf[28][2];
    const unsigned wtvo0 = lane * 16 + (w & 1) * 1024, wtvo1 = wtvo0 ^ 1024, wtsb = WS_WT + (28 * w * 16 + 2 * dt2) * 1024;
    unsigned wtso;
#define LOAD_WT(I) do { if constexpr (((I) & 1) == 0) aload_a_first<((I) >> 1) * 16384, 0>(wf[(I) >> 1][0], wtso, rs, wtvo0, wtsb); \
                        else aload_a_next<0>(wf[(I) >> 1][1], wtso, rs, wtvo1); } while (0)
    {
        const unsigned ab0 = lbase + (g16 & 1) * G1_UP + (8 * (g16 >> 1) + q4) * 224 + phi * 8, ab1 = ab0 + 2 * G1_SLOT, ab2 = ab0 + 4 * G1_SLOT;
        auto rdw = [&](auto STP, auto P) {
            constexpr int st = STP.v, p = P.v, sl = g1slot(st >> 2), off = (sl & 1) * G1_SLOT + (st & 3) * 16 * 224 + p * 32;
            const unsigned base = sl < 2 ? ab0 : sl < 4 ? ab1 : ab2;
            return cat8(atr<off>(base), atr<off + 4 * 224>(base));
        };
        bf16x8 cur[7], nxt[7];
        u32x4 fx[2];
        waitv<G1S.younger(1, 1, 0)>();
        LBAR();
        sfor<0, 7>([&](auto P) { cur[P.v] = rdw(IC<0>{}, P); });
        SCHED();
        TS(10);
        sfor<0, 32>([&](auto ST) {
            constexpr int st = ST.v, s = st >> 2, kk = st & 3;
            if constexpr (kk == 0 && s > 0) {
                G1_STAMP(2 * s - 2);
                if constexpr (s <= 6) waitv<G1S.younger(1, s + 1, st)>();
                BAR();
                G1_STAMP(2 * s - 1);
                SCHED();
            }
            if constexpr (st == 6) {
                asm volatile("s_waitcnt vmcnt(%2)" : "+v"(fx[0]), "+v"(fx[1]) : "i"(G1S.younger(3, 1, st)) : "memory");
                const u64 f0 = ((u64)fx[0][1] << 32) | fx[0][0], f1 = ((u64)fx[0][3] << 32) | fx[0][2], f2 = ((u64)fx[1][1] << 32) | fx[1][0], f3 = ((u64)fx[1][3] << 32) | fx[1][2];
                if (!__all(f0 == tag && f1 == tag && f2 == tag && f3 == tag)) wait_flags<4>(ws, WS_FX, 64, tag, lane);
                SCHED();
            }
            waitv<G1S.younger(0, st, st)>();
            sfor<0, 7>([&](auto P) {
                constexpr int p = P.v;
                if constexpr (p == 0) waitl<8>(); else if constexpr (p == 3) waitl<(st < 31 ? 10 : 4)>(); else if constexpr (p == 5) waitl<(st < 31 ? 10 : 0)>();
                mfma32_va<st == 0>(acc[p], cur[p], xf[st]);
                if constexpr (st < 31) nxt[p] = rdw(IC<st + 1>{}, P);
                sfor<0, G1S.n>([&](auto K) {
                    constexpr G1Op o = G1S.op[K.v];
                    if constexpr (o.step == st && o.gap == p) {
                        if constexpr (o.kind == 0) LOAD_X(o.a);
                        else if constexpr (o.kind == 1) STAGE_W1(o.a, o.b);
                        else if constexpr (o.kind == 2) LOAD_WT(o.a);
                        else if constexpr (o.kind == 3) aload_v<o.a * 16>(fx[o.a], rs, WS_FXP + lane * 32, 0);
                        else aload_v<0>(o.a == 0 ? uu0 : uu1, rs, WS_XT + ((tid >> 1) * 512 + c0 + (tid & 1)) * 16, o.a * 128 * 512 * 16);
                    }
                });
                SCHED();
            });
#pragma unroll
            for (int p = 0; p < 7; ++p) cur[p] = nxt[p];
        });
    }
    MFMA_DRAIN();
    TS(11);
    asm volatile("s_waitcnt vmcnt(%3)" : "+v"(uu0), "+v"(uu1), "+v"(pf) : "i"(G1S.younger(4, 1, 32)) : "memory");
    if (tid < 64) *(LAS u32x4*)(lds + L_U + (tid >> 5) * CV_UCH + (tid & 31) * 16) = u32x4{0, 0, 0, 0};
    *(LAS u32x4*)(lds + L_U + (tid & 1) * CV_UCH + 512 + (tid >> 1) * 16) = uu0;
    *(LAS u32x4*)(lds + L_U + (tid & 1) * CV_UCH + 512 + 2048 + (tid >> 1) * 16) = uu1;
    SCHED();
    TS(12);
    const int chl = w >> 1, rh = w & 1;
    LAS char* ub = lds + L_U + chl * CV_UCH + 2 * (256 + 16 * (l15 - (g16 >> 1)) + 8 * (g16 & 1)) - 32 * (8 * rh + 6);
    LAS char* kbm[8];
    {
        const int i0 = 2047 - 16 * (g16 >> 1) - l15 + 8 * (g16 & 1);
        LAS char* kb = lds + L_KIMG + chl * CV_KCH + ((l15 & 1) ? 0 : 4160) + 4 * (i0 >> 1) - 32 * (8 * rh + 6);
#pragma unroll
        for (int m = 0; m < 8; ++m) { kbm[m] = kb - 512 * m; asm volatile("" : "+v"(kbm[m])); }
    }
    auto ldG = [&](int m, int rr) {
        const LAS unsigned* p = (const LAS unsigned*)(kbm[m] + 64 * (3 - rr));
        u32x4 t; t[0] = p[0]; t[1] = p[1]; t[2] = p[2]; t[3] = p[3];
        return __builtin_bit_cast(bf16x8, t);
    };
    auto ldF = [&](int m, int rr) { return *(const LAS bf16x8*)(ub + 64 * (3 - rr) + 512 * m); };
    bf16x8 G[2][8], Fr[2][8];
    {
        const unsigned hvo = WS_H + (32 * (w & 1) + (lane & 31)) * 32 + (lane >> 5) * 16, hso = ((mt * 2 + (w >> 1)) * 112 + chunk * 7) * 2048;
        sfor<0, 7>([&](auto P) {
            constexpr int p = P.v;
            u32x4 o;
            f32x2 e[4], gu[4];
#pragma unroll
            for (int r = 0; r < 4; ++r) {
                const f32x2 g = {acc[p][2 * r], acc[p][2 * r + 1]}, u = {acc[p][8 + 2 * r], acc[p][9 + 2 * r]};
                const f32x2 t = g * -1.44269504088896341f;
                e[r] = f32x2{__builtin_amdgcn_exp2f(t[0]), __builtin_amdgcn_exp2f(t[1])};
                gu[r] = g * u;
            }
            SCHED();
            sfor<G1_WT + (2 * p * WT_EPI) / 14, G1_WT + ((2 * p + 1) * WT_EPI) / 14>([&](auto I) { LOAD_WT(I.v); });
#pragma unroll
            for (int r = 0; r < 4; ++r) {
                const f32x2 d = e[r] + 1.0f;
                e[r] = f32x2{__builtin_amdgcn_rcpf(d[0]), __builtin_amdgcn_rcpf(d[1])};
            }
#pragma unroll
            for (int r = 0; r < 4; ++r) o[r] = pk2v(gu[r] * e[r]);
            SCHED();
            sfor<G1_WT + ((2 * p + 1) * WT_EPI) / 14, G1_WT + ((2 * p + 2) * WT_EPI) / 14>([&](auto I) { LOAD_WT(I.v); });
            st128(rs, o, hvo, hso + p * 2048);
            SCHED();
            if constexpr (p == 2) { LBAR(); SCHED(); }
            if constexpr (p >= 3) { G[0][2 * p - 6] = ldG(2 * p - 6, 0); G[0][2 * p - 5] = ldG(2 * p - 5, 0); Fr[0][2 * p - 6] = ldF(2 * p - 6, 0); Fr[0][2 * p - 5] = ldF(2 * p - 5, 0); SCHED(); }
        });
    }
    TS(13);

    constexpr int PD = 12;
    bf16x8 hf[PD][2];
    const unsigned hvoff0 = WS_H + (lane & 31) * 32 + (lane >> 5) * 16 + (w >> 1) * 1024, hvoff1 = hvoff0 ^ 1024;
    const unsigned hsoff = ((mt2 * 2 + th2) * 112 + 28 * w) * 2048;
    auto ldh = [&](int s, int t) { return asbf(ld128(rs, (t ? hvoff1 : hvoff0) + (s & 1) * 2048, hsoff + (s >> 1) * 4096)); };
    f32x4 cacc[8];
    {
        u64 fh;
        sfor<0, 4>([&](auto RR) {
            constexpr int rr = RR.v, c = rr & 1, n = c ^ 1;
            CV_STAMP(rr);
            if constexpr (rr == 1) {
                waitv<((56 - G1_WT - WT_EPI + 2) / 3 < 9 ? (56 - G1_WT - WT_EPI + 2) / 3 : 9)>();
                BAR();
                if (tid == 0) st_flag(ws, WS_FH + (mt * 16 + chunk) * 128, tag);
                SCHED();
            }
            if constexpr (rr == 3) {
                asm volatile("s_waitcnt vmcnt(0)" : "+v"(fh)::"memory");
                if (!__all(fh == tag)) wait_flags<1>(ws, WS_FH + (mt2 * 16 + 4 * w) * 128, 4, tag, lane);
                SCHED();
            }
            sfor<0, 36>([&](auto C) {
                constexpr int cnt = C.v;
                constexpr int m = cnt < 8 ? 0 : cnt < 15 ? 1 : cnt < 21 ? 2 : cnt < 26 ? 3 : cnt < 30 ? 4 : cnt < 33 ? 5 : cnt < 35 ? 6 : 7;
                constexpr int first = m * 8 - (m * (m - 1)) / 2, i = m + (cnt - first);
                mfma16_vv<rr == 0 && m == 0>(cacc[i], G[c][m], Fr[c][i - m]);
                if constexpr (rr < 3) {
                    if constexpr (cnt < 8) Fr[n][cnt] = ldF(cnt, rr + 1);
                    else if constexpr (cnt < 24 && !(cnt & 1)) G[n][(cnt - 8) >> 1] = ldG((cnt - 8) >> 1, rr + 1);
                }
                if constexpr (rr < 3 && cnt % 4 == 2) {
                    constexpr int nrest = 56 - G1_WT - WT_EPI, per = (nrest + 2) / 3, k = cnt / 4, i = G1_WT + WT_EPI + rr * per + k;
                    if constexpr (k < per && i < 56) LOAD_WT(i);
                }
                if constexpr (rr == 2 && cnt == 8) aload8sc1(fh, ws + WS_FH + (mt2 * 16 + 4 * w) * 128, (lane & 3) * 128);
                if constexpr (rr == 3 && cnt % 3 == 1 && cnt / 3 < 10) hf[cnt / 6][(cnt / 3) & 1] = ldh(cnt / 6, (cnt / 3) & 1);
                SCHED();
            });
        });
    }
    MFMA_DRAIN();
    TS(14);
    {
        LAS char* red = lds + L_RED;
#pragma unroll
        for (int i = 0; i < 4; ++i) {
            f32x4 lo = cacc[i], hi = cacc[4 + i];
            asm volatile("" : "+v"(lo), "+v"(hi));
            *(LAS f32x4*)(red + (w * 4 + i) * 1024 + lane * 16) = rh ? lo : hi;
            cacc[i] = rh ? hi : lo;
            hf[5 + i][0] = ldh(5 + i, 0); hf[5 + i][1] = ldh(5 + i, 1);
            SCHED();
        }
        LBAR();
        const float dsk = bf2f(__builtin_amdgcn_readfirstlane((int)p_dsk));
#pragma unroll
        for (int i = 0; i < 4; ++i) {
            const f32x4 y = cacc[i] + *(LAS f32x4*)(red + ((w ^ 1) * 4 + i) * 1024 + lane * 16);
            const int t = 256 * (4 * rh + i) + 16 * l15 + 4 * g16;
            const u32x2 uu = *(LAS u32x2*)(lds + L_U + chl * CV_UCH + 2 * (256 + t));
            cacc[i] = f32x4{y[0] + bflo(uu[0]) * dsk, y[1] + bfhi(uu[0]) * dsk, y[2] + bflo(uu[1]) * dsk, y[3] + bfhi(uu[1]) * dsk};
            if (i < 3) { hf[9 + i][0] = ldh(9 + i, 0); hf[9 + i][1] = ldh(9 + i, 1); }
            SCHED();
        }
#pragma unroll
        for (int i = 0; i < 4; ++i) {
            const int t = 256 * (4 * rh + i) + 16 * l15 + 4 * g16;
            st64(rs, u32x2{pk2(cacc[i][0], cacc[i][1]), pk2(cacc[i][2], cacc[i][3])}, WS_ST + ((c0 + chl) * L_ + t) * 2, 0);
        }
    }
    SCHED();
    TS(15);

    f32x16 a2[2][2];
    u32x4 sv[2], xv[2];
    u64 fs;
    const int d0 = 64 * dt2, t0 = mt2 * 128 + th2 * 64;
    const int tt2 = w >> 1, dh2 = w & 1;
    sfor<0, 24>([&](auto S) {
        constexpr int s = S.v;
        const bf16x8 h0 = hf[s % PD][0], h1 = hf[s % PD][1];
        mfma32_av<s == 0>(a2[0][0], wf[s][0], h0);
        mfma32_av<s == 0>(a2[1][0], wf[s][1], h0);
        SCHED();
        mfma32_av<s == 0>(a2[0][1], wf[s][0], h1);
        mfma32_av<s == 0>(a2[1][1], wf[s][1], h1);
        sfor<PD, 28>([&](auto T) {
            constexpr int t = T.v;
            if constexpr (((t - PD) * 21) / 16 == s) { hf[t % PD][0] = ldh(t, 0); hf[t % PD][1] = ldh(t, 1); }
        });
        if constexpr (s == 10) {
            G2_STAMP(0);
            waitv<G2_YOUNGER>();
            BAR();
            G2_STAMP(1);
            if (tid == 0) st_flag(ws, WS_FS + tb * 8, tag);
        }
        if constexpr (s == 17) aload8sc1(fs, ws + WS_FS + (d0 >> 1) * 8, (lane & 31) * 8);
        if constexpr (s == 21) {
            G2_STAMP(2);
            asm volatile("s_waitcnt vmcnt(%1)" : "+v"(fs) : "i"(G2_AFTER_FS) : "memory");
            G2_STAMP(3);
            if (!__all(fs == tag)) wait_flags<1, 8>(ws, WS_FS + (d0 >> 1) * 8, 32, tag, lane);
#pragma unroll
            for (int i = 0; i < 2; ++i) {
                const int e = tid + 256 * i;
                sv[i] = ld128(rs, WS_ST + ((d0 + (e >> 3)) * L_ + t0) * 2 + (e & 7) * 16, 0);
            }
#pragma unroll
            for (int g = 0; g < 2; ++g) xv[g] = *(const u32x4*)(x + (size_t)(t0 + 32 * tt2 + (lane & 31)) * D_ + d0 + 32 * dh2 + 16 * g + 8 * (lane >> 5));
        }
        SCHED();
    });
    {
        auto put = [&](auto DH, auto TT) {
            const int q = 2 * (TT.v ^ (w >> 1)) + (DH.v ^ (w & 1));
            const f32x16 a = a2[DH.v][TT.v];
            LAS char* p = lds + (q * 4 + w) * 4096 + lane * 16;
#pragma unroll
            for (int g = 0; g < 4; ++g) *(LAS f32x4*)(p + g * 1024) = f32x4{a[4 * g], a[4 * g + 1], a[4 * g + 2], a[4 * g + 3]};
        };
        sfor<24, 28>([&](auto S) { mfma32_av<false>(a2[1][1], wf[S.v][1], hf[S.v % PD][1]); });
        sfor<24, 28>([&](auto S) { mfma32_av<false>(a2[1][0], wf[S.v][1], hf[S.v % PD][0]); });
        SCHED();
        put(IC<1>{}, IC<1>{});
        SCHED();
        sfor<24, 28>([&](auto S) { mfma32_av<false>(a2[0][1], wf[S.v][0], hf[S.v % PD][1]); });
        SCHED();
        put(IC<1>{}, IC<0>{});
        SCHED();
        sfor<24, 28>([&](auto S) { mfma32_av<false>(a2[0][0], wf[S.v][0], hf[S.v % PD][0]); });
        SCHED();
        put(IC<0>{}, IC<1>{});
#pragma unroll
        for (int i = 0; i < 2; ++i) *(LAS u32x4*)(lds + L_ST + (tid + 256 * i) * 16) = sv[i];
        MFMA_DRAIN();
        TS(16);
        LBAR();
    }
    TS(17);
    {
        const f32x16 own = a2[0][0];
        f32x4 f[4];
#pragma unroll
        for (int g = 0; g < 4; ++g) {
            f[g] = f32x4{own[4 * g], own[4 * g + 1], own[4 * g + 2], own[4 * g + 3]};
#pragma unroll
            for (int o = 1; o < 4; ++o) f[g] += *(LAS f32x4*)(lds + ((w * 4 + ((w + o) & 3)) * 4 + g) * 1024 + lane * 16);
        }
        LAS char* sp = lds + L_ST + (32 * dh2 + 8 * (lane >> 5) + q4) * 128 + (32 * tt2 + 16 * (g16 & 1) + 4 * pp) * 2;
#pragma unroll
        for (int g2 = 0; g2 < 2; ++g2) {
            const s16x4 sa = tr_read(sp + g2 * 16 * 128), sb = tr_read(sp + (g2 * 16 + 4) * 128);
            const u32x2 s01 = __builtin_bit_cast(u32x2, sa), s23 = __builtin_bit_cast(u32x2, sb);
            u32x4 o;
            o[0] = pk2v(unpk(xv[g2][0]) + unpk(s01[0]) * silu2(f32x2{f[2 * g2][0], f[2 * g2][1]}));
            o[1] = pk2v(unpk(xv[g2][1]) + unpk(s01[1]) * silu2(f32x2{f[2 * g2][2], f[2 * g2][3]}));
            o[2] = pk2v(unpk(xv[g2][2]) + unpk(s23[0]) * silu2(f32x2{f[2 * g2 + 1][0], f[2 * g2 + 1][1]}));
            o[3] = pk2v(unpk(xv[g2][3]) + unpk(s23[1]) * silu2(f32x2{f[2 * g2 + 1][2], f[2 * g2 + 1][3]}));
            *(u32x4*)(out + (size_t)(t0 + 32 * tt2 + (lane & 31)) * D_ + d0 + 32 * dh2 + 16 * g2 + 8 * (lane >> 5)) = o;
        }
    }
    if (b == 77 && w == 0) {
        asm volatile("s_waitcnt vmcnt(0)" ::: "memory");
        u64 mn = ~0ull;
        for (int i = 0; i < 4; ++i) { const u64 v = ld_flag(ws, (20u << 20) + (lane * 4 + i) * 8); mn = v < mn ? v : mn; }
        for (int o = 32; o >= 1; o >>= 1) { const u64 a = __shfl_xor(mn, o); mn = a < mn ? a : mn; }
        const u64 now = __builtin_amdgcn_s_memrealtime();
        const u64 until = now + 4 * (probe_a ? probe_a - mn : 0);
        while (__builtin_amdgcn_s_memrealtime() < until) __builtin_amdgcn_s_sleep(1);
    }
    if (b == 0 && tid == 0) st_flag(ws, WS_EP, tag);
    TS(18);
    TSR(47);
    TS_DUMP();
    asm volatile(".Lfused_pf_end:" ::: "memory");
}

extern "C" void kernel_launch(void* const* d_in, const int* in_sizes, int n_in, void* d_out, int out_size, void* d_ws, size_t ws_size,
                              hipStream_t stream) {
    (void)hipFuncSetAttribute((const void*)fused_s4_ffn, hipFuncAttributeMaxDynamicSharedMemorySize, LDS_TOTAL);
    fused_s4_ffn<<<256, 256, LDS_TOTAL, stream>>>((const bf16_t*)d_in[0], (const bf16_t*)d_in[1], (const bf16_t*)d_in[2], (const bf16_t*)d_in[3],
                                                  (const bf16_t*)d_in[4], (const bf16_t*)d_in[5], (const bf16_t*)d_in[6], (const bf16_t*)d_in[7],
                                                  (const bf16_t*)d_in[8], (const bf16_t*)d_in[10], (const bf16_t*)d_in[11], (const bf16_t*)d_in[12],
                                                  (bf16_t*)d_out, (char*)d_ws);
}
```

```cpp
#include <hip/hip_runtime.h>
#include <stdint.h>

typedef unsigned short bf16_t;
typedef short bf16x8 __attribute__((ext_vector_type(8)));
typedef short s16x4 __attribute__((ext_vector_type(4)));
typedef float f32x2 __attribute__((ext_vector_type(2)));
typedef float f32x4 __attribute__((ext_vector_type(4)));
typedef float f32x16 __attribute__((ext_vector_type(16)));
typedef unsigned u32x4 __attribute__((ext_vector_type(4)));
typedef unsigned u32x2 __attribute__((ext_vector_type(2)));
typedef __bf16 bf16v2 __attribute__((ext_vector_type(2)));
typedef unsigned long long u64;
#define LAS __attribute__((address_space(3)))
#define GAS __attribute__((address_space(1)))
#define DI __device__ __forceinline__

constexpr int L_ = 2048, D_ = 512, N_ = 128, F_ = 1792;

constexpr unsigned WS_XN = 0;
constexpr unsigned WS_XT = 2u << 20;
constexpr unsigned WS_ST = 4u << 20;
constexpr unsigned WS_H = 6u << 20;
constexpr unsigned WS_WT = 14u << 20;
constexpr unsigned WS_FX = 16u << 20;
constexpr unsigned WS_FH = WS_FX + 256 * 128;
constexpr unsigned WS_FS = WS_FH + 256 * 128;
constexpr unsigned WS_FXP = WS_FS + 256 * 128;
constexpr unsigned WS_EP = WS_FXP + 256 * 8;
constexpr unsigned WS_USED = 17u << 20;

constexpr int G1_SLOT = 28800, G1_UP = 14336 + 128;
constexpr int g1slot(int s) { return s % 5 == 0 ? 0 : s % 5 == 1 ? 3 : s % 5 == 2 ? 4 : s % 5 == 3 ? 1 : 2; }
constexpr int KG_BASE = G1_SLOT, KG_RS = 528, KG_CH = 53 * KG_RS;
constexpr int L_WTS = 3 * G1_SLOT;
constexpr int L_KC = 144128;
constexpr int L_KIMG = 146816;
constexpr int L_LNS = L_KIMG, LN_RS = 1056;
constexpr int CV_KCH = 8320;
constexpr int L_U = 2 * G1_SLOT, CV_UCH = 4608;
constexpr int L_RED = L_U + 2 * CV_UCH;
constexpr int L_ST = 65536;
constexpr int LDS_TOTAL = 163840;
static_assert(KG_BASE + 2 * KG_CH <= 3 * G1_SLOT && KG_BASE + KG_CH + 256 < 65536, "K-gen scratch");
static_assert(L_KIMG + 2 * CV_KCH <= LDS_TOTAL && L_RED + 16384 <= 3 * G1_SLOT && L_ST + 8192 <= 3 * G1_SLOT, "lds map");

DI float bf2f(unsigned u16) { return __uint_as_float(u16 << 16); }
DI float bflo(unsigned w) { return __uint_as_float(w << 16); }
DI float bfhi(unsigned w) { return __uint_as_float(w & 0xffff0000u); }
DI unsigned pk2v(f32x2 v) { return __builtin_bit_cast(unsigned, __builtin_convertvector(v, bf16v2)); }
DI unsigned pk2(float lo, float hi) { return pk2v(f32x2{lo, hi}); }
DI f32x2 unpk(unsigned w) { return f32x2{bflo(w), bfhi(w)}; }
DI float rbf(float x) { return bflo(pk2(x, x)); }

#define WAITV(n) asm volatile("s_waitcnt vmcnt(" #n ")" ::: "memory")
#define WAITL(n) asm volatile("s_waitcnt lgkmcnt(" #n ")" ::: "memory")
#define BAR() do { asm volatile("" ::: "memory"); __builtin_amdgcn_s_barrier(); asm volatile("" ::: "memory"); } while (0)
#define LBAR() do { WAITL(0); BAR(); } while (0)
#define SCHED() __builtin_amdgcn_sched_barrier(0)

#define TS(i) do {} while (0)
#define TSR(i) do {} while (0)
#define TS_DUMP() do {} while (0)

template <int N> struct IC { static constexpr int v = N; };
template <int B, int E, class F> DI void sfor(F&& f) { if constexpr (B < E) { f(IC<B>{}); sfor<B + 1, E>(f); } }
template <int N> DI void waitv() { asm volatile("s_waitcnt vmcnt(%0)" ::"i"(N) : "memory"); }
template <int N> DI void waitl() { asm volatile("s_waitcnt lgkmcnt(%0)" ::"i"(N) : "memory"); }

template <int CTRL, int RMASK> DI float dpp_add(float v) {
    return v + __builtin_bit_cast(float, __builtin_amdgcn_update_dpp(0, __builtin_bit_cast(int, v), CTRL, RMASK, 0xf, true));
}
DI void wave_sum4(float& a, float& b, float& c, float& d) {
#define STEP(C, M) a = dpp_add<C, M>(a); b = dpp_add<C, M>(b); c = dpp_add<C, M>(c); d = dpp_add<C, M>(d);
    STEP(0x111, 0xf) STEP(0x112, 0xf) STEP(0x114, 0xf) STEP(0x118, 0xf) STEP(0x142, 0xa) STEP(0x143, 0xc)
#undef STEP
    a = __builtin_bit_cast(float, __builtin_amdgcn_readlane(__builtin_bit_cast(int, a), 63));
    b = __builtin_bit_cast(float, __builtin_amdgcn_readlane(__builtin_bit_cast(int, b), 63));
    c = __builtin_bit_cast(float, __builtin_amdgcn_readlane(__builtin_bit_cast(int, c), 63));
    d = __builtin_bit_cast(float, __builtin_amdgcn_readlane(__builtin_bit_cast(int, d), 63));
}
DI s16x4 tr_read(LAS char* p) { return __builtin_amdgcn_ds_read_tr16_b64_v4i16((LAS s16x4*)p); }
template <int OFF> DI s16x4 atr(unsigned addr) { s16x4 r; asm volatile("ds_read_b64_tr_b16 %0, %1 offset:%2" : "=v"(r) : "v"(addr), "i"(OFF)); return r; }
DI bf16x8 cat8(s16x4 a, s16x4 b) { return __builtin_shufflevector(a, b, 0, 1, 2, 3, 4, 5, 6, 7); }

typedef __amdgpu_buffer_rsrc_t rsrc_t;
constexpr int SC1 = 16;
DI u32x4 ld128(rsrc_t r, unsigned voff, unsigned soff) { return __builtin_amdgcn_raw_buffer_load_b128(r, voff, soff, SC1); }
DI void st128(rsrc_t r, u32x4 v, unsigned voff, unsigned soff) {
    asm volatile("buffer_store_dwordx4 %0, %1, %2, %3 offen sc1\n\ts_nop 1" ::"v"(v), "v"(voff), "s"(r), "s"(soff) : "memory");
}
DI void st64(rsrc_t r, u32x2 v, unsigned voff, unsigned soff) { __builtin_amdgcn_raw_buffer_store_b64(v, r, voff, soff, SC1); }
DI bf16x8 asbf(u32x4 v) { return __builtin_bit_cast(bf16x8, v); }

template <int SIMM, int LIMM, int IMM> DI void bdma_first(unsigned& so, rsrc_t r, unsigned voff, unsigned sbase, unsigned lbase) {
    asm volatile("s_add_u32 m0, %1, %2\n\ts_add_u32 %0, %3, %4\n\tbuffer_load_dwordx4 %5, %6, %0 offen offset:%7 lds"
                 : "=&s"(so) : "s"(lbase), "i"(LIMM), "s"(sbase), "i"(SIMM), "v"(voff), "s"(r), "i"(IMM) : "memory", "scc");
}
template <int IMM> DI void bdma_next(unsigned so, rsrc_t r, unsigned voff) {
    asm volatile("buffer_load_dwordx4 %0, %1, %2 offen offset:%3 lds" ::"v"(voff), "s"(r), "s"(so), "i"(IMM) : "memory");
}
template <int SIMM, int IMM> DI void aload_a_first(bf16x8& d, unsigned& so, rsrc_t r, unsigned voff, unsigned sbase) {
    asm volatile("s_add_u32 %1, %4, %5\n\tbuffer_load_dwordx4 %0, %2, %3, %1 offen offset:%6 sc1"
                 : "=&a"(d), "=&s"(so) : "v"(voff), "s"(r), "s"(sbase), "i"(SIMM), "i"(IMM) : "memory", "scc");
}
template <int IMM> DI void aload_a_next(bf16x8& d, unsigned so, rsrc_t r, unsigned voff) {
    asm volatile("buffer_load_dwordx4 %0, %1, %2, %3 offen offset:%4 sc1" : "=&a"(d) : "v"(voff), "s"(r), "s"(so), "i"(IMM) : "memory");
}
template <int IMM> DI void aload_v(u32x4& d, rsrc_t r, unsigned voff, unsigned so) {
    asm volatile("buffer_load_dwordx4 %0, %1, %2, %3 offen offset:%4 sc1" : "=&v"(d) : "v"(voff), "s"(r), "s"(so), "i"(IMM) : "memory");
}
DI void warm4(unsigned& pf, const void* sbase, unsigned voff) { asm volatile("global_load_dword %0, %1, %2" : "+v"(pf) : "v"(voff), "s"(sbase) : "memory"); }
DI void aload16(u32x4& d, const void* sbase, unsigned voff) { asm volatile("global_load_dwordx4 %0, %1, %2" : "=&v"(d) : "v"(voff), "s"(sbase) : "memory"); }
DI void aload2(unsigned& d, const void* sbase, unsigned voff) { asm volatile("global_load_ushort %0, %1, %2" : "=&v"(d) : "v"(voff), "s"(sbase) : "memory"); }
DI void aload8sc1(u64& d, const void* sbase, unsigned voff) { asm volatile("global_load_dwordx2 %0, %1, %2 sc1" : "=&v"(d) : "v"(voff), "s"(sbase) : "memory"); }

template <bool FIRST> DI void mfma32_va(f32x16& c, bf16x8 a, bf16x8 b) {
    if constexpr (FIRST) asm volatile("v_mfma_f32_32x32x16_bf16 %0, %1, %2, 0" : "=&v"(c) : "v"(a), "a"(b));
    else asm volatile("v_mfma_f32_32x32x16_bf16 %0, %1, %2, %0" : "+v"(c) : "v"(a), "a"(b));
}
template <bool FIRST> DI void mfma32_av(f32x16& c, bf16x8 a, bf16x8 b) {
    if constexpr (FIRST) asm volatile("v_mfma_f32_32x32x16_bf16 %0, %1, %2, 0" : "=&v"(c) : "a"(a), "v"(b));
    else asm volatile("v_mfma_f32_32x32x16_bf16 %0, %1, %2, %0" : "+v"(c) : "a"(a), "v"(b));
}
template <bool FIRST> DI void mfma16_vv(f32x4& c, bf16x8 a, bf16x8 b) {
    if constexpr (FIRST) asm volatile("v_mfma_f32_16x16x32_bf16 %0, %1, %2, 0" : "=&v"(c) : "v"(a), "v"(b));
    else asm volatile("v_mfma_f32_16x16x32_bf16 %0, %1, %2, %0" : "+v"(c) : "v"(a), "v"(b));
}
#define MFMA_DRAIN() asm volatile("s_nop 15\n\ts_nop 3" ::: "memory")

constexpr int G1_WT = 40;
struct G1Op { int step, gap, kind, a, b; };
struct G1Sched {
    G1Op op[160]; int n;
    constexpr G1Sched() : op{}, n(0) {
        for (int i = 0; i < 7; ++i) op[n++] = G1Op{-1, 0, 1, 1, i};
        for (int u = 0; u < 8; ++u) op[n++] = G1Op{-1, 0, 0, u, 0};
        for (int i = 0; i < 7; ++i) op[n++] = G1Op{-1, 0, 1, 2, i};
        op[n++] = G1Op{-1, 0, 6, 0, 0};
        int wt = 0;
        for (int s = 0; s < 8; ++s) {
            G1Op list[24] = {}; int m = 0;
            if (s == 0) for (int i = 0; i < 2; ++i) list[m++] = G1Op{0, 0, 3, i, 0};
            if (s < 3) { for (int i = 0; i < 4; ++i) list[m++] = G1Op{0, 0, 0, 8 + 8 * s + i, 0};
                         for (int i = 0; i < 4; ++i) list[m++] = G1Op{0, 0, 1, 3 + s, i};
                         for (int i = 4; i < 8; ++i) list[m++] = G1Op{0, 0, 0, 8 + 8 * s + i, 0};
                         for (int i = 4; i < 7; ++i) list[m++] = G1Op{0, 0, 1, 3 + s, i}; }
            if (s == 3 || s == 4) for (int i = 0; i < 7; ++i) list[m++] = G1Op{0, 0, 1, 3 + s, i};
            if (s == 5) for (int i = 0; i < 2; ++i) list[m++] = G1Op{0, 0, 4, i, 0};
            const int nw = s < 3 ? 0 : s < 5 ? 6 : s < 7 ? 10 : 8;
            for (int i = 0; i < nw; ++i) list[m++] = G1Op{0, 0, 2, wt++, 0};
            const int span = s == 7 ? 21 : 28;
            for (int i = 0; i < m; ++i) { const int g = (i * span) / m; list[i].step = 4 * s + g / 7; list[i].gap = g % 7; op[n++] = list[i]; }
        }
    }
    constexpr int younger(int kind, int a, int st) const {
        int last = -1;
        for (int i = 0; i < n; ++i) if (op[i].kind == kind && op[i].a == a) last = i;
        int c = 0;
        for (int i = last + 1; i < n; ++i) if (op[i].step < st) ++c;
        return c;
    }
};
constexpr G1Sched G1S{};

DI u64 ld_flag(const char* ws, unsigned off) { return __hip_atomic_load((GAS u64*)(ws + off), __ATOMIC_RELAXED, __HIP_MEMORY_SCOPE_AGENT); }
DI void st_flag(char* ws, unsigned off, u64 v) { __hip_atomic_store((GAS u64*)(ws + off), v, __ATOMIC_RELAXED, __HIP_MEMORY_SCOPE_AGENT); }
template <int NPL, int STRIDE = 128>
DI void wait_flags(const char* ws, unsigned base, int nl, u64 tag, int lane) {
    const unsigned off = base + (lane < nl ? lane : 0) * STRIDE;
#pragma unroll 1
    for (int spin = 0; spin < 40000; ++spin) {
        bool ok = true;
#pragma unroll
        for (int i = 0; i < NPL; ++i) ok &= ld_flag(ws, off + i * 64 * STRIDE) == tag;
        if (__all(ok)) break;
        __builtin_amdgcn_s_sleep(1);
    }
    asm volatile("" ::: "memory");
}

typedef f32x2 cf;
DI cf cmul(cf a, cf b) { return cf{a[0] * b[0] - a[1] * b[1], a[0] * b[1] + a[1] * b[0]}; }
DI void cmul2(cf& z0, cf a0, cf b0, cf& z1, cf a1, cf b1) {
    cf t0, t1;
    asm("v_pk_mul_f32 %2, %4, %5 op_sel_hi:[0,1]\n\tv_pk_mul_f32 %3, %6, %7 op_sel_hi:[0,1]\n\t"
        "v_pk_fma_f32 %0, %4, %5, %2 op_sel:[1,1,0] op_sel_hi:[1,0,1] neg_lo:[0,1,0]\n\t"
        "v_pk_fma_f32 %1, %6, %7, %3 op_sel:[1,1,0] op_sel_hi:[1,0,1] neg_lo:[0,1,0]"
        : "=&v"(z0), "=&v"(z1), "=&v"(t0), "=&v"(t1) : "v"(a0), "v"(b0), "v"(a1), "v"(b1));
}
DI cf zdir(float a2, float th, float l) {
    const float m = __builtin_amdgcn_exp2f(a2 * l);
    const float r = __builtin_amdgcn_fractf(th * l);
    return cf{m * __builtin_amdgcn_cosf(r), m * __builtin_amdgcn_sinf(r)};
}
DI f32x2 silu2(f32x2 g) {
    const f32x2 t = g * -1.44269504088896341f;
    const f32x2 d = f32x2{__builtin_amdgcn_exp2f(t[0]), __builtin_amdgcn_exp2f(t[1])} + 1.0f;
    return g * f32x2{__builtin_amdgcn_rcpf(d[0]), __builtin_amdgcn_rcpf(d[1])};
}

#define PUBLISH_AT 5
constexpr int g2_younger() { int c = 0; for (int t = 12; t < 28; ++t) if (((t - 12) * 21) / 16 <= 10) c += 2; return c; }
#define G2_YOUNGER g2_younger()
#define G1_STAMP(i) do {} while (0)
#define CV_STAMP(i) do {} while (0)
#define G2_STAMP(i) do {} while (0)

extern __shared__ __attribute__((aligned(16))) char dyn_smem[];

__global__ __launch_bounds__(256, 1) void fused_s4_ffn(const bf16_t* __restrict__ x, const bf16_t* __restrict__ A_re, const bf16_t* __restrict__ A_im,
                                                       const bf16_t* __restrict__ B_re, const bf16_t* __restrict__ B_im, const bf16_t* __restrict__ C_re,
                                                       const bf16_t* __restrict__ C_im, const bf16_t* __restrict__ log_dt, const bf16_t* __restrict__ Dskip,
                                                       const bf16_t* __restrict__ wg, const bf16_t* __restrict__ wu, const bf16_t* __restrict__ wd,
                                                       bf16_t* __restrict__ out, char* ws) {
    LAS char* lds = (LAS char*)dyn_smem;
    const unsigned lbase = (unsigned)(size_t)lds;
    const int tid = threadIdx.x, w = __builtin_amdgcn_readfirstlane(tid >> 6), lane = tid & 63;
    const int b = blockIdx.x, xcd = b & 7, j = b >> 3;
    const int tb = xcd * 32 + j;
    const int mt = 8 * (xcd >> 2) + (j >> 2), chunk = 4 * (xcd & 3) + (j & 3);
    const int mt2 = 2 * xcd + (j >> 4), th2 = (j >> 3) & 1, dt2 = j & 7;
    const int c0 = 2 * tb;
    TSR(46); TS(0);

    u32x4 raw[2];
#pragma unroll
    for (int r = 0; r < 2; ++r) aload16(raw[r], x + (size_t)(8 * tb + 2 * w + r) * D_, lane * 16);
    unsigned pf = 0;
    {
        u64 pc; unsigned len;
        asm volatile("s_getpc_b64 %0\n.Lfused_pf_here:\n\ts_mov_b32 %1, .Lfused_pf_end-.Lfused_pf_here" : "=s"(pc), "=s"(len));
        const unsigned o = (unsigned)(12 * j + (lane < 12 ? lane : 11)) * 128u;
        warm4(pf, (const void*)pc, o < len ? o : len);
    }
    u64 ep;
    aload8sc1(ep, ws + WS_EP, 0);
    const int kch = c0 + (w >> 1);
    const unsigned kpo = (64 * (w & 1) + lane) * 2;
    unsigned p_are, p_aim, p_br, p_bi, p_cr, p_ci, p_ldt, p_dsk;
    aload2(p_are, A_re + kch * N_, kpo); aload2(p_aim, A_im + kch * N_, kpo); aload2(p_br, B_re + kch * N_, kpo);
    aload2(p_bi, B_im + kch * N_, kpo); aload2(p_cr, C_re + kch * N_, kpo); aload2(p_ci, C_im + kch * N_, kpo);
    aload2(p_ldt, log_dt + kch, 0); aload2(p_dsk, Dskip + kch, 0);
    SCHED();
    const rsrc_t rs = __builtin_amdgcn_make_buffer_rsrc(ws, 0, WS_USED, 0x00020000);
    const rsrc_t rw1 = __builtin_amdgcn_make_buffer_rsrc((void*)((w & 1) ? wu : wg), 0, D_ * F_ * 2, 0x00020000);
    const rsrc_t rwd = __builtin_amdgcn_make_buffer_rsrc((void*)wd, 0, D_ * F_ * 2, 0x00020000);
    const int wtU0 = 7 * (b >> 4) + w, wtU1 = w < 3 ? wtU0 + 4 : wtU0;
    {
        unsigned so;
        const unsigned vo = (lane >> 2) * 1024 + (lane & 3) * 16;
        bdma_first<0, L_WTS, 0>(so, rwd, vo, wtU0 * 16384 + (b & 15) * 64, lbase + w * 2048);
        bdma_first<0, L_WTS + 1024, 0>(so, rwd, vo, wtU1 * 16384 + (b & 15) * 64, lbase + w * 2048);
    }
    unsigned voff[7];
    {
        const int k = (lane * 147) >> 11, c = lane - 14 * k;
        unsigned v = k * (F_ * 2) + c * 16;
        int cc = c;
#pragma unroll
        for (int i = 0; i < 7; ++i) {
            voff[i] = v - (i & 3) * 1024;
            cc += 8; v += 4 * (F_ * 2) + 128;
            if (cc >= 14) { cc -= 14; v += F_ * 2 - 224; }
        }
    }
    const unsigned w1s = (32 * (w >> 1)) * (F_ * 2) + chunk * 224;
    const unsigned w1l = lbase + (w & 1) * G1_UP + (w >> 1) * 7168;
    unsigned w1so;
#define STAGE_W1(S, I) do { if constexpr (((I) & 3) == 0) bdma_first<64 * (S) * (F_ * 2), g1slot(S) * G1_SLOT + (I) * 1024, 0>(w1so, rw1, voff[I], w1s, w1l); \
                            else bdma_next<((I) & 3) * 1024>(w1so, rw1, voff[I]); } while (0)
    SCHED();
    const int g16 = lane >> 4, q4 = (lane & 15) >> 2, pp = lane & 3, l15 = lane & 15;
    const int phi = ((pp & 1) << 1) | (pp >> 1);
    unsigned ksc[8];
    {
        const int col8 = 8 * (16 * (w & 1) + l15);
        const unsigned kcb = lbase + L_KC + (w >> 1) * 1312, kcf = kcb - 2 * 17024;
        const int A = (g16 == 0 ? 0 : g16 == 1 ? 4 : g16 == 2 ? 256 : 512) + col8, B0 = g16 == 2 ? 2 : g16 == 3 ? 4 : 1, B1 = g16 == 2 ? 4 : g16 == 3 ? 256 : 2;
#pragma unroll
        for (int r = 0; r < 4; ++r) {
            const int v = A + (r & 1) * B0 + (r >> 1) * B1;
            ksc[r] = v < 128 ? kcb + 2 * v : kcf + 2 * (__float_as_uint((float)v) >> 16);
            asm volatile("" : "+v"(ksc[r]));
        }
#pragma unroll
        for (int r = 0; r < 4; ++r) {
            const unsigned a = kcf + 2 * (__float_as_uint((float)(1024 + 256 * r + col8)) >> 16);
            ksc[4 + r] = g16 == 0 ? a : (r == 0 && g16 == 1 && col8 == 0) ? kcb + 2 * 640 : kcb + 2 * 648;
            asm volatile("" : "+v"(ksc[4 + r]));
        }
    }
    unsigned kga[9];
    {
        const float f = (float)(2047 - 8 * tid);
        const unsigned kcf = lbase + L_KC - 2 * 17024;
#pragma unroll
        for (int e = 0; e < 9; e += 2) {
            const unsigned bits = pk2(f - e, f - (e + 1));
            kga[e] = kcf + 2 * (bits & 0xffffu);
            if (e < 8) kga[e + 1] = kcf + 2 * (bits >> 16);
        }
        if (w == 3) {
#pragma unroll
            for (int e = 0; e < 9; ++e) {
                const int l = 2047 - 8 * tid - e;
                if (l < 256) kga[e] = lbase + L_KC + 2 * (l < 0 ? 649 : l);
            }
        }
#pragma unroll
        for (int e = 0; e < 9; ++e) asm volatile("" : "+v"(kga[e]));
    }
    SCHED();
    {
        const int r = 21 * (w >> 1) + (lane < 21 ? lane : 20), row = (r * 147) >> 10;
        warm4(pf, (w & 1) ? wu : wg, (6 * j + row) * (F_ * 2) + (xcd & 3) * 896 + (r - 7 * row) * 128);
    }
    SCHED();
    asm volatile("s_waitcnt vmcnt(3)" : "+v"(ep), "+v"(raw[0]), "+v"(raw[1]), "+v"(p_are), "+v"(p_aim), "+v"(p_br), "+v"(p_bi), "+v"(p_cr), "+v"(p_ci),
                 "+v"(p_ldt), "+v"(p_dsk)::"memory");
    SCHED();
    TS(1);
    {
        f32x2 v[2][4];
        float s1[2], s2[2], x0[2];
        sfor<0, 2>([&](auto R) {
            constexpr int r = R.v;
            x0[r] = bflo((unsigned)__builtin_amdgcn_readfirstlane((int)raw[r][0]));
            f32x2 a = {0.f, 0.f}, q = {0.f, 0.f};
#pragma unroll
            for (int jj = 0; jj < 4; ++jj) { v[r][jj] = unpk(raw[r][jj]); const f32x2 d = v[r][jj] - x0[r]; a += d; q += d * d; }
            s1[r] = a[0] + a[1]; s2[r] = q[0] + q[1];
            STAGE_W1(0, r);
            SCHED();
        });
        wave_sum4(s1[0], s2[0], s1[1], s2[1]);
        STAGE_W1(0, 2);
        SCHED();
        sfor<0, 2>([&](auto R) {
            constexpr int r = R.v;
            const float md = s1[r] * (1.0f / 512.0f);
            const float var = fmaxf(s2[r] * (1.0f / 512.0f) - md * md, 0.f);
            const float mb = rbf(x0[r] + md);
            const float sd = rbf(__builtin_amdgcn_sqrtf(rbf(rbf(var) + 1e-5f)));
            const float rsd = __builtin_amdgcn_rcpf(sd);
            STAGE_W1(0, 3 + 2 * r);
            SCHED();
            u32x4 o;
#pragma unroll
            for (int jj = 0; jj < 4; ++jj) o[jj] = pk2v(unpk(pk2v(v[r][jj] - mb)) * rsd);
            *(LAS u32x4*)(lds + L_LNS + (2 * w + r) * LN_RS + lane * 16) = o;
            STAGE_W1(0, 4 + 2 * r);
            SCHED();
        });
    }
    WAITV(8);
    LBAR();
    TS(2);
    {
        LAS char* tp = lds + L_WTS + w * 2048 + (8 * (g16 >> 1) + q4) * 64 + (g16 & 1) * 32 + phi * 8;
        const bf16x8 f0 = cat8(tr_read(tp), tr_read(tp + 256)), f1 = cat8(tr_read(tp + 1024), tr_read(tp + 1024 + 256));
        st128(rs, __builtin_bit_cast(u32x4, f0), WS_WT + lane * 16, (wtU0 * 16 + (b & 15)) * 1024);
        if (w < 3) st128(rs, __builtin_bit_cast(u32x4, f1), WS_WT + lane * 16, (wtU1 * 16 + (b & 15)) * 1024);
    }
    SCHED();

    {
        const int half = tid & 1, tok = (tid >> 1) & 7, U = tid >> 4;
        const u32x4 va = *(LAS u32x4*)(lds + L_LNS + tok * LN_RS + U * 32 + half * 16);
        const u32x4 vb = *(LAS u32x4*)(lds + L_LNS + tok * LN_RS + U * 32 + half * 16 + 512);
        LAS char* tp = lds + L_LNS + q4 * LN_RS + (128 * w + 16 * g16 + 4 * pp) * 2;
        const s16x4 a0 = tr_read(tp), b0 = tr_read(tp + 4 * LN_RS), a1 = tr_read(tp + 128), b1 = tr_read(tp + 4 * LN_RS + 128);
        const unsigned xo = WS_XN + ((tb >> 2) * 32 + U) * 1024 + ((tb & 3) * 8 + tok) * 32 + half * 16;
        st128(rs, va, xo, 0);
        st128(rs, vb, xo, 16 * 1024);
        const unsigned to = WS_XT + (tb * 512 + 128 * w + 16 * g16 + l15) * 16;
        st128(rs, __builtin_bit_cast(u32x4, cat8(a0, b0)), to, 0);
        st128(rs, __builtin_bit_cast(u32x4, cat8(a1, b1)), to, 1024);
    }
    SCHED();
    const u64 tag = ((u64)(unsigned)__builtin_amdgcn_readfirstlane((int)(unsigned)ep) |
                     ((u64)(unsigned)__builtin_amdgcn_readfirstlane((int)(unsigned)(ep >> 32)) << 32)) + 1;
    TS(3);

    {
        const float dt = rbf(__builtin_amdgcn_exp2f(bf2f(p_ldt) * 1.44269504088896341f));
        const float lr = dt * bf2f(p_are), li = dt * bf2f(p_aim);
        const cf dB{dt * bf2f(p_br), dt * bf2f(p_bi)};
        const cf CBt = cmul(cf{bf2f(p_cr), bf2f(p_ci)}, dB);
        const cf CB{CBt[0], -CBt[1]};
        const float a2 = lr * 1.44269504088896341f, th = li * 0.15915494309189535f;
        asm volatile("s_mov_b32 m0, %0" ::"s"(lbase + KG_BASE + (w >> 1) * KG_CH + (w & 1) * 256) : "memory");
#define PUT2(R0, Z0, R1, Z1) asm volatile("ds_write_addtid_b32 %0 offset:%2\n\tds_write_addtid_b32 %1 offset:%3" \
            ::"v"(pk2v(Z0)), "v"(pk2v(Z1)), "i"((R0) * KG_RS), "i"((R1) * KG_RS) : "memory")
#define PUT4(R0, Z0, R1, Z1, R2, Z2, R3, Z3) asm volatile("ds_write_addtid_b32 %0 offset:%4\n\tds_write_addtid_b32 %1 offset:%5\n\tds_write_addtid_b32 %2 offset:%6\n\tds_write_addtid_b32 %3 offset:%7" \
            ::"v"(pk2v(Z0)), "v"(pk2v(Z1)), "v"(pk2v(Z2)), "v"(pk2v(Z3)), "i"((R0) * KG_RS), "i"((R1) * KG_RS), "i"((R2) * KG_RS), "i"((R3) * KG_RS) : "memory")
        const cf z1 = zdir(a2, -th, 1.f), Z256 = zdir(a2, -th, 256.f), w8 = zdir(a2, th, 8.f);
        cf z2, z3, z4, z5, z6, z7, w16, q1, q2, q3, q4_, q5, q6, q7, q256, q512, q768, q1024, q1280, q1536, q1792, q2048, r0, r1, r2, r3, r4;
        cmul2(z2, z1, z1, w16, w8, w8);
        cmul2(q256, CB, Z256, q1, CB, z1);
        cmul2(z3, z2, z1, z4, z2, z2);
        cmul2(q512, q256, Z256, q2, CB, z2);
        cmul2(z5, z4, z1, z6, z4, z2);
        PUT4(0, CB, 1, q1, 8, q256, 2, q2);
        cmul2(z7, z4, z3, q3, CB, z3);
        cmul2(q768, q512, Z256, q4_, CB, z4);
        cmul2(q5, CB, z5, q1024, q768, Z256);
        cmul2(q6, CB, z6, q1280, q1024, Z256);
        PUT4(12, q512, 3, q3, 14, q768, 4, q4_);
        cmul2(q7, CB, z7, q1536, q1280, Z256);
        cmul2(r0, q256, z2, q1792, q1536, Z256);
        PUT4(5, q5, 16, q1024, 6, q6, 17, q1280);
        cmul2(r1, q256, z4, q2048, q1792, Z256);
        cmul2(r2, q256, z6, r3, q512, z4);
        PUT4(7, q7, 18, q1536, 9, r0, 19, q1792);
        cf pa{1.f, 0.f}, pb = w8, pc_, pd, w32;
        cmul2(r4, q768, z4, w32, w16, w16);
        cmul2(pc_, pa, w16, pd, pb, w16);
        PUT4(10, r1, 20, q2048, 11, r2, 13, r3);
        TS(4);
        sfor<0, 8>([&](auto J) {
            constexpr int jj = J.v;
            cf na, nb, nc, nd;
            if constexpr (jj < 7) { cmul2(na, pa, w32, nb, pb, w32); cmul2(nc, pc_, w32, nd, pd, w32); }
            PUT4(21 + 4 * jj, pa, 22 + 4 * jj, pb, 23 + 4 * jj, pc_, 24 + 4 * jj, pd);
            if constexpr (jj == 0) asm volatile("ds_write_addtid_b32 %0 offset:%1" ::"v"(pk2v(r4)), "i"(15 * KG_RS) : "memory");
            if constexpr (jj < 7) { pa = na; pb = nb; pc_ = nc; pd = nd; }
            if constexpr (jj == PUBLISH_AT) {
                SCHED();
                WAITV(0);
                BAR();
                if (tid == 0) { st_flag(ws, WS_FX + tb * 128, tag); st_flag(ws, WS_FXP + tb * 8, tag); }
                SCHED();
            }
        });
#undef PUT2
#undef PUT4
        if (tid < 2) *(LAS unsigned short*)(lds + L_KC + tid * 1312 + 649 * 2) = 0;
        LBAR();
    }
    TS(5);
    bf16x8 xf[32];
    const unsigned xvo = (lane & 31) * 32 + (lane >> 5) * 16, xsb = WS_XN + (mt * 4 + w) * 32 * 1024;
    unsigned xso;
#define LOAD_X(U) do { if constexpr (((U) & 3) == 0) aload_a_first<((U) >> 2) * 4096, 0>(xf[U], xso, rs, xvo, xsb); \
                       else aload_a_next<((U) & 3) * 1024>(xf[U], xso, rs, xvo); } while (0)
    {
        LAS char* qa = lds + KG_BASE + (w >> 1) * KG_CH + l15 * KG_RS + g16 * 16;
        LAS char* pa_ = qa + (21 + 16 * (w & 1)) * KG_RS;
        bf16x8 fa[8], fb[8], fc[8];
#pragma unroll
        for (int s = 0; s < 8; ++s) { fb[s] = *(LAS bf16x8*)(pa_ + s * 64); fa[s] = *(LAS bf16x8*)(qa + s * 64); fc[s] = *(LAS bf16x8*)(qa + 16 * KG_RS + s * 64); }
        SCHED();
        TS(6);
        u64 fxw;
        aload8sc1(fxw, ws + WS_FX + (16 * mt + 4 * w) * 128, (lane & 3) * 128);
        f32x4 k0, k1;
        sfor<0, 8>([&](auto S) {
            mfma16_vv<S.v == 0>(k0, fa[S.v], fb[S.v]);
            if constexpr (S.v < 7) STAGE_W1(1, S.v);
            SCHED();
            mfma16_vv<S.v == 0>(k1, fc[S.v], fb[S.v]);
            SCHED();
        });
        asm volatile("s_waitcnt vmcnt(7)" : "+v"(fxw)::"memory");
        if (!__all(fxw == tag)) wait_flags<1>(ws, WS_FX + (16 * mt + 4 * w) * 128, 4, tag, lane);
        TS(7);
        sfor<0, 8>([&](auto U) { LOAD_X(U.v); });
        SCHED();
        asm volatile("s_nop 7" : "+v"(k0), "+v"(k1));
#pragma unroll
        for (int r = 0; r < 4; r += 2) {
            const unsigned a0 = pk2(k0[r], k0[r + 1]), a1 = pk2(k1[r], k1[r + 1]);
            *(LAS unsigned short*)(size_t)ksc[r] = (unsigned short)a0;
            *(LAS unsigned short*)(size_t)ksc[r + 1] = (unsigned short)(a0 >> 16);
            *(LAS unsigned short*)(size_t)ksc[4 + r] = (unsigned short)a1;
            *(LAS unsigned short*)(size_t)ksc[5 + r] = (unsigned short)(a1 >> 16);
        }
    }
    SCHED();
    LBAR();
    TS(8);
    {
        unsigned val[2][9];
#pragma unroll
        for (int ch = 0; ch < 2; ++ch)
#pragma unroll
            for (int e = 0; e < 9; ++e) val[ch][e] = *(LAS unsigned short*)(size_t)(kga[e] + ch * 1312);
        SCHED();
        sfor<0, 7>([&](auto I) { STAGE_W1(2, I.v); });
        {
            const int r = 35 * (w >> 1) + (lane < 35 ? lane : 34), row = (r * 147) >> 10;
            warm4(pf, (w & 1) ? wu : wg, (192 + 10 * j + row) * (F_ * 2) + (xcd & 3) * 896 + (r - 7 * row) * 128);
        }
#pragma unroll
        for (int ch = 0; ch < 2; ++ch) {
            u32x4 E, O;
#pragma unroll
            for (int m = 0; m < 4; ++m) { E[m] = val[ch][2 * m] | (val[ch][2 * m + 1] << 16); O[m] = val[ch][2 * m + 1] | (val[ch][2 * m + 2] << 16); }
            *(LAS u32x4*)(lds + L_KIMG + ch * CV_KCH + tid * 16) = E;
            *(LAS u32x4*)(lds + L_KIMG + ch * CV_KCH + 4160 + tid * 16) = O;
        }
        if (tid < 16) *(LAS u32x4*)(lds + L_KIMG + (tid >> 3) * CV_KCH + ((tid >> 2) & 1) * 4160 + 4096 + (tid & 3) * 16) = u32x4{0, 0, 0, 0};
    }
    SCHED();
    TS(9);

    f32x16 acc[7];
    u32x4 uu0, uu1;
    bf16x8 wf[28][2];
    const unsigned wtvo = lane * 16, wtsb = WS_WT + (28 * w * 16 + 2 * dt2) * 1024;
    unsigned wtso;
#define LOAD_WT(I) do { if constexpr (((I) & 1) == 0) aload_a_first<((I) >> 1) * 16384, 0>(wf[(I) >> 1][0], wtso, rs, wtvo, wtsb); \
                        else aload_a_next<1024>(wf[(I) >> 1][1], wtso, rs, wtvo); } while (0)
    {
        const unsigned ab0 = lbase + (g16 & 1) * G1_UP + (8 * (g16 >> 1) + q4) * 224 + phi * 8, ab1 = ab0 + 2 * G1_SLOT, ab2 = ab0 + 4 * G1_SLOT;
        auto rdw = [&](auto STP, auto P) {
            constexpr int st = STP.v, p = P.v, sl = g1slot(st >> 2), off = (sl & 1) * G1_SLOT + (st & 3) * 16 * 224 + p * 32;
            const unsigned base = sl < 2 ? ab0 : sl < 4 ? ab1 : ab2;
            return cat8(atr<off>(base), atr<off + 4 * 224>(base));
        };
        bf16x8 cur[7], nxt[7];
        u32x4 fx[2];
        waitv<G1S.younger(1, 1, 0)>();
        LBAR();
        sfor<0, 7>([&](auto P) { cur[P.v] = rdw(IC<0>{}, P); });
        SCHED();
        TS(10);
        sfor<0, 32>([&](auto ST) {
            constexpr int st = ST.v, s = st >> 2, kk = st & 3;
            if constexpr (kk == 0 && s > 0) {
                G1_STAMP(2 * s - 2);
                if constexpr (s <= 6) waitv<G1S.younger(1, s + 1, st)>();
                BAR();
                G1_STAMP(2 * s - 1);
                SCHED();
            }
            if constexpr (st == 6) {
                asm volatile("s_waitcnt vmcnt(%2)" : "+v"(fx[0]), "+v"(fx[1]) : "i"(G1S.younger(3, 1, st)) : "memory");
                const u64 f0 = ((u64)fx[0][1] << 32) | fx[0][0], f1 = ((u64)fx[0][3] << 32) | fx[0][2], f2 = ((u64)fx[1][1] << 32) | fx[1][0], f3 = ((u64)fx[1][3] << 32) | fx[1][2];
                if (!__all(f0 == tag && f1 == tag && f2 == tag && f3 == tag)) wait_flags<4>(ws, WS_FX, 64, tag, lane);
                SCHED();
            }
            if constexpr (st < 28) waitv<G1S.younger(0, st, st)>();
            sfor<0, 7>([&](auto P) {
                constexpr int p = P.v;
                if constexpr (p == 0) waitl<8>(); else if constexpr (p == 3) waitl<(st < 31 ? 10 : 4)>(); else if constexpr (p == 5) waitl<(st < 31 ? 10 : 0)>();
                mfma32_va<st == 0>(acc[p], cur[p], xf[st]);
                if constexpr (st < 31) nxt[p] = rdw(IC<st + 1>{}, P);
                sfor<0, G1S.n>([&](auto K) {
                    constexpr G1Op o = G1S.op[K.v];
                    if constexpr (o.step == st && o.gap == p) {
                        if constexpr (o.kind == 0) LOAD_X(o.a);
                        else if constexpr (o.kind == 1) STAGE_W1(o.a, o.b);
                        else if constexpr (o.kind == 2) LOAD_WT(o.a);
                        else if constexpr (o.kind == 3) aload_v<o.a * 16>(fx[o.a], rs, WS_FXP + lane * 32, 0);
                        else aload_v<0>(o.a == 0 ? uu0 : uu1, rs, WS_XT + ((tid >> 1) * 512 + c0 + (tid & 1)) * 16, o.a * 128 * 512 * 16);
                    }
                });
                SCHED();
            });
#pragma unroll
            for (int p = 0; p < 7; ++p) cur[p] = nxt[p];
        });
    }
    MFMA_DRAIN();
    TS(11);
    asm volatile("s_waitcnt vmcnt(%3)" : "+v"(uu0), "+v"(uu1), "+v"(pf) : "i"(G1S.younger(4, 1, 32)) : "memory");
    if (tid < 64) *(LAS u32x4*)(lds + L_U + (tid >> 5) * CV_UCH + (tid & 31) * 16) = u32x4{0, 0, 0, 0};
    *(LAS u32x4*)(lds + L_U + (tid & 1) * CV_UCH + 512 + (tid >> 1) * 16) = uu0;
    *(LAS u32x4*)(lds + L_U + (tid & 1) * CV_UCH + 512 + 2048 + (tid >> 1) * 16) = uu1;
    SCHED();
    TS(12);
    {
        const unsigned hvo = WS_H + (32 * (w & 1) + (lane & 31)) * 32 + (lane >> 5) * 16, hso = ((mt * 2 + (w >> 1)) * 112 + chunk * 7) * 2048;
        sfor<0, 7>([&](auto P) {
            constexpr int p = P.v;
            u32x4 o;
            sfor<0, 2>([&](auto R) {
                constexpr int r = 2 * R.v, i = G1_WT + 3 * p + R.v;
                const f32x2 g0 = {acc[p][2 * r], acc[p][2 * r + 1]}, u0 = {acc[p][8 + 2 * r], acc[p][9 + 2 * r]};
                const f32x2 g1 = {acc[p][2 * r + 2], acc[p][2 * r + 3]}, u1 = {acc[p][10 + 2 * r], acc[p][11 + 2 * r]};
                o[r] = pk2v(silu2(g0) * u0);
                o[r + 1] = pk2v(silu2(g1) * u1);
                SCHED();
                if constexpr (i < 56) LOAD_WT(i);
                if constexpr (R.v == 1 && i + 1 < 56) LOAD_WT(i + 1);
                SCHED();
            });
            st128(rs, o, hvo, hso + p * 2048);
            SCHED();
        });
    }
    LBAR();
    TS(13);

    const int chl = w >> 1, rh = w & 1;
    constexpr int PD = 12;
    bf16x8 hf[PD][2];
    const unsigned hvoff = WS_H + (lane & 31) * 32 + (lane >> 5) * 16;
    const unsigned hsoff = ((mt2 * 2 + th2) * 112 + 28 * w) * 2048;
    auto ldh = [&](int s, int t) { return asbf(ld128(rs, hvoff + (s & 1) * 2048 + t * 1024, hsoff + (s >> 1) * 4096)); };
    f32x4 cacc[8];
    {
        LAS char* ub = lds + L_U + chl * CV_UCH + 2 * (256 + 16 * (l15 - (g16 >> 1)) + 8 * (g16 & 1)) - 32 * (8 * rh + 6);
        const int i0 = 2047 - 16 * (g16 >> 1) - l15 + 8 * (g16 & 1);
        LAS char* kb = lds + L_KIMG + chl * CV_KCH + ((l15 & 1) ? 0 : 4160) + 4 * (i0 >> 1) - 32 * (8 * rh + 6);
        LAS char* kbm[8];
#pragma unroll
        for (int m = 0; m < 8; ++m) { kbm[m] = kb - 512 * m; asm volatile("" : "+v"(kbm[m])); }
        auto ldG = [&](int m, int rr) {
            const LAS unsigned* p = (const LAS unsigned*)(kbm[m] + 64 * (3 - rr));
            u32x4 t; t[0] = p[0]; t[1] = p[1]; t[2] = p[2]; t[3] = p[3];
            return __builtin_bit_cast(bf16x8, t);
        };
        auto ldF = [&](int m, int rr) { return *(const LAS bf16x8*)(ub + 64 * (3 - rr) + 512 * m); };
        bf16x8 G[2][8], Fr[2][8];
#pragma unroll
        for (int m = 0; m < 8; ++m) { G[0][m] = ldG(m, 0); Fr[0][m] = ldF(m, 0); }
        SCHED();
        u64 fh;
        sfor<0, 4>([&](auto RR) {
            constexpr int rr = RR.v, c = rr & 1, n = c ^ 1;
            CV_STAMP(rr);
            if constexpr (rr == 1) {
                WAITV(0);
                BAR();
                if (tid == 0) st_flag(ws, WS_FH + (mt * 16 + chunk) * 128, tag);
                SCHED();
            }
            if constexpr (rr == 3) {
                asm volatile("s_waitcnt vmcnt(0)" : "+v"(fh)::"memory");
                if (!__all(fh == tag)) wait_flags<1>(ws, WS_FH + (mt2 * 16 + 4 * w) * 128, 4, tag, lane);
                SCHED();
            }
            sfor<0, 36>([&](auto C) {
                constexpr int cnt = C.v;
                constexpr int m = cnt < 8 ? 0 : cnt < 15 ? 1 : cnt < 21 ? 2 : cnt < 26 ? 3 : cnt < 30 ? 4 : cnt < 33 ? 5 : cnt < 35 ? 6 : 7;
                constexpr int first = m * 8 - (m * (m - 1)) / 2, i = m + (cnt - first);
                mfma16_vv<rr == 0 && m == 0>(cacc[i], G[c][m], Fr[c][i - m]);
                if constexpr (rr < 3) {
                    if constexpr (cnt < 8) Fr[n][cnt] = ldF(cnt, rr + 1);
                    else if constexpr (cnt < 24 && !(cnt & 1)) G[n][(cnt - 8) >> 1] = ldG((cnt - 8) >> 1, rr + 1);
                }
                if constexpr (rr == 2 && cnt == 8) aload8sc1(fh, ws + WS_FH + (mt2 * 16 + 4 * w) * 128, (lane & 3) * 128);
                if constexpr (rr == 3 && cnt % 3 == 1 && cnt / 3 < 10) hf[cnt / 6][(cnt / 3) & 1] = ldh(cnt / 6, (cnt / 3) & 1);
                SCHED();
            });
        });
    }
    MFMA_DRAIN();
    TS(14);
    {
        LAS char* red = lds + L_RED;
#pragma unroll
        for (int i = 0; i < 4; ++i) {
            f32x4 lo = cacc[i], hi = cacc[4 + i];
            asm volatile("" : "+v"(lo), "+v"(hi));
            *(LAS f32x4*)(red + (w * 4 + i) * 1024 + lane * 16) = rh ? lo : hi;
            cacc[i] = rh ? hi : lo;
            hf[5 + i][0] = ldh(5 + i, 0); hf[5 + i][1] = ldh(5 + i, 1);
            SCHED();
        }
        LBAR();
        const float dsk = bf2f(__builtin_amdgcn_readfirstlane((int)p_dsk));
#pragma unroll
        for (int i = 0; i < 4; ++i) {
            const f32x4 y = cacc[i] + *(LAS f32x4*)(red + ((w ^ 1) * 4 + i) * 1024 + lane * 16);
            const int t = 256 * (4 * rh + i) + 16 * l15 + 4 * g16;
            const u32x2 uu = *(LAS u32x2*)(lds + L_U + chl * CV_UCH + 2 * (256 + t));
            cacc[i] = f32x4{y[0] + bflo(uu[0]) * dsk, y[1] + bfhi(uu[0]) * dsk, y[2] + bflo(uu[1]) * dsk, y[3] + bfhi(uu[1]) * dsk};
            if (i < 3) { hf[9 + i][0] = ldh(9 + i, 0); hf[9 + i][1] = ldh(9 + i, 1); }
            SCHED();
        }
#pragma unroll
        for (int i = 0; i < 4; ++i) {
            const int t = 256 * (4 * rh + i) + 16 * l15 + 4 * g16;
            st64(rs, u32x2{pk2(cacc[i][0], cacc[i][1]), pk2(cacc[i][2], cacc[i][3])}, WS_ST + ((c0 + chl) * L_ + t) * 2, 0);
        }
    }
    SCHED();
    TS(15);

    f32x16 a2[2][2];
    u32x4 sv[2], xv[2];
    u64 fs;
    const int d0 = 64 * dt2, t0 = mt2 * 128 + th2 * 64;
    const int tt2 = w >> 1, dh2 = w & 1;
    sfor<0, 28>([&](auto S) {
        constexpr int s = S.v;
        const bf16x8 h0 = hf[s % PD][0], h1 = hf[s % PD][1];
        mfma32_av<s == 0>(a2[0][0], wf[s][0], h0);
        mfma32_av<s == 0>(a2[1][0], wf[s][1], h0);
        SCHED();
        mfma32_av<s == 0>(a2[0][1], wf[s][0], h1);
        mfma32_av<s == 0>(a2[1][1], wf[s][1], h1);
        sfor<PD, 28>([&](auto T) {
            constexpr int t = T.v;
            if constexpr (((t - PD) * 21) / 16 == s) { hf[t % PD][0] = ldh(t, 0); hf[t % PD][1] = ldh(t, 1); }
        });
        if constexpr (s == 10) {
            G2_STAMP(0);
            waitv<G2_YOUNGER>();
            BAR();
            G2_STAMP(1);
            if (tid == 0) st_flag(ws, WS_FS + tb * 8, tag);
        }
        if constexpr (s == 19) aload8sc1(fs, ws + WS_FS + (d0 >> 1) * 8, (lane & 31) * 8);
        if constexpr (s == 23) {
            G2_STAMP(2);
            asm volatile("s_waitcnt vmcnt(0)" : "+v"(fs)::"memory");
            G2_STAMP(3);
            if (!__all(fs == tag)) wait_flags<1, 8>(ws, WS_FS + (d0 >> 1) * 8, 32, tag, lane);
#pragma unroll
            for (int i = 0; i < 2; ++i) {
                const int e = tid + 256 * i;
                sv[i] = ld128(rs, WS_ST + ((d0 + (e >> 3)) * L_ + t0) * 2 + (e & 7) * 16, 0);
            }
#pragma unroll
            for (int g = 0; g < 2; ++g) xv[g] = *(const u32x4*)(x + (size_t)(t0 + 32 * tt2 + (lane & 31)) * D_ + d0 + 32 * dh2 + 16 * g + 8 * (lane >> 5));
        }
        SCHED();
    });
    MFMA_DRAIN();
    TS(16);
    {
        sfor<0, 4>([&](auto Q) {
            constexpr int q = Q.v, tt = q >> 1, dh = q & 1;
            if (q != w) {
                const f32x16 a = a2[dh][tt];
#pragma unroll
                for (int g = 0; g < 4; ++g)
                    *(LAS f32x4*)(lds + ((q * 4 + w) * 4 + g) * 1024 + lane * 16) = f32x4{a[4 * g], a[4 * g + 1], a[4 * g + 2], a[4 * g + 3]};
            }
        });
#pragma unroll
        for (int i = 0; i < 2; ++i) *(LAS u32x4*)(lds + L_ST + (tid + 256 * i) * 16) = sv[i];
        LBAR();
    }
    TS(17);
    {
        f32x16 own;
        { f32x16 q0 = a2[0][0], q1 = a2[1][0], q2 = a2[0][1], q3 = a2[1][1];
          asm volatile("" : "+v"(q0), "+v"(q1), "+v"(q2), "+v"(q3));
          own = w == 0 ? q0 : w == 1 ? q1 : w == 2 ? q2 : q3; }
        f32x4 f[4];
#pragma unroll
        for (int g = 0; g < 4; ++g) {
            f[g] = f32x4{own[4 * g], own[4 * g + 1], own[4 * g + 2], own[4 * g + 3]};
#pragma unroll
            for (int o = 1; o < 4; ++o) f[g] += *(LAS f32x4*)(lds + ((w * 4 + ((w + o) & 3)) * 4 + g) * 1024 + lane * 16);
        }
        LAS char* sp = lds + L_ST + (32 * dh2 + 8 * (lane >> 5) + q4) * 128 + (32 * tt2 + 16 * (g16 & 1) + 4 * pp) * 2;
#pragma unroll
        for (int g2 = 0; g2 < 2; ++g2) {
            const s16x4 sa = tr_read(sp + g2 * 16 * 128), sb = tr_read(sp + (g2 * 16 + 4) * 128);
            const u32x2 s01 = __builtin_bit_cast(u32x2, sa), s23 = __builtin_bit_cast(u32x2, sb);
            u32x4 o;
            o[0] = pk2v(unpk(xv[g2][0]) + unpk(s01[0]) * silu2(f32x2{f[2 * g2][0], f[2 * g2][1]}));
            o[1] = pk2v(unpk(xv[g2][1]) + unpk(s01[1]) * silu2(f32x2{f[2 * g2][2], f[2 * g2][3]}));
            o[2] = pk2v(unpk(xv[g2][2]) + unpk(s23[0]) * silu2(f32x2{f[2 * g2 + 1][0], f[2 * g2 + 1][1]}));
            o[3] = pk2v(unpk(xv[g2][3]) + unpk(s23[1]) * silu2(f32x2{f[2 * g2 + 1][2], f[2 * g2 + 1][3]}));
            *(u32x4*)(out + (size_t)(t0 + 32 * tt2 + (lane & 31)) * D_ + d0 + 32 * dh2 + 16 * g2 + 8 * (lane >> 5)) = o;
        }
    }
    if (b == 0 && tid == 0) st_flag(ws, WS_EP, tag);
    TS(18);
    TSR(47);
    TS_DUMP();
    asm volatile(".Lfused_pf_end:" ::: "memory");
}

extern "C" void kernel_launch(void* const* d_in, const int* in_sizes, int n_in, void* d_out, int out_size, void* d_ws, size_t ws_size,
                              hipStream_t stream) {
    (void)hipFuncSetAttribute((const void*)fused_s4_ffn, hipFuncAttributeMaxDynamicSharedMemorySize, LDS_TOTAL);
    fused_s4_ffn<<<256, 256, LDS_TOTAL, stream>>>((const bf16_t*)d_in[0], (const bf16_t*)d_in[1], (const bf16_t*)d_in[2], (const bf16_t*)d_in[3],
                                                  (const bf16_t*)d_in[4], (const bf16_t*)d_in[5], (const bf16_t*)d_in[6], (const bf16_t*)d_in[7],
                                                  (const bf16_t*)d_in[8], (const bf16_t*)d_in[10], (const bf16_t*)d_in[11], (const bf16_t*)d_in[12],
                                                  (bf16_t*)d_out, (char*)d_ws);
}
```
